```python
import torch

def _softmax(x, axis=-1):
    return torch.softmax(x, dim=axis)
def _gelu(x):
    return torch.nn.functional.gelu(x, approximate="tanh")
def _silu(x):
    return torch.nn.functional.silu(x)
def _sigmoid(x):
    return torch.sigmoid(x)
def _layernorm(x, axis=-1, eps=1e-5):
    m = x.mean(dim=axis, keepdim=True)
    v = x.var(dim=axis, keepdim=True, unbiased=False)
    return (x - m) / torch.sqrt(v + eps)
def _scatter_add(tgt, idx, src, axis=0):
    return tgt.clone().index_add_(axis, idx, src)
def _segment_sum(src, seg, n):
    out = torch.zeros((n,) + src.shape[1:], dtype=src.dtype)
    return out.index_add_(0, seg, src)
def _pad(x, pw):
    flat = tuple(v for pair in reversed(pw) for v in pair)
    return torch.nn.functional.pad(x, flat)

kernel_name = 'concat_heads__ssm_conv_s4__grouped_kv_mqa__prompt'

def setup_inputs():

    _w = (lambda a: (lambda t: t.to(torch.bfloat16) if t.is_floating_point() else t)(torch.as_tensor(a)))
    def _A_setup():
        B, L, D, N = 1, 2048, 512, 128
        A_re = -(lambda _x: torch.exp(torch.as_tensor(_x)))((lambda *s: torch.randn(*s, dtype=torch.bfloat16) * 3.2)(D, N))
        A_im = torch.pi * (lambda *s: torch.randn(*s, dtype=torch.bfloat16) * 0.05)(D, N)
        B_re = (lambda *s: torch.randn(*s, dtype=torch.bfloat16) * 0.1)(D, N)
        B_im = (lambda *s: torch.randn(*s, dtype=torch.bfloat16) * 0.1)(D, N)
        C_re = (lambda *s: torch.randn(*s, dtype=torch.bfloat16) * 0.1)(D, N)
        C_im = (lambda *s: torch.randn(*s, dtype=torch.bfloat16) * 0.1)(D, N)
        log_dt = (lambda *s: torch.randn(*s, dtype=torch.bfloat16) * 0.05)(D) * 0.5 - 4.0
        Dskip = (lambda *s: torch.randn(*s, dtype=torch.bfloat16) * 0.05)(D)
        return {
            "u": _w((lambda *s: torch.randn(*s, dtype=torch.bfloat16) * 0.05)(B, L, D)),
            "A_re": _w(A_re),
            "A_im": _w(A_im),
            "B_re": _w(B_re),
            "B_im": _w(B_im),
            "C_re": _w(C_re),
            "C_im": _w(C_im),
            "log_dt": _w(log_dt),
            "Dskip": _w(Dskip),
        }
    _A_in = _A_setup()

    def _B_setup():
        B, S, D, H = 1, 2048, 512, 32
        Dh = D // H
        return {
            "x": _w((lambda *s: torch.randn(*s, dtype=torch.bfloat16) * 0.05)(B, S, D)),
            "wq": _w((lambda *s: torch.randn(*s, dtype=torch.bfloat16) * 0.8)(D, H * Dh)),
            "wk": _w((lambda *s: torch.randn(*s, dtype=torch.bfloat16) * 0.8)(D, Dh)),
            "wv": _w((lambda *s: torch.randn(*s, dtype=torch.bfloat16) * 0.05)(D, Dh)),
            "wo": _w((lambda *s: torch.randn(*s, dtype=torch.bfloat16) * 0.05)(H * Dh, D)),
        }
    _B_in = _B_setup()

    return {
        "A_u": _A_in["u"],
        "A_A_re": _A_in["A_re"],
        "A_A_im": _A_in["A_im"],
        "A_B_re": _A_in["B_re"],
        "A_B_im": _A_in["B_im"],
        "A_C_re": _A_in["C_re"],
        "A_C_im": _A_in["C_im"],
        "A_log_dt": _A_in["log_dt"],
        "A_Dskip": _A_in["Dskip"],
        "B_x": _B_in["x"],
        "B_wq": _B_in["wq"],
        "B_wk": _B_in["wk"],
        "B_wv": _B_in["wv"],
        "B_wo": _B_in["wo"],
    }

def reference(A_u, A_A_re, A_A_im, A_B_re, A_B_im, A_C_re, A_C_im, A_log_dt, A_Dskip, B_x, B_wq, B_wk, B_wv, B_wo):
    def _A_ref(u, A_re, A_im, B_re, B_im, C_re, C_im, log_dt, Dskip):
        Bsz, L, D = u.shape
        dt = torch.exp(log_dt)[:, None]
        A = A_re + 1j * A_im
        Bc = B_re + 1j * B_im
        Cc = C_re + 1j * C_im
        logdA = dt * A
        dB = dt * Bc
        l = (lambda x, dt: x.to(dt))(torch.arange(L), A_re.dtype)
        P = torch.exp(logdA[:, :, None] * l[None, None, :])
        K = torch.einsum("dn,dnl,dn->dl", Cc, P, dB).real
        L2 = 2 * L
        Kp = _pad(K, ((0, 0), (0, L)))
        up = (lambda x, *d: x.permute(*d))(u, 0, 2, 1)
        up = _pad(up, ((0, 0), (0, 0), (0, L)))
        Kf = (lambda x, axes=(-2,-1): torch.fft.rfft2(x.float(), dim=axes))(Kp[:, None, :], axes=(-2, -1))
        Uf = (lambda x, axes=(-2,-1): torch.fft.rfft2(x.float(), dim=axes))(up[:, :, None, :], axes=(-2, -1))
        Yf = Uf * Kf[None, :, :, :]
        y = (lambda x, s, axes=(-2,-1): torch.fft.irfft2(x, s=s, dim=axes).to(torch.bfloat16))(Yf, (1, L2), axes=(-2, -1))[:, :, 0, :L]
        y = (lambda x, *d: x.permute(*d))(y, 0, 2, 1) + u * Dskip[None, None, :]
        return y

    def _B_ref(x, wq, wk, wv, wo):
        B, S, D = x.shape
        H = 32
        Dh = D // H
        q = (lambda x, *d: x.permute(*d))((x @ wq).reshape(B, S, H, Dh), 0, 2, 1, 3)
        k = x @ wk
        v = x @ wv
        scores = torch.einsum("bhqd,bkd->bhqk", q, k) * (Dh ** -0.5)
        p = _softmax(scores, axis=-1)
        o = torch.einsum("bhqk,bkd->bhqd", p, v)
        return (lambda x, *d: x.permute(*d))(o, 0, 2, 1, 3).reshape(B, S, H * Dh) @ wo

    a = _A_ref(A_u, A_A_re, A_A_im, A_B_re, A_B_im, A_C_re, A_C_im, A_log_dt, A_Dskip)
    b = _B_ref(B_x, B_wq, B_wk, B_wv, B_wo)
    return torch.concatenate([a, b], axis=-1)
```

```cpp
#include <hip/hip_runtime.h>
#include <hip/hip_bf16.h>
#include <stdint.h>

typedef float f32x16 __attribute__((ext_vector_type(16)));
typedef float f32x8 __attribute__((ext_vector_type(8)));
typedef float f32x4 __attribute__((ext_vector_type(4)));
typedef float f32x2 __attribute__((ext_vector_type(2)));
typedef __bf16 bf16x8 __attribute__((ext_vector_type(8)));
typedef __bf16 bf16x2 __attribute__((ext_vector_type(2)));
typedef _Float16 f16x8 __attribute__((ext_vector_type(8)));
typedef _Float16 f16x2 __attribute__((ext_vector_type(2)));
typedef short s16x4 __attribute__((ext_vector_type(4)));
typedef unsigned u32x4 __attribute__((ext_vector_type(4)));
typedef unsigned u32x2 __attribute__((ext_vector_type(2)));
#define LAS __attribute__((address_space(3)))
#define GAS __attribute__((address_space(1)))
typedef GAS unsigned gu32;
typedef GAS unsigned long long gu64;
#define RLX_AGENT __ATOMIC_RELAXED, __HIP_MEMORY_SCOPE_AGENT

constexpr unsigned L_KC = 0, KC_CH = 33152;
constexpr unsigned L_TLO = 0, TLO_CH = 16896, L_THI = 33792, THI_CH = 11088;
constexpr unsigned L_WO = 0;
constexpr unsigned L_B = 66304;
constexpr unsigned L_WQ = L_B;
constexpr unsigned L_R2 = L_B + 65536;
constexpr unsigned L_OA = L_B, OA_SLOT = 2560;
constexpr unsigned L_P = L_B + 40960;
constexpr unsigned L_U = L_B + 73728, U_CH = 7600;
constexpr unsigned L_Q = L_B + 88928;
constexpr unsigned L_M = L_Q + 8192;
constexpr unsigned LDS_BYTES = L_M + 256;
static_assert(LDS_BYTES <= 163840, "lds");
constexpr unsigned M_KVCNT = 0, M_SWBAR = 4, M_QMAX = 8, M_KMAX = 12, M_SCALE = 16  ;

constexpr unsigned W_E = 0, W_F1 = 256, W_F2 = 2048, W_KV = 8192, W_OX = W_KV + 131072, W_END = W_OX + 2097152;
constexpr unsigned W_DBG = W_END;

constexpr unsigned kSpinCap = 1u << 20;

#define LD32(a) (*(LAS unsigned*)(size_t)(a))
#define LDF(a) (*(LAS float*)(size_t)(a))
#define LD64(a) (*(LAS u32x2*)(size_t)(a))
#define LD128(a) (*(LAS u32x4*)(size_t)(a))
#define LDF4(a) (*(LAS f32x4*)(size_t)(a))
#define BAR() asm volatile("s_waitcnt lgkmcnt(0)\n\ts_barrier" ::: "memory")

#define STAMP(i)

__device__ __forceinline__ float bflo(unsigned v) { return __uint_as_float(v << 16); }
__device__ __forceinline__ float bfhi(unsigned v) { return __uint_as_float(v & 0xffff0000u); }
__device__ __forceinline__ unsigned pkbf(float a, float b) { f32x2 x = {a, b}; return __builtin_bit_cast(unsigned, __builtin_convertvector(x, bf16x2)); }
__device__ __forceinline__ unsigned pkh(float a, float b) { f32x2 x = {a, b}; return __builtin_bit_cast(unsigned, __builtin_convertvector(x, f16x2)); }
__device__ __forceinline__ float wave_max(float v) {
#pragma unroll
  for (int m = 32; m >= 1; m >>= 1) v = fmaxf(v, __shfl_xor(v, m));
  return v;
}
struct cf { float x, y; };
__device__ __forceinline__ cf cmul(cf a, cf b) { return {a.x * b.x - a.y * b.y, a.x * b.y + a.y * b.x}; }
__device__ __forceinline__ cf cexp_(float re, float im) {
  float m = __builtin_amdgcn_exp2f(re * 1.44269504088896f);
  float f = __builtin_amdgcn_fractf(im * 0.159154943091895f);
  return {m * __builtin_amdgcn_cosf(f), m * __builtin_amdgcn_sinf(f)};
}
__device__ __forceinline__ u32x4 trfrag(unsigned a0, unsigned a1) {
  s16x4 lo = __builtin_amdgcn_ds_read_tr16_b64_v4i16((LAS s16x4*)(size_t)a0);
  s16x4 hi = __builtin_amdgcn_ds_read_tr16_b64_v4i16((LAS s16x4*)(size_t)a1);
  u32x2 l = __builtin_bit_cast(u32x2, lo), h = __builtin_bit_cast(u32x2, hi);
  return (u32x4){l[0], l[1], h[0], h[1]};
}
__device__ __forceinline__ f32x16 mfma32(u32x4 a, u32x4 b, f32x16 c) {
  return __builtin_amdgcn_mfma_f32_32x32x16_bf16(__builtin_bit_cast(bf16x8, a), __builtin_bit_cast(bf16x8, b), c, 0, 0, 0);
}
__device__ __forceinline__ f32x4 mfma16(u32x4 a, u32x4 b, f32x4 c) {
  return __builtin_amdgcn_mfma_f32_16x16x32_bf16(__builtin_bit_cast(bf16x8, a), __builtin_bit_cast(bf16x8, b), c, 0, 0, 0);
}
#define GLD1(dst, voff, base) asm volatile("global_load_dword %0, %1, %2" : "=v"(dst) : "v"(voff), "s"(base))
#define GLD4(dst, voff, base, imm) asm volatile("global_load_dwordx4 %0, %1, %2 offset:%3" : "=v"(dst) : "v"(voff), "s"(base), "i"(imm))
__device__ __forceinline__ void gdma(unsigned voff, const void* base, unsigned lds_dst) {
  unsigned keep;
  asm volatile("s_mov_b32 %0, m0\n\ts_mov_b32 m0, %3\n\ts_nop 0\n\tglobal_load_lds_dwordx4 %1, %2\n\ts_mov_b32 m0, %0"
               : "=&s"(keep) : "v"(voff), "s"(base), "s"(lds_dst) : "memory");
}
__device__ __forceinline__ unsigned wswz(unsigned row) { return ((((row >> 1) & 3u) ^ (((row >> 3) & 1u) << 1)) << 1); }

__device__ __forceinline__ void sw_barrier4(unsigned addr, unsigned target, int lane) {
  asm volatile("s_waitcnt lgkmcnt(0)" ::: "memory");
  if (lane == 0) __hip_atomic_fetch_add((LAS unsigned*)(size_t)addr, 1u, __ATOMIC_RELAXED, __HIP_MEMORY_SCOPE_WORKGROUP);
  for (unsigned spins = 0; spins < kSpinCap; ++spins) {
    unsigned v = __hip_atomic_load((LAS unsigned*)(size_t)addr, __ATOMIC_RELAXED, __HIP_MEMORY_SCOPE_WORKGROUP);
    if ((unsigned)__builtin_amdgcn_readfirstlane(v) >= target) break;
    __builtin_amdgcn_s_sleep(2);
  }
  asm volatile("" ::: "memory");
}

#define QK(S, Kf, Qf) asm volatile("v_mfma_f32_32x32x16_bf16 %0, %1, %2, %3" : "=&v"(S) : "v"(Kf), "v"(Qf), "v"(bias))
#define PV0(O, Vf, S) { f32x8 h_ = __builtin_shufflevector(S, S, 0, 1, 2, 3, 4, 5, 6, 7); \
  asm volatile("v_smfmac_f32_32x32x32_bf16 %0, %1, %2, %3" : "+v"(O) : "v"(Vf), "v"(h_), "v"(sidx)); }
#define PV1(O, Vf, S) { f32x8 h_ = __builtin_shufflevector(S, S, 8, 9, 10, 11, 12, 13, 14, 15); \
  asm volatile("v_smfmac_f32_32x32x32_bf16 %0, %1, %2, %3" : "+v"(O) : "v"(Vf), "v"(h_), "v"(sidx)); }
#define EX(S, i) { float t_ = S[i]; asm volatile("v_exp_f32 %0, %0" : "+v"(t_)); S[i] = t_; }
#define ZERO(O) asm volatile("v_mfma_f32_32x32x8_bf16 %0, %1, %1, 0" : "=&v"(O) : "v"(zz))
#define BLOCK(Sc, Sp, PREV, Kn, Qn, Vc0, Oc, EXTRA0, EXTRA1) \
  EX(Sc, 0); EX(Sc, 1); \
  PREV; \
  EX(Sc, 2); EX(Sc, 3); EX(Sc, 4); \
  EXTRA0; \
  QK(Sp, Kn, Qn); \
  EX(Sc, 5); EX(Sc, 6); EX(Sc, 7); EX(Sc, 8); EX(Sc, 9); EX(Sc, 10); \
  PV0(Oc, Vc0, Sc); \
  EX(Sc, 11); EX(Sc, 12); EX(Sc, 13); \
  EXTRA1; \
  EX(Sc, 14); EX(Sc, 15);
#define FLUSH(O, a, cnt) { u32x4 q0_ = __builtin_bit_cast(u32x4, __builtin_shufflevector(O, O, 0, 1, 2, 3)); \
  u32x4 q1_ = __builtin_bit_cast(u32x4, __builtin_shufflevector(O, O, 4, 5, 6, 7)); float l_ = O[8]; \
  asm volatile("ds_write_b128 %0, %1\n\tds_write_b128 %0, %2 offset:1024\n\tds_write2st64_b32 %3, %4, %5 offset0:8 offset1:9" \
               :: "v"((a) + lane * 16), "v"(q0_), "v"(q1_), "v"((a) + lane * 4), "v"(l_), "v"(cnt), "v"(O) : "memory"); }

__global__ __launch_bounds__(512, 2) void fused_kernel(
    const unsigned short* __restrict__ u, const unsigned short* __restrict__ Are, const unsigned short* __restrict__ Aim,
    const unsigned short* __restrict__ Bre, const unsigned short* __restrict__ Bim, const unsigned short* __restrict__ Cre,
    const unsigned short* __restrict__ Cim, const unsigned* __restrict__ logdt2, const unsigned* __restrict__ dskip2,
    const unsigned short* __restrict__ x, const unsigned short* __restrict__ wq, const unsigned short* __restrict__ wk,
    const unsigned short* __restrict__ wv, const unsigned short* __restrict__ wo, unsigned short* __restrict__ out,
    unsigned char* __restrict__ ws) {
  __shared__ __attribute__((aligned(128))) unsigned char smem[LDS_BYTES];
  const unsigned sb = (unsigned)(size_t)(LAS unsigned char*)smem;
  const int tid = threadIdx.x, lane = tid & 63;
  const int w = __builtin_amdgcn_readfirstlane(tid >> 6);
  const int b = blockIdx.x, xcd = b & 7, bi = b >> 3;
  const int grp = xcd * 4 + (bi >> 3), hg = bi & 7, cp = xcd * 32 + bi;
  const bool producer = hg < 4;
  const int chain = hg & 1, ktile = 2 * grp + ((hg >> 1) & 1);
  const int h = lane >> 5, g4 = lane >> 4, l15 = lane & 15, l31 = lane & 31;
  const int qq = w & 3, kh = w >> 2;
  STAMP(0);

  const int tch = (w >> 1) & 1, par = w & 1;
  unsigned pAr, pAi, pBr, pBi, pCr, pCi;
  {
    unsigned vo = (unsigned)((2 * cp + tch) * 256 + lane * 4);
    GLD1(pAr, vo, Are); GLD1(pAi, vo, Aim); GLD1(pBr, vo, Bre); GLD1(pBi, vo, Bim); GLD1(pCr, vo, Cre); GLD1(pCi, vo, Cim);
  }
  u32x4 x32[4];
  if (producer) {
    int r = l31;
    if (chain) r = (r & 19) | ((r & 4) << 1) | ((r & 8) >> 1);
    unsigned vo = (unsigned)((32 * ktile + r) * 1024 + w * 128 + h * 16);
    GLD4(x32[0], vo, x, 0); GLD4(x32[1], vo, x, 32); GLD4(x32[2], vo, x, 64); GLD4(x32[3], vo, x, 96);
    const unsigned short* wkv = chain ? wv : wk;
    unsigned wo_ = (unsigned)(w * 2048 + lane * 16);
    gdma(wo_, wkv, sb + L_R2 + w * 2048);
    gdma(wo_ + 1024, wkv, sb + L_R2 + w * 2048 + 1024);
  }
  u32x4 xq[8];
  {
    unsigned vo = (unsigned)((64 * grp + 16 * qq + l15) * 1024 + kh * 512 + g4 * 16);
    GLD4(xq[0], vo, x, 0); GLD4(xq[1], vo, x, 64); GLD4(xq[2], vo, x, 128); GLD4(xq[3], vo, x, 192);
    GLD4(xq[4], vo, x, 256); GLD4(xq[5], vo, x, 320); GLD4(xq[6], vo, x, 384); GLD4(xq[7], vo, x, 448);
  }
  {
    const unsigned r8 = lane >> 3, c = lane & 7;
#pragma unroll
    for (int i = 0; i < 8; ++i) {
      unsigned row = 64 * w + 8 * i + r8;
      gdma(row * 1024 + hg * 128 + ((c ^ wswz(row)) << 4), wq, sb + L_WQ + (64 * w + 8 * i) * 128);
    }
  }
  u32x4 upf;
  GLD4(upf, (unsigned)((64 * bi + (tid >> 3)) * 1024 + xcd * 128 + (tid & 7) * 16), u, 0);

  unsigned E;
  asm volatile("s_load_dword %0, %1, 0x0\n\ts_waitcnt lgkmcnt(0)" : "=s"(E) : "s"(ws) : "memory");
  const unsigned tag = E + ((E * 0x9E3779B1u ^ (E >> 15) ^ 0x5bd1e995u) | 1u);
  if (tid < 16) LD32(sb + L_M + tid * 4) = 0;
  BAR();
  asm volatile("s_waitcnt vmcnt(17)" : "+v"(pAr), "+v"(pAi), "+v"(pBr), "+v"(pBi), "+v"(pCr), "+v"(pCi) :: "memory");
  STAMP(1);

  if (producer) {
    asm volatile("" : "+v"(x32[0]), "+v"(x32[1]), "+v"(x32[2]), "+v"(x32[3]));
    f32x16 acc = {0};
    const unsigned wa = sb + L_R2 + w * 2048 + (8 * h + (l15 >> 2)) * 32 + (lane & 3) * 8;
    u32x4 wf[4];
#pragma unroll
    for (int s = 0; s < 4; ++s) wf[s] = trfrag(wa + s * 512, wa + s * 512 + 128);
    f32x4 p0, p1;
    if (chain) {
#pragma unroll
      for (int s = 0; s < 4; ++s) acc = mfma32(x32[s], wf[s], acc);
      f32x4 a0 = __builtin_shufflevector(acc, acc, 0, 1, 2, 3), a1 = __builtin_shufflevector(acc, acc, 4, 5, 6, 7);
      f32x4 a2 = __builtin_shufflevector(acc, acc, 8, 9, 10, 11), a3 = __builtin_shufflevector(acc, acc, 12, 13, 14, 15);
      const bool up = lane & 16;
      p0 = up ? a2 : a0; p1 = up ? a3 : a1;
    } else {
#pragma unroll
      for (int s = 0; s < 4; ++s) acc = mfma32(wf[s], x32[s], acc);
      p0 = __builtin_shufflevector(acc, acc, 0, 1, 2, 3); p1 = __builtin_shufflevector(acc, acc, 4, 5, 6, 7);
    }
    LDF4(sb + L_R2 + w * 2048 + lane * 16) = p0;
    LDF4(sb + L_R2 + w * 2048 + 1024 + lane * 16) = p1;
    asm volatile("s_waitcnt lgkmcnt(0)" ::: "memory");
    if (lane == 0) __hip_atomic_fetch_add((LAS unsigned*)(size_t)(sb + L_M + M_KVCNT), 1u, __ATOMIC_RELAXED, __HIP_MEMORY_SCOPE_WORKGROUP);
    if (w == 0) {
      for (unsigned spins = 0; spins < kSpinCap; ++spins) {
        unsigned v = __hip_atomic_load((LAS unsigned*)(size_t)(sb + L_M + M_KVCNT), __ATOMIC_RELAXED, __HIP_MEMORY_SCOPE_WORKGROUP);
        if ((unsigned)__builtin_amdgcn_readfirstlane(v) >= 8u) break;
      }
      asm volatile("" ::: "memory");
#pragma unroll
      for (int k = 1; k < 8; ++k) { p0 += LDF4(sb + L_R2 + k * 2048 + lane * 16); p1 += LDF4(sb + L_R2 + k * 2048 + 1024 + lane * 16); }
      u32x4 pk = {pkbf(p0[0], p0[1]), pkbf(p0[2], p0[3]), pkbf(p1[0], p1[1]), pkbf(p1[2], p1[3])};
      const __amdgpu_buffer_rsrc_t rs = __builtin_amdgcn_make_buffer_rsrc(ws + W_KV, 0, 131072, 0x00020000);
      __builtin_amdgcn_raw_buffer_store_b128(pk, rs, ktile * 2048 + chain * 1024 + lane * 16, 0, 16);
      float n2 = 0.f;
#pragma unroll
      for (int j = 0; j < 4; ++j) { float a = bflo(pk[j]), c = bfhi(pk[j]); n2 += a * a + c * c; }
      n2 += __shfl_xor(n2, 32);
      n2 = wave_max(n2);
      asm volatile("s_waitcnt vmcnt(0)" ::: "memory");
      if (lane == 0)
        __hip_atomic_store((gu64*)(ws + W_F1) + chain * 64 + ktile, ((unsigned long long)tag << 32) | __float_as_uint(n2), RLX_AGENT);
    }
  }
  STAMP(2);

  {
    const unsigned ld2 = logdt2[cp];
    const float ldt = tch ? bfhi(ld2) : bflo(ld2);
    const float dt = bflo(pkbf(expf(ldt), 0.f) & 0xffffu);
    const float ar0 = dt * bflo(pAr), ar1 = dt * bfhi(pAr);
    const float lam = fmaxf(0.f, wave_max(fmaxf(ar0, ar1)));
    const float ar = (par ? ar1 : ar0) - lam, ai = dt * (par ? bfhi(pAi) : bflo(pAi));
    const unsigned col = (unsigned)(par * 64 + lane) * 4;
    if (w < 4) {
      const unsigned t = sb + L_TLO + tch * TLO_CH + col;
      cf p[16];
      cf g = cexp_(8.f * ar, 8.f * ai);
      p[0] = {1.f, 0.f}; p[1] = g;
      g = cmul(g, g); p[2] = g; p[3] = cmul(p[1], g);
      g = cmul(g, g);
      p[4] = g;
#pragma unroll
      for (int i = 1; i < 4; ++i) p[4 + i] = cmul(p[i], g);
      g = cmul(g, g);
      p[8] = g;
#pragma unroll
      for (int i = 1; i < 8; ++i) p[8 + i] = cmul(p[i], g);
      g = cmul(g, g);
#pragma unroll
      for (int i = 0; i < 16; ++i) {
        LD32(t + i * 528) = pkh(p[i].x, -p[i].y);
        cf q = i ? cmul(p[i], g) : g;
        LD32(t + (16 + i) * 528) = pkh(q.x, -q.y);
      }
    } else {
      const unsigned t = sb + L_THI + tch * THI_CH + col;
      cf c0 = cmul({bflo(pCr), bflo(pCi)}, {dt * bflo(pBr), dt * bflo(pBi)});
      cf c1 = cmul({bfhi(pCr), bfhi(pCi)}, {dt * bfhi(pBr), dt * bfhi(pBi)});
      float cm = wave_max(fmaxf(fmaxf(fabsf(c0.x), fabsf(c0.y)), fmaxf(fabsf(c1.x), fabsf(c1.y))));
      int ex = (int)((__float_as_uint(cm) >> 23) & 255u);
      ex = ex < 8 ? 8 : (ex > 245 ? 245 : ex);
      const float sdn = __uint_as_float((unsigned)(252 - ex) << 23), sup = __uint_as_float((unsigned)(ex + 2) << 23);
      if (par == 0 && lane == 0) { LDF(sb + L_M + M_SCALE + tch * 8) = sup; LDF(sb + L_M + M_SCALE + tch * 8 + 4) = lam * 1.44269504088896f; }
      cf c = par ? c1 : c0; c.x *= sdn; c.y *= sdn;
      const cf w1 = cexp_(ar, ai), W = cexp_(256.f * ar, 256.f * ai);
      const cf w2 = cmul(w1, w1), w4 = cmul(w2, w2), w6 = cmul(w4, w2);
      cf v = c;
#pragma unroll
      for (int j = 0; j < 8; ++j) { LD32(t + j * 528) = pkh(v.x, v.y); if (j < 7) v = cmul(v, w1); }
      v = cmul(c, W); LD32(t + 8 * 528) = pkh(v.x, v.y);
      cf e = cmul(v, w2); LD32(t + 9 * 528) = pkh(e.x, e.y);
      e = cmul(v, w4); LD32(t + 10 * 528) = pkh(e.x, e.y);
      e = cmul(v, w6); LD32(t + 11 * 528) = pkh(e.x, e.y);
      v = cmul(v, W); LD32(t + 12 * 528) = pkh(v.x, v.y);
      e = cmul(v, w4); LD32(t + 13 * 528) = pkh(e.x, e.y);
      v = cmul(v, W); LD32(t + 14 * 528) = pkh(v.x, v.y);
      e = cmul(v, w4); LD32(t + 15 * 528) = pkh(e.x, e.y);
#pragma unroll
      for (int j = 16; j < 21; ++j) { v = cmul(v, W); LD32(t + j * 528) = pkh(v.x, v.y); }
    }
  }
  STAMP(3);
  BAR();
  const unsigned KT = sb + L_R2;
  if (w < 2) {
    const unsigned aa = sb + L_TLO + w * TLO_CH + l31 * 528 + h * 16;
    const unsigned ba = sb + L_THI + w * THI_CH + (l31 < 20 ? l31 : 20) * 528 + h * 16;
    f32x16 acc = {0};
#pragma unroll 4
    for (int ks = 0; ks < 16; ++ks)
      acc = __builtin_amdgcn_mfma_f32_32x32x16_f16(__builtin_bit_cast(f16x8, LD128(aa + ks * 32)), __builtin_bit_cast(f16x8, LD128(ba + ks * 32)), acc, 0, 0, 0);
    const float sup = LDF(sb + L_M + M_SCALE + w * 8), lam2 = LDF(sb + L_M + M_SCALE + w * 8 + 4);
    const int j = l31;
    const int e = j < 8 ? j : j < 12 ? 256 + 2 * (j - 8) : j < 16 ? 512 + 4 * (j & 1) + 256 * ((j - 12) >> 1) : 1024 + 256 * (j - 16);
#pragma unroll
    for (int r = 0; r < 16; ++r) {
      const int l = e + 8 * ((r & 3) + 8 * (r >> 2) + 4 * h);
      const float kv = acc[r] * sup * __builtin_amdgcn_exp2f(lam2 * (float)l);
      if (j <= 20 && l <= 2048) *(LAS unsigned short*)(size_t)(KT + (32 + l) * 4 + 2 * w) = (unsigned short)pkbf(kv, 0.f);
    }
  } else if (w == 2) {
    if (lane < 32) LD32(KT + lane * 4) = 0;
  }
  BAR();
#pragma unroll
  for (int k = 0; k < 4; ++k) {
    const int l = 256 + tid + 512 * k;
    const int sh = l >= 1024 ? 3 : l >= 512 ? 2 : 1;
    const int r = ((l + (1 << (sh - 1)) - 1 + ((l >> sh) & 1)) >> sh) << sh;
    if (l < 2048 && r != l) LD32(KT + (32 + l) * 4) = LD32(KT + (32 + r) * 4);
  }
  BAR();
#pragma unroll 1
  for (int id = tid; id < 2072; id += 512) {
    unsigned d[8];
#pragma unroll
    for (int j = 0; j < 8; ++j) d[j] = LD32(KT + (id + 8 - j) * 4);
    u32x4 c0, c1;
#pragma unroll
    for (int m = 0; m < 4; ++m) { c0[m] = __builtin_amdgcn_perm(d[2 * m + 1], d[2 * m], 0x05040100u); c1[m] = __builtin_amdgcn_perm(d[2 * m + 1], d[2 * m], 0x07060302u); }
    LD128(sb + L_KC + id * 16) = c0;
    LD128(sb + L_KC + KC_CH + id * 16) = c1;
  }
  STAMP(4);

  asm volatile("s_waitcnt vmcnt(0)" : "+v"(xq[0]), "+v"(xq[1]), "+v"(xq[2]), "+v"(xq[3]), "+v"(xq[4]), "+v"(xq[5]), "+v"(xq[6]), "+v"(xq[7]), "+v"(upf) :: "memory");
  BAR();
  STAMP(5);
  {
    f32x4 acc[4] = {{0, 0, 0, 0}, {0, 0, 0, 0}, {0, 0, 0, 0}, {0, 0, 0, 0}};
    const unsigned r0 = 256 * kh + 8 * g4 + (l15 >> 2), r1 = r0 + 4;
    const unsigned pc = (lane & 3) >> 1, ph = (lane & 1) * 8;
    const unsigned s0 = wswz(r0), s1 = wswz(r1);
    unsigned a0[4], a1[4];
#pragma unroll
    for (int nt = 0; nt < 4; ++nt) {
      a0[nt] = sb + L_WQ + r0 * 128 + (((2 * nt + pc) ^ s0) << 4) + ph;
      a1[nt] = sb + L_WQ + r1 * 128 + (((2 * nt + pc) ^ s1) << 4) + ph;
    }
#pragma unroll
    for (int s = 0; s < 8; ++s)
#pragma unroll
      for (int nt = 0; nt < 4; ++nt) acc[nt] = mfma16(trfrag(a0[nt] + s * 4096, a1[nt] + s * 4096), xq[s], acc[nt]);
    const unsigned xa = sb + L_R2 + lane * 16;
    LDF4(xa + w * 2048) = kh ? acc[0] : acc[2];
    LDF4(xa + w * 2048 + 1024) = kh ? acc[1] : acc[3];
    BAR();
    f32x4 q0 = (kh ? acc[2] : acc[0]) + LDF4(xa + (w ^ 4) * 2048);
    f32x4 q1 = (kh ? acc[3] : acc[1]) + LDF4(xa + (w ^ 4) * 2048 + 1024);
    const float cs = 0.25f * 1.44269504088896f;
    float nmax = 0.f;
#pragma unroll
    for (int e = 0; e < 2; ++e) {
      f32x4 q = e ? q1 : q0;
      unsigned t0 = pkbf(q[0], q[1]), t1 = pkbf(q[2], q[3]);
      u32x2 o = {pkbf(bflo(t0) * cs, bfhi(t0) * cs), pkbf(bflo(t1) * cs, bfhi(t1) * cs)};
      const int qt = (qq >> 1) * 4 + 2 * kh + e;
      LD64(sb + L_Q + qt * 1024 + (g4 & 1) * 512 + (16 * (qq & 1) + l15) * 16 + (g4 >> 1) * 8) = o;
      float n2 = bflo(o[0]) * bflo(o[0]) + bfhi(o[0]) * bfhi(o[0]) + bflo(o[1]) * bflo(o[1]) + bfhi(o[1]) * bfhi(o[1]);
      n2 += __shfl_xor(n2, 16); n2 += __shfl_xor(n2, 32);
      nmax = fmaxf(nmax, n2);
    }
    nmax = wave_max(nmax);
    if (!(nmax < 3.0e38f)) nmax = 3.0e38f;
    if (lane == 0) __hip_atomic_fetch_max((LAS unsigned*)(size_t)(sb + L_M + M_QMAX), __float_as_uint(nmax), __ATOMIC_RELAXED, __HIP_MEMORY_SCOPE_WORKGROUP);
  }
  BAR();
  STAMP(6);
  LD64(sb + L_OA + (tid >> 5) * OA_SLOT + 2304 + (tid & 31) * 8) = (u32x2){0u, 0u};
  if (tid < 248) { const int c = tid >= 124, i = tid - 124 * c; LD128(sb + L_U + c * U_CH + (i >> 2) * 80 + (i & 3) * 16) = (u32x4){0u, 0u, 0u, 0u}; }
  if (w == 0) {
    float km = 0.f;
    for (unsigned spins = 0; spins < kSpinCap; ++spins) {
      unsigned long long fk = __hip_atomic_load((gu64*)(ws + W_F1) + lane, RLX_AGENT);
      unsigned long long fv = __hip_atomic_load((gu64*)(ws + W_F1) + 64 + lane, RLX_AGENT);
      km = __uint_as_float((unsigned)fk);
      if (__all((unsigned)(fk >> 32) == tag && (unsigned)(fv >> 32) == tag)) break;
      __builtin_amdgcn_s_sleep(1);
    }
    if (!(km < 3.0e38f)) km = 3.0e38f;
    km = wave_max(km);
    if (lane == 0) LDF(sb + L_M + M_KMAX) = km;
  }
  BAR();
  STAMP(7);
  u32x4 Kf[8], Vf[16];
  {
    const __amdgpu_buffer_rsrc_t rs = __builtin_amdgcn_make_buffer_rsrc(ws + W_KV, 0, 131072, 0x00020000);
    const int ko = w * 16384 + lane * 16;
#pragma unroll
    for (int i = 0; i < 16; ++i) Vf[i] = (u32x4){0x3f803f80u, 0x3f803f80u, 0x3f803f80u, 0x3f803f80u};
    Kf[0] = __builtin_amdgcn_raw_buffer_load_b128(rs, ko, 0, 16);
    Kf[1] = __builtin_amdgcn_raw_buffer_load_b128(rs, ko + 2048, 0, 16);
    if ((lane & 16) == 0) {
#pragma unroll
      for (int i = 0; i < 4; ++i) Vf[i] = __builtin_amdgcn_raw_buffer_load_b128(rs, ko + (i >> 1) * 2048 + 1024 + (i & 1) * 256, 0, 16);
    }
#pragma unroll
    for (int i = 2; i < 8; ++i) Kf[i] = __builtin_amdgcn_raw_buffer_load_b128(rs, ko + i * 2048, 0, 16);
    if ((lane & 16) == 0) {
#pragma unroll
      for (int i = 4; i < 16; ++i) Vf[i] = __builtin_amdgcn_raw_buffer_load_b128(rs, ko + (i >> 1) * 2048 + 1024 + (i & 1) * 256, 0, 16);
    }
  }
  unsigned ug[8];
  if (w < 4) {
#pragma unroll
    for (int i = 0; i < 8; ++i) ug[i] = ((const unsigned*)u)[(8 * tid + i) * 256 + cp];
  }
  const float bound = sqrtf(LDF(sb + L_M + M_QMAX)) * sqrtf(LDF(sb + L_M + M_KMAX)) * 1.002f + 1e-3f;
  const bool exact = __builtin_amdgcn_readfirstlane(!(bound <= 32.0f));
  const int j4 = w & 3;
  const unsigned qaddr = sb + L_Q + lane * 16;
  const unsigned maddr = sb + L_U + 64 + (l31 >> 2) * 80 + (l31 & 3) * 4;
  if (exact) {
    if (tid < 256) LDF(sb + L_U + 64 + (tid >> 2) * 80 + (tid & 3) * 4) = -3.0e38f;
    BAR();
#pragma unroll 1
    for (int t = 0; t < 8; ++t) {
      const u32x4 q = LD128(qaddr + t * 1024);
      float m = -3.0e38f;
#pragma unroll
      for (int i = 0; i < 8; ++i) {
        f32x16 z = {0};
        f32x16 s = mfma32(Kf[i], q, z);
#pragma unroll
        for (int r = 0; r < 16; ++r) m = fmaxf(m, s[r]);
      }
      m = fmaxf(m, __shfl_xor(m, 32));
      if (lane < 32) __builtin_amdgcn_ds_fmaxf((LAS float*)(size_t)(maddr + t * 640), m, __ATOMIC_RELAXED, __HIP_MEMORY_SCOPE_WORKGROUP, false);
    }
    BAR();
  }
  STAMP(8);

  f32x16 bias;
  {
    const float b0 = exact ? -LDF(maddr + ((2 * j4) & 7) * 640) : -bound;
#pragma unroll
    for (int i = 0; i < 16; ++i) bias[i] = b0;
  }
  int sidx = 0xDDDD; asm volatile("" : "+v"(sidx));
  s16x4 zz = {0, 0, 0, 0}; asm volatile("" : "+v"(zz));
  const unsigned oabase = sb + L_OA + (w >> 2) * 8 * OA_SLOT;
  f32x16 SA, SB_, Oa, Ob;

#define SETBIAS(tile) if (exact) { const float b_ = -LDF(maddr + (tile) * 640); asm volatile("s_nop 7" ::: "memory"); \
    _Pragma("unroll") for (int i_ = 0; i_ < 16; ++i_) bias[i_] = b_; asm volatile("s_nop 1" : "+v"(bias)); }
#define OLOAD(O, tile, want) { const unsigned a_ = oabase + (tile) * OA_SLOT; u32x4 q0_, q1_; u32x2 lc_; \
    for (unsigned spins = 0; spins < kSpinCap; ++spins) { \
      asm volatile("ds_read2st64_b32 %2, %4 offset0:8 offset1:9\n\tds_read_b128 %0, %3\n\tds_read_b128 %1, %3 offset:1024\n\ts_waitcnt lgkmcnt(0)" \
                   : "=&v"(q0_), "=&v"(q1_), "=&v"(lc_) : "v"(a_ + lane * 16), "v"(a_ + lane * 4) : "memory"); \
      if (__all(lc_[1] == (want))) break; } \
    _Pragma("unroll") for (int i_ = 0; i_ < 4; ++i_) { O[i_] = __uint_as_float(q0_[i_]); O[4 + i_] = __uint_as_float(q1_[i_]); } \
    O[8] = __uint_as_float(lc_[0]); O[9] = __uint_as_float(lc_[1]); asm volatile("s_nop 1" : "+v"(O)); }

#pragma unroll 1
  for (int part = 0; part < 2; ++part) {
    const int p0 = part ? 1 : 0, p1 = part ? 4 : 1;
    int ta = (2 * j4 + 2 * p0) & 7;
    u32x4 Qa = LD128(qaddr + ta * 1024), Qb = LD128(qaddr + (ta + 1) * 1024);
    if (part == 0) { ZERO(Oa); ZERO(Ob); } else { OLOAD(Oa, ta, 1u); SETBIAS(ta); }
    QK(SA, Kf[0], Qa);
    asm volatile("s_nop 15" : "+v"(SA));
#pragma unroll 1
    for (int p = p0; p < p1; ++p) {
      const int tb = ta + 1, tn = (ta + 2) & 7, tp = (ta + 7) & 7;
      const unsigned cnt = p + 1;
      const bool first = (p == p0), last = (p == p1 - 1);
      BLOCK(SA, SB_, if (!first) PV1(Ob, Vf[15], SB_), Kf[1], Qa, Vf[0], Oa, , );
      BLOCK(SB_, SA, PV1(Oa, Vf[1], SA), Kf[2], Qa, Vf[2], Oa, , if (!first) FLUSH(Ob, oabase + tp * OA_SLOT, cnt - 1));
      BLOCK(SA, SB_, PV1(Oa, Vf[3], SB_), Kf[3], Qa, Vf[4], Oa, , );
      BLOCK(SB_, SA, PV1(Oa, Vf[5], SA), Kf[4], Qa, Vf[6], Oa, , );
      BLOCK(SA, SB_, PV1(Oa, Vf[7], SB_), Kf[5], Qa, Vf[8], Oa, , );
      BLOCK(SB_, SA, PV1(Oa, Vf[9], SA), Kf[6], Qa, Vf[10], Oa, , );
      BLOCK(SA, SB_, PV1(Oa, Vf[11], SB_), Kf[7], Qa, Vf[12], Oa, , if (p > 0) OLOAD(Ob, tb, (unsigned)p));
      BLOCK(SB_, SA, PV1(Oa, Vf[13], SA), Kf[0], Qb, Vf[14], Oa, SETBIAS(tb), Qa = LD128(qaddr + tn * 1024));
      BLOCK(SA, SB_, PV1(Oa, Vf[15], SB_), Kf[1], Qb, Vf[0], Ob, , );
      BLOCK(SB_, SA, PV1(Ob, Vf[1], SA), Kf[2], Qb, Vf[2], Ob, , FLUSH(Oa, oabase + ta * OA_SLOT, cnt));
      BLOCK(SA, SB_, PV1(Ob, Vf[3], SB_), Kf[3], Qb, Vf[4], Ob, , );
      BLOCK(SB_, SA, PV1(Ob, Vf[5], SA), Kf[4], Qb, Vf[6], Ob, , );
      BLOCK(SA, SB_, PV1(Ob, Vf[7], SB_), Kf[5], Qb, Vf[8], Ob, , );
      BLOCK(SB_, SA, PV1(Ob, Vf[9], SA), Kf[6], Qb, Vf[10], Ob, , );
      BLOCK(SA, SB_, PV1(Ob, Vf[11], SB_), Kf[7], Qb, Vf[12], Ob, , if (!last) OLOAD(Oa, tn, cnt));
      BLOCK(SB_, SA, PV1(Ob, Vf[13], SA), Kf[0], Qa, Vf[14], Ob, SETBIAS(tn), Qb = LD128(qaddr + (tn + 1) * 1024));
      ta = tn;
    }
    asm volatile("s_nop 1" : "+v"(SB_));
    PV1(Ob, Vf[15], SB_);
    asm volatile("s_nop 15" : "+v"(Ob), "+v"(SA));
    FLUSH(Ob, oabase + ((ta + 7) & 7) * OA_SLOT, (unsigned)p1);
    STAMP(9 + 3 * part);

    if (part == 0 && w < 4) {
      int lane3 = lane; asm volatile("" : "+v"(lane3));
      const int lane = lane3, tid = w * 64 + lane, h = lane >> 5, l31 = lane & 31;
      {
        u32x4 c0, c1;
#pragma unroll
        for (int m = 0; m < 4; ++m) { c0[m] = __builtin_amdgcn_perm(ug[2 * m + 1], ug[2 * m], 0x05040100u); c1[m] = __builtin_amdgcn_perm(ug[2 * m + 1], ug[2 * m], 0x07060302u); }
        const unsigned ua = sb + L_U + (31 + (tid >> 2)) * 80 + (tid & 3) * 16;
        LD128(ua) = c0; LD128(ua + U_CH) = c1;
      }
      sw_barrier4(sb + L_M + M_SWBAR, 4u, lane);
      STAMP(10);
      const int ch = w >> 1, pi = w & 1;
      const unsigned aA = sb + L_KC + ch * KC_CH + (unsigned)(128 * (2 * (32 * pi - 1) + (l31 >> 3) - h + 3) + 16 * (lane & 7));
      const unsigned aF = sb + L_U + ch * U_CH + (unsigned)(80 * (l31 + 16 - 16 * pi) + 16 * h);
      f32x16 y0 = {0}, y1 = {0};
#pragma unroll 8
      for (int i = 0; i < 32; ++i) {
        const unsigned fo = 80 * (15 - (i >> 1)) + ((i & 1) ? 0 : 32);
        const u32x4 A0 = LD128(aA + 256 * i), A1 = LD128(aA + 256 * i + 16384);
        const u32x4 F0 = LD128(aF + fo), F1 = LD128(aF + fo + 2560);
        y0 = mfma32(A0, F0, y0);
        y1 = mfma32(A0, F1, y1);
        y1 = mfma32(A1, F0, y1);
      }
      const unsigned pa = sb + L_P + lane * 16;
#pragma unroll
      for (int q = 0; q < 4; ++q) {
        LDF4(pa + w * 8192 + q * 1024) = (f32x4){y0[4 * q], y0[4 * q + 1], y0[4 * q + 2], y0[4 * q + 3]};
        LDF4(pa + w * 8192 + 4096 + q * 1024) = (f32x4){y1[4 * q], y1[4 * q + 1], y1[4 * q + 2], y1[4 * q + 3]};
      }
      sw_barrier4(sb + L_M + M_SWBAR, 8u, lane);
      {
        const unsigned r8 = lane >> 3, c = lane & 7;
#pragma unroll 4
        for (int i = 0; i < 16; ++i) {
          unsigned row = 128 * w + 8 * i + r8;
          gdma(row * 1024 + hg * 128 + ((c ^ wswz(row)) << 4), wo, sb + L_WO + (128 * w + 8 * i) * 128);
        }
      }
      const unsigned d2 = dskip2[cp];
      const float d0 = bflo(d2), d1 = bfhi(d2);
      const int tau = w >> 1, io = w & 1;
#pragma unroll
      for (int qi = 0; qi < 2; ++qi) {
        const int q = 2 * io + qi;
        const unsigned o = pa + tau * 4096 + q * 1024;
        f32x4 ya = LDF4(o) + LDF4(o + 8192), yb = LDF4(o + 16384) + LDF4(o + 24576);
        const unsigned uo = sb + L_U + (31 + 32 * tau + l31) * 80 + 16 * q + 8 * h;
        const u32x2 ua = LD64(uo), ub = LD64(uo + U_CH);
        const int t = 1024 * tau + 32 * l31 + 8 * q + 4 * h;
#pragma unroll
        for (int e = 0; e < 4; ++e) {
          const float u0 = (e & 1) ? bfhi(ua[e >> 1]) : bflo(ua[e >> 1]), u1 = (e & 1) ? bfhi(ub[e >> 1]) : bflo(ub[e >> 1]);
          const unsigned yy = pkbf(ya[e], yb[e]), ud = pkbf(u0 * d0, u1 * d1);
          ((unsigned*)out)[(t + e) * 512 + cp] = pkbf(bflo(yy) + bflo(ud), bfhi(yy) + bfhi(ud));
        }
      }
      STAMP(11);
    }
  }
  asm volatile("s_waitcnt vmcnt(0)" ::: "memory");
  BAR();
  STAMP(13);

  int lane2 = lane; asm volatile("" : "+v"(lane2));
  {
  const int lane = lane2, g4 = lane >> 4, l15 = lane & 15;
  const __amdgpu_buffer_rsrc_t rsO = __builtin_amdgcn_make_buffer_rsrc(ws + W_OX, 0, 2097152, 0x00020000);
  {
    const unsigned s0 = sb + L_OA + w * OA_SLOT, s1 = s0 + 8 * OA_SLOT;
    f32x4 o0 = LDF4(s0 + lane * 16) + LDF4(s1 + lane * 16), o1 = LDF4(s0 + 1024 + lane * 16) + LDF4(s1 + 1024 + lane * 16);
    const float inv = 1.0f / (LDF(s0 + 2048 + lane * 4) + LDF(s1 + 2048 + lane * 4));
    u32x4 pk = {pkbf(o0[0] * inv, o0[1] * inv), pkbf(o0[2] * inv, o0[3] * inv), pkbf(o1[0] * inv, o1[1] * inv), pkbf(o1[2] * inv, o1[3] * inv)};
    __builtin_amdgcn_raw_buffer_store_b128(pk, rsO, ((2 * grp + (w >> 2)) * 32 + 4 * hg + (w & 3)) * 1024 + lane * 16, 0, 16);
    asm volatile("s_waitcnt vmcnt(0)" ::: "memory");
  }
  BAR();
  if (tid == 0) __hip_atomic_store((gu32*)(ws + W_F2) + grp * 32 + hg, tag, RLX_AGENT);
  STAMP(14);

  {
    u32x4 wf[8][4];
    const unsigned r0 = 256 * kh + 16 * (g4 >> 1) + 4 * (g4 & 1) + (l15 >> 2), r1 = r0 + 8;
    const unsigned pc = (lane & 3) >> 1, ph = (lane & 1) * 8;
    const unsigned s0 = wswz(r0), s1 = wswz(r1);
#pragma unroll
    for (int nt = 0; nt < 4; ++nt) {
      const unsigned a0 = sb + L_WO + r0 * 128 + (((2 * nt + pc) ^ s0) << 4) + ph, a1 = sb + L_WO + r1 * 128 + (((2 * nt + pc) ^ s1) << 4) + ph;
#pragma unroll
      for (int s = 0; s < 8; ++s) wf[s][nt] = trfrag(a0 + s * 4096, a1 + s * 4096);
    }
#pragma unroll
    for (int s = 0; s < 8; ++s) asm volatile("" : "+v"(wf[s][0]), "+v"(wf[s][1]), "+v"(wf[s][2]), "+v"(wf[s][3]));
    for (unsigned spins = 0; spins < kSpinCap; ++spins) {
      unsigned f = __hip_atomic_load((gu32*)(ws + W_F2) + grp * 32 + 4 * kh + (lane & 3), RLX_AGENT);
      if (__all(f == tag)) break;
      __builtin_amdgcn_s_sleep(1);
    }
    __builtin_amdgcn_fence(__ATOMIC_ACQUIRE, "wavefront");
    STAMP(15);
    const int oo = ((2 * grp + (qq >> 1)) * 32 + 16 * kh + (g4 >> 1)) * 1024 + ((g4 & 1) * 32 + 16 * (qq & 1) + l15) * 16;
    u32x4 of[8];
#pragma unroll
    for (int s = 0; s < 8; ++s) of[s] = __builtin_amdgcn_raw_buffer_load_b128(rsO, oo + s * 2048, 0, 16);
    f32x4 acc[4] = {{0, 0, 0, 0}, {0, 0, 0, 0}, {0, 0, 0, 0}, {0, 0, 0, 0}};
#pragma unroll
    for (int s = 0; s < 8; ++s)
#pragma unroll
      for (int nt = 0; nt < 4; ++nt) acc[nt] = mfma16(wf[s][nt], of[s], acc[nt]);
    const unsigned xa = sb + L_P + lane * 16;
    LDF4(xa + w * 2048) = kh ? acc[0] : acc[2];
    LDF4(xa + w * 2048 + 1024) = kh ? acc[1] : acc[3];
    BAR();
    f32x4 q0 = (kh ? acc[2] : acc[0]) + LDF4(xa + (w ^ 4) * 2048);
    f32x4 q1 = (kh ? acc[3] : acc[1]) + LDF4(xa + (w ^ 4) * 2048 + 1024);
    auto sa = __builtin_amdgcn_permlane16_swap(pkbf(q0[0], q0[1]), pkbf(q1[0], q1[1]), false, false);
    auto sc = __builtin_amdgcn_permlane16_swap(pkbf(q0[2], q0[3]), pkbf(q1[2], q1[3]), false, false);
    u32x4 v = {sa[0], sc[0], sa[1], sc[1]};
    *(u32x4*)(out + (64 * grp + 16 * qq + l15) * 1024 + 512 + 64 * hg + 32 * kh + 16 * (g4 & 1) + 8 * (g4 >> 1)) = v;
  }
  STAMP(16);
  }
  if (b == 0 && w == 0) {
    for (unsigned spins = 0; spins < kSpinCap; ++spins) {
      bool ok = true;
#pragma unroll
      for (int k = 0; k < 4; ++k) {
        const int i = lane + 64 * k;
        ok &= __hip_atomic_load((gu32*)(ws + W_F2) + (i >> 3) * 32 + (i & 7), RLX_AGENT) == tag;
      }
      if (__all(ok)) break;
      __builtin_amdgcn_s_sleep(1);
    }
    if (lane == 0) __hip_atomic_store((gu32*)(ws + W_E), tag, RLX_AGENT);
  }
}

extern "C" void kernel_launch(void* const* d_in, const int* in_sizes, int n_in, void* d_out, int out_size, void* d_ws,
                              size_t ws_size, hipStream_t stream) {
  typedef const unsigned short* P;
  fused_kernel<<<256, 512, 0, stream>>>((P)d_in[0], (P)d_in[1], (P)d_in[2], (P)d_in[3], (P)d_in[4], (P)d_in[5], (P)d_in[6],
                                        (const unsigned*)d_in[7], (const unsigned*)d_in[8], (P)d_in[9], (P)d_in[10], (P)d_in[11],
                                        (P)d_in[12], (P)d_in[13], (unsigned short*)d_out, (unsigned char*)d_ws);
}
```

```cpp
#include <hip/hip_runtime.h>
#include <hip/hip_bf16.h>
#include <stdint.h>

typedef float f32x16 __attribute__((ext_vector_type(16)));
typedef float f32x8 __attribute__((ext_vector_type(8)));
typedef float f32x4 __attribute__((ext_vector_type(4)));
typedef float f32x2 __attribute__((ext_vector_type(2)));
typedef __bf16 bf16x8 __attribute__((ext_vector_type(8)));
typedef __bf16 bf16x2 __attribute__((ext_vector_type(2)));
typedef _Float16 f16x8 __attribute__((ext_vector_type(8)));
typedef _Float16 f16x2 __attribute__((ext_vector_type(2)));
typedef short s16x4 __attribute__((ext_vector_type(4)));
typedef unsigned u32x4 __attribute__((ext_vector_type(4)));
typedef unsigned u32x2 __attribute__((ext_vector_type(2)));
#define LAS __attribute__((address_space(3)))
#define GAS __attribute__((address_space(1)))
typedef GAS unsigned gu32;
typedef GAS unsigned long long gu64;
#define RLX_AGENT __ATOMIC_RELAXED, __HIP_MEMORY_SCOPE_AGENT

constexpr unsigned L_X = 0;
constexpr unsigned L_TLO = 0, TLO_CH = 16896, L_THI = 33792, THI_CH = 11088;
constexpr unsigned L_KC = 0, KC_CH = 16640;
constexpr unsigned L_WO = 0;
constexpr unsigned L_B = 65536;
constexpr unsigned L_WQ = L_B;
constexpr unsigned L_R2 = L_B + 65536;
constexpr unsigned L_DUMP = L_B;
constexpr unsigned L_OA = L_B, OA_SLOT = 2304;
constexpr unsigned L_P = L_B + 40960;
constexpr unsigned L_U = L_B + 73728, U_CH = 7600;
constexpr unsigned L_Q = L_B + 88928;
constexpr unsigned L_MX = L_Q + 8192;
constexpr unsigned L_M = L_MX + 1024;
constexpr unsigned LDS_BYTES = L_M + 128;
static_assert(LDS_BYTES <= 163840, "lds");
constexpr unsigned M_RDY = 4, M_QMAX = 8, M_KMAX = 12, M_SCALE = 16  , M_SW = 32  ;
#define CONV_PRIO 0
#define N_SLOW 8
static_assert(N_SLOW >= 4 && N_SLOW <= 28, "the convolution's stores ride on the first four key tiles");

constexpr unsigned KV_TILE = 2304, KV_BYTES = 64 * KV_TILE;
constexpr unsigned W_E = 0, W_F1 = 2048, W_F2 = 4096, W_KV = 8192, W_OX = W_KV + KV_BYTES, W_END = W_OX + 2097152;
constexpr unsigned W_DBG = W_END;

constexpr unsigned kSpinCap = 1u << 20;

#define LD32(a) (*(LAS unsigned*)(size_t)(a))
#define LDF(a) (*(LAS float*)(size_t)(a))
#define LD64(a) (*(LAS u32x2*)(size_t)(a))
#define LD128(a) (*(LAS u32x4*)(size_t)(a))
#define LDF4(a) (*(LAS f32x4*)(size_t)(a))
#define BAR() asm volatile("s_waitcnt lgkmcnt(0)\n\ts_barrier" ::: "memory")

#define STAMP(i)
#define STAMP_INIT()
#define STAMP_OUT()

__device__ __forceinline__ float bflo(unsigned v) { return __uint_as_float(v << 16); }
__device__ __forceinline__ float bfhi(unsigned v) { return __uint_as_float(v & 0xffff0000u); }
__device__ __forceinline__ unsigned pkbf(float a, float b) { f32x2 x = {a, b}; return __builtin_bit_cast(unsigned, __builtin_convertvector(x, bf16x2)); }
__device__ __forceinline__ unsigned pkh(float a, float b) { f32x2 x = {a, b}; return __builtin_bit_cast(unsigned, __builtin_convertvector(x, f16x2)); }
template <int CTRL> __device__ __forceinline__ unsigned dppu(unsigned v) { return __builtin_amdgcn_update_dpp(0u, v, CTRL, 0xf, 0xf, true); }
__device__ __forceinline__ unsigned umax(unsigned a, unsigned b) { return a > b ? a : b; }
__device__ __forceinline__ unsigned row_umax(unsigned v) {
  v = umax(v, dppu<0x128>(v)); v = umax(v, dppu<0x124>(v)); v = umax(v, dppu<0x122>(v)); return umax(v, dppu<0x121>(v));
}
__device__ __forceinline__ unsigned wave_umax(unsigned v) {
  v = row_umax(v);
  auto a = __builtin_amdgcn_permlane16_swap(v, v, false, false);
  v = umax(a[0], a[1]);
  auto c = __builtin_amdgcn_permlane32_swap(v, v, false, false);
  return umax(c[0], c[1]);
}
__device__ __forceinline__ float sum16(float a, float b) {
  auto r = __builtin_amdgcn_permlane16_swap(__float_as_uint(a), __float_as_uint(b), false, false);
  return __uint_as_float(r[0]) + __uint_as_float(r[1]);
}
__device__ __forceinline__ float sum32(float v) {
  auto c = __builtin_amdgcn_permlane32_swap(__float_as_uint(v), __float_as_uint(v), false, false);
  return __uint_as_float(c[0]) + __uint_as_float(c[1]);
}
__device__ __forceinline__ float sq2(unsigned v, float c) {
  return __builtin_amdgcn_fdot2_f32_bf16(__builtin_bit_cast(bf16x2, v), __builtin_bit_cast(bf16x2, v), c, false);
}
__device__ __forceinline__ unsigned opaque(unsigned v) { asm volatile("" : "+v"(v)); return v; }
struct cf { float x, y; };
__device__ __forceinline__ cf cmul(cf a, cf b) { return {a.x * b.x - a.y * b.y, a.x * b.y + a.y * b.x}; }
__device__ __forceinline__ cf cexp_(float re, float im) {
  float m = __builtin_amdgcn_exp2f(re * 1.44269504088896f);
  float f = __builtin_amdgcn_fractf(im * 0.159154943091895f);
  return {m * __builtin_amdgcn_cosf(f), m * __builtin_amdgcn_sinf(f)};
}
__device__ __forceinline__ u32x4 trfrag(unsigned a0, unsigned a1) {
  s16x4 lo = __builtin_amdgcn_ds_read_tr16_b64_v4i16((LAS s16x4*)(size_t)a0);
  s16x4 hi = __builtin_amdgcn_ds_read_tr16_b64_v4i16((LAS s16x4*)(size_t)a1);
  u32x2 l = __builtin_bit_cast(u32x2, lo), h = __builtin_bit_cast(u32x2, hi);
  return (u32x4){l[0], l[1], h[0], h[1]};
}
__device__ __forceinline__ f32x16 mfma32(u32x4 a, u32x4 b, f32x16 c) {
  return __builtin_amdgcn_mfma_f32_32x32x16_bf16(__builtin_bit_cast(bf16x8, a), __builtin_bit_cast(bf16x8, b), c, 0, 0, 0);
}
__device__ __forceinline__ f32x4 mfma16(u32x4 a, u32x4 b, f32x4 c) {
  return __builtin_amdgcn_mfma_f32_16x16x32_bf16(__builtin_bit_cast(bf16x8, a), __builtin_bit_cast(bf16x8, b), c, 0, 0, 0);
}
#define GLD1(dst, voff, base) asm volatile("global_load_dword %0, %1, %2" : "=v"(dst) : "v"(voff), "s"(base))
#define GLD4(dst, voff, base, imm) asm volatile("global_load_dwordx4 %0, %1, %2 offset:%3" : "=v"(dst) : "v"(voff), "s"(base), "i"(imm))
__device__ __forceinline__ void gdma4(unsigned voff, const void* base, unsigned lds_dst) {
  unsigned keep;
  asm volatile("s_mov_b32 %0, m0\n\ts_mov_b32 m0, %3\n\ts_nop 0\n\tglobal_load_lds_dwordx4 %1, %2\n\tglobal_load_lds_dwordx4 %1, %2 offset:1024\n\t"
               "global_load_lds_dwordx4 %1, %2 offset:2048\n\tglobal_load_lds_dwordx4 %1, %2 offset:3072\n\ts_mov_b32 m0, %0"
               : "=&s"(keep) : "v"(voff), "s"(base), "s"(lds_dst) : "memory");
}
#define DMA_PAIR "global_load_lds_dwordx4 %2, %1\n\ts_add_u32 m0, m0, 0x400\n\ts_nop 0\n\tglobal_load_lds_dwordx4 %3, %1\n\ts_add_u32 m0, m0, 0x400\n\tv_add_u32 %2, 0x4000, %2\n\tv_add_u32 %3, 0x4000, %3\n\t"
__device__ __forceinline__ void gdma_rows16(unsigned v0, unsigned v1, const void* base, unsigned lds_dst) {
  unsigned keep;
  asm volatile("s_mov_b32 %0, m0\n\ts_mov_b32 m0, %4\n\ts_nop 0\n\t" DMA_PAIR DMA_PAIR DMA_PAIR DMA_PAIR DMA_PAIR DMA_PAIR DMA_PAIR DMA_PAIR "s_mov_b32 m0, %0"
               : "=&s"(keep), "+s"(base), "+v"(v0), "+v"(v1) : "s"(lds_dst) : "memory", "scc");
}
#define DMA_X(v, o) "global_load_lds_dwordx4 " v ", %1 offset:" #o "\n\ts_add_u32 m0, m0, "
__device__ __forceinline__ void gdma_x(unsigned v0, unsigned v1, const void* base, unsigned lds_dst) {
  unsigned keep;
  asm volatile("s_mov_b32 %0, m0\n\ts_mov_b32 m0, %4\n\ts_nop 0\n\t"
               DMA_X("%2", 0) "896\n\ts_nop 0\n\t" DMA_X("%2", 128) "896\n\ts_nop 0\n\t" DMA_X("%2", 256) "896\n\ts_nop 0\n\t" DMA_X("%2", 384) "1408\n\ts_nop 0\n\t"
               DMA_X("%3", 0) "896\n\ts_nop 0\n\t" DMA_X("%3", 128) "896\n\ts_nop 0\n\t" DMA_X("%3", 256) "896\n\ts_nop 0\n\t" DMA_X("%3", 384) "0\n\t"
               "s_mov_b32 m0, %0"
               : "=&s"(keep) : "s"(base), "v"(v0), "v"(v1), "s"(lds_dst) : "memory", "scc");
}
__device__ __forceinline__ void gdma_rows4(unsigned v0, unsigned v1, const void* base, unsigned lds_dst) {
  unsigned keep;
  asm volatile("s_mov_b32 %0, m0\n\ts_mov_b32 m0, %4\n\ts_nop 0\n\t" DMA_PAIR DMA_PAIR "s_mov_b32 m0, %0"
               : "=&s"(keep), "+s"(base), "+v"(v0), "+v"(v1) : "s"(lds_dst) : "memory", "scc");
}
__device__ __forceinline__ void gdma1(unsigned voff, const void* base, unsigned lds_dst) {
  unsigned keep;
  asm volatile("s_mov_b32 %0, m0\n\ts_mov_b32 m0, %3\n\ts_nop 0\n\tglobal_load_lds_dwordx4 %1, %2\n\ts_mov_b32 m0, %0"
               : "=&s"(keep) : "v"(voff), "s"(base), "s"(lds_dst) : "memory");
}
__device__ __forceinline__ unsigned wswz(unsigned row) { return ((((row >> 1) & 3u) ^ (((row >> 3) & 1u) << 1)) << 1); }

__device__ __forceinline__ void wait4(unsigned addr, unsigned want) {
  _Pragma("unroll 1") for (unsigned spins = 0; spins < kSpinCap; ++spins) {
    u32x4 v;
    asm volatile("ds_read_b128 %0, %1\n\ts_waitcnt lgkmcnt(0)" : "=v"(v) : "v"(addr) : "memory");
    if (__all(v[0] == want && v[1] == want && v[2] == want && v[3] == want)) break;
    __builtin_amdgcn_s_sleep(1);
  }
}
__device__ __forceinline__ void post(unsigned addr, unsigned val) {
  asm volatile("ds_write_b32 %0, %1" :: "v"(addr), "v"(val) : "memory");
}

__device__ __forceinline__ void sync4(unsigned sb, int w, int k, unsigned tag) {
  post(sb + L_M + M_SW + k * 16 + w * 4, tag);
  wait4(sb + L_M + M_SW + k * 16, tag);
}

#define STR_(x) #x
#define STR(x) STR_(x)
#define VR(b, o) "v[" STR(b) "+" STR(o) "]"
#define VRR(b, o, n) "v[" STR(b) "+" STR(o) ":" STR(b) "+" STR(o) "+" STR(n) "-1]"
#define AR4(i) "a[" STR(i) ":" STR(i) "+3]"
#define A_EX(S, i) "v_exp_f32 " VR(S, i) ", " VR(S, i) "\n\t"
#define A_QK(S, r, q) "v_mfma_f32_32x32x16_bf16 " VRR(S, 0, 16) ", " AR4(80 + 12 * r) ", " AR4(64 + 4 * q) ", v[80:95]\n\t"
#define A_PV0(O, r, S) "v_smfmac_f32_32x32x32_bf16 " O ", " AR4(84 + 12 * r) ", " VRR(S, 0, 8) ", v79\n\t"
#define A_PV1(O, r, S) "v_smfmac_f32_32x32x32_bf16 " O ", " AR4(88 + 12 * r) ", " VRR(S, 8, 8) ", v79\n\t"
#define A_RD4(o, q) "v_accvgpr_read_b32 " VR(80, o) ", a[60+" #q "]\n\tv_accvgpr_read_b32 " VR(81, o) ", a[60+" #q "]\n\t" \
                    "v_accvgpr_read_b32 " VR(82, o) ", a[60+" #q "]\n\tv_accvgpr_read_b32 " VR(83, o) ", a[60+" #q "]\n\t"
#define A_SHIFT_EXACT(q) A_RD4(0, q) A_RD4(4, q) A_RD4(8, q) A_RD4(12, q) "s_nop 1\n\t"
#define A_SHIFT_NONE(q) ""
#define A_BLOCK(Sc, Sp, Op, rp, rn, qn, Oc, rc, SH, XM, X0, X1) \
  A_EX(Sc, 0) A_EX(Sc, 1) A_PV1(Op, rp, Sp) XM A_EX(Sc, 2) A_EX(Sc, 3) A_EX(Sc, 4) A_EX(Sc, 5) X0 SH(qn) A_QK(Sp, rn, qn) \
  A_EX(Sc, 6) A_EX(Sc, 7) A_EX(Sc, 8) A_EX(Sc, 9) A_EX(Sc, 10) A_PV0(Oc, rc, Sc) \
  A_EX(Sc, 11) A_EX(Sc, 12) A_EX(Sc, 13) X1 A_EX(Sc, 14) A_EX(Sc, 15)
#define A_KV(r) "s_mul_i32 %[so], %[tl], 2304\n\ts_add_u32 %[tl], %[tl], 1\n\ts_and_b32 %[tl], %[tl], 63\n\t" \
  "buffer_load_dwordx4 " AR4(80 + 12 * r) ", %[ko], %[rk], %[so] offen sc1\n\tbuffer_load_dwordx4 " AR4(84 + 12 * r) ", %[vo], %[rk], %[so] offen sc1\n\t" \
  "buffer_load_dwordx4 " AR4(88 + 12 * r) ", %[vo], %[rk], %[so] offen offset:640 sc1\n\t"
#define A_STEP(r, rp, rn, SH, XM) \
  A_BLOCK(96, 112, "%[o3]", rp, r, 1, "%[o0]", r, SH, XM, "", A_KV(rp) "s_bitcmp1_b32 %[fl], 0\n\ts_cbranch_scc1 2" #r "f\n\t3" #r ":\n\t") \
  A_BLOCK(112, 96, "%[o0]", r, r, 2, "%[o1]", r, SH, "", "", "") \
  A_BLOCK(96, 112, "%[o1]", r, r, 3, "%[o2]", r, SH, "", "", "") \
  A_BLOCK(112, 96, "%[o2]", r, rn, 0, "%[o3]", r, SH, "", "s_waitcnt vmcnt(6)\n\t", "s_sub_u32 %[n], %[n], 1\n\ts_cmp_eq_u32 %[n], 0\n\t")
#define A_YST(r, ya, yb, so) "2" #r ":\n\ts_movk_i32 %[so], " #so "\n\tbuffer_store_dword " ya ", %[yo], %[ry], %[so] offen\n\t" \
  "buffer_store_dword " yb ", %[yo], %[ry], %[so] offen offset:2048\n\ts_branch 3" #r "b\n\t"
#define A_EXIT(r) "4" #r ":\n\t" A_PV1("%[o3]", r, 112) "s_branch 99f\n\t"
#define A_MOV4(o) "v_mov_b32 " VR(80, o) ", %[nb]\n\tv_mov_b32 " VR(81, o) ", %[nb]\n\tv_mov_b32 " VR(82, o) ", %[nb]\n\tv_mov_b32 " VR(83, o) ", %[nb]\n\t"
#define A_ZERO(O) "v_mfma_f32_32x32x8_bf16 " O ", v[78:79], v[78:79], 0\n\t"
#define ITSTAMP
#define ITOPS
#define ITCLOB
#define A_ATT(SH) \
  "ds_read_b128 a[64:67], %[qa]\n\tds_read_b128 a[68:71], %[qa] offset:1024\n\tds_read_b128 a[72:75], %[qa] offset:2048\n\tds_read_b128 a[76:79], %[qa] offset:3072\n\t" \
  "v_mov_b32 v78, 0\n\tv_mov_b32 v79, 0\n\t" A_MOV4(0) A_MOV4(4) A_ZERO("%[o0]") A_MOV4(8) A_MOV4(12) A_ZERO("%[o1]") A_ZERO("%[o2]") A_ZERO("%[o3]") \
  "s_waitcnt vmcnt(6) lgkmcnt(0)\n\t" SH(0) A_QK(96, 0, 0) "v_mov_b32 v79, 0xdddd\n\ts_setprio 1\n\ts_nop 13\n\t" \
  A_EX(96, 0) A_EX(96, 1) "s_branch 11f\n\t" \
  "10:\n\t" \
  A_STEP(0, 3, 1, SH, "11:\n\t") "s_cbranch_scc1 40f\n\t" \
  A_STEP(1, 0, 2, SH, "") "s_cbranch_scc1 41f\n\t" \
  A_STEP(2, 1, 3, SH, "") "s_cbranch_scc1 42f\n\t" \
  A_STEP(3, 2, 0, SH, "") ITSTAMP "s_mov_b32 %[fl], 0\n\ts_cbranch_scc0 10b\n\t" \
  A_EXIT(3) A_EXIT(0) A_EXIT(1) A_EXIT(2) \
  A_YST(0, "%[y0]", "%[y1]", 0) A_YST(1, "%[y2]", "%[y3]", 0x1000) A_YST(2, "%[y4]", "%[y5]", 0x4000) A_YST(3, "%[y6]", "%[y7]", 0x5000) \
  "99:\n\ts_setprio 0\n\ts_nop 15"
#define C8(p, a, b, c, d, e, f, g, h) p #a, p #b, p #c, p #d, p #e, p #f, p #g, p #h
#define CLOB_V78_127 "v78", "v79", C8("v", 80, 81, 82, 83, 84, 85, 86, 87), C8("v", 88, 89, 90, 91, 92, 93, 94, 95), CLOB_V96_127
#define CLOB_V96_127 C8("v", 96, 97, 98, 99, 100, 101, 102, 103), C8("v", 104, 105, 106, 107, 108, 109, 110, 111), \
  C8("v", 112, 113, 114, 115, 116, 117, 118, 119), C8("v", 120, 121, 122, 123, 124, 125, 126, 127)
#define CLOB_A0_31 C8("a", 0, 1, 2, 3, 4, 5, 6, 7), C8("a", 8, 9, 10, 11, 12, 13, 14, 15), C8("a", 16, 17, 18, 19, 20, 21, 22, 23), C8("a", 24, 25, 26, 27, 28, 29, 30, 31)
#define CLOB_A32_59 C8("a", 32, 33, 34, 35, 36, 37, 38, 39), C8("a", 40, 41, 42, 43, 44, 45, 46, 47), C8("a", 48, 49, 50, 51, 52, 53, 54, 55), "a56", "a57", "a58", "a59"
#define CLOB_A60_127 "a60", "a61", "a62", "a63", C8("a", 64, 65, 66, 67, 68, 69, 70, 71), C8("a", 72, 73, 74, 75, 76, 77, 78, 79), \
  C8("a", 80, 81, 82, 83, 84, 85, 86, 87), C8("a", 88, 89, 90, 91, 92, 93, 94, 95), \
  C8("a", 96, 97, 98, 99, 100, 101, 102, 103), C8("a", 104, 105, 106, 107, 108, 109, 110, 111), \
  C8("a", 112, 113, 114, 115, 116, 117, 118, 119), C8("a", 120, 121, 122, 123, 124, 125, 126, 127)
#define CLOB_A_ALL CLOB_A0_31, CLOB_A32_59, CLOB_A60_127
typedef int i32x4 __attribute__((ext_vector_type(4)));
__device__ __forceinline__ i32x4 make_srd(const void* p, unsigned bytes) {
  const unsigned long long a = (unsigned long long)p;
  return (i32x4){(int)(unsigned)a, (int)((unsigned)(a >> 32) & 0xffffu), (int)bytes, 0x00020000};
}

__device__ __forceinline__ unsigned kv_first(unsigned tl, unsigned ko, unsigned vo, i32x4 rk) {
  unsigned so;
  asm volatile(A_KV(0) A_KV(1) A_KV(2) : [tl] "+s"(tl), [so] "=&s"(so) : [ko] "v"(ko), [vo] "v"(vo), [rk] "s"(rk) : "memory", "scc", CLOB_A60_127);
  return tl;
}
template <bool EXACT>
__device__ __forceinline__ void attend(f32x16 (&O)[4], unsigned tl, unsigned n, unsigned fl, unsigned qa, float nb, unsigned ko, unsigned vo, i32x4 rk,
                                       unsigned yo, i32x4 ry, const unsigned (&y)[8], unsigned sa) {
  unsigned so;
#define ATT_OPS : [o0] "=&v"(O[0]), [o1] "=&v"(O[1]), [o2] "=&v"(O[2]), [o3] "=&v"(O[3]), [tl] "+s"(tl), [n] "+s"(n), [fl] "+s"(fl), [so] "=&s"(so) ITOPS \
    : [qa] "v"(qa), [nb] "v"(nb), [ko] "v"(ko), [vo] "v"(vo), [rk] "s"(rk), [yo] "v"(yo), [ry] "s"(ry), [y0] "a"(y[0]), [y1] "a"(y[1]), [y2] "a"(y[2]), \
      [y3] "a"(y[3]), [y4] "a"(y[4]), [y5] "a"(y[5]), [y6] "a"(y[6]), [y7] "a"(y[7]) : "memory", "scc", CLOB_V78_127, CLOB_A60_127 ITCLOB
  if (EXACT) asm volatile(A_ATT(A_SHIFT_EXACT) ATT_OPS);
  else asm volatile(A_ATT(A_SHIFT_NONE) ATT_OPS);
}

__global__ __launch_bounds__(512, 2) void fused_kernel(
    const unsigned short* __restrict__ u, const unsigned short* __restrict__ Are, const unsigned short* __restrict__ Aim,
    const unsigned short* __restrict__ Bre, const unsigned short* __restrict__ Bim, const unsigned short* __restrict__ Cre,
    const unsigned short* __restrict__ Cim, const unsigned* __restrict__ logdt2, const unsigned* __restrict__ dskip2,
    const unsigned short* __restrict__ x, const unsigned short* __restrict__ wq, const unsigned short* __restrict__ wk,
    const unsigned short* __restrict__ wv, const unsigned short* __restrict__ wo, unsigned short* __restrict__ out,
    unsigned char* __restrict__ ws) {
  __shared__ __attribute__((aligned(128))) unsigned char smem[LDS_BYTES];
  const unsigned sb = (unsigned)(size_t)(LAS unsigned char*)smem;
  const int tid = threadIdx.x, lane = tid & 63;
  const int w = __builtin_amdgcn_readfirstlane(tid >> 6);
  const int b = blockIdx.x, xcd = b & 7, bi = b >> 3;
  const int grp = xcd * 4 + (bi >> 3), hg = bi & 7, cp = xcd * 32 + bi;
  const int h = lane >> 5, g4 = lane >> 4, l15 = lane & 15, l31 = lane & 31;
  const int qq = w & 3, kh = w >> 2;
  const int chain = hg >> 2;
  const int vl = (qq - hg - 1) & 3;
  STAMP_INIT();
  asm volatile("" :: "s"(u), "s"(Are), "s"(Aim), "s"(Bre), "s"(Bim), "s"(Cre), "s"(Cim), "s"(logdt2), "s"(dskip2), "s"(x), "s"(wq),
               "s"(wk), "s"(wv), "s"(wo), "s"(out), "s"(ws));
  unsigned E, ld2, d2;
  const unsigned r8 = lane >> 3, c8 = lane & 7;
  const unsigned xa0 = sb + L_X + qq * 16384 + (l15 >> 3) * 4096 + (l15 & 7) * 128 + ((g4 ^ (l15 >> 1)) << 4);
  const unsigned wq0 = r8 * 1024 + hg * 128 + ((c8 ^ wswz(r8)) << 4);
  const int tch = (w >> 1) & 1, par = w & 1;
  unsigned T[53], tag;
#define MAKE_TAG() asm volatile("s_load_dword %0, %3, 0x0\n\ts_load_dword %1, %4, %6\n\ts_load_dword %2, %5, %6\n\ts_waitcnt lgkmcnt(0)" \
    : "=&s"(E), "=&s"(ld2), "=&s"(d2) : "s"(ws), "s"(logdt2), "s"(dskip2), "s"(cp * 4) : "memory"); \
  tag = __builtin_amdgcn_readfirstlane(E + (((E * 0x9E3779B1u ^ (E >> 15) ^ 0x5bd1e995u) & 0x7fffffffu) | 0x101u))

  if (kh) {
    const unsigned xo = (64 * grp + 16 * qq + r8) * 1024 + ((c8 ^ (r8 >> 1)) << 4);
    if (vl != 3) asm volatile("s_barrier" ::: "memory");
    else __builtin_amdgcn_s_setprio(3);
    gdma_x(xo, (xo ^ 64) + 8192, x, sb + L_X + qq * 16384);
    gdma_x(xo + 512, (xo ^ 64) + 8704, x, sb + L_X + qq * 16384 + 8192);
    if (vl != 3) {
      gdma_rows16(wq0 + vl * 131072, (wq0 ^ 64) + vl * 131072 + 8192, wq, sb + L_WQ + vl * 16384);
      MAKE_TAG();
      asm volatile("s_waitcnt vmcnt(0)" ::: "memory");
    } else {
      const unsigned short* wkv = chain ? wv : wk;
#pragma unroll
      for (int i = 0; i < 4; ++i) gdma4((unsigned)(i * 4096 + lane * 16), wkv, sb + L_R2 + i * 4096);
      asm volatile("s_barrier" ::: "memory");
      const unsigned wa = opaque(sb + L_R2 + (8 * g4 + (l15 >> 2)) * 32 + (lane & 3) * 8);
      const unsigned xe = opaque(xa0), xd = opaque(xa0 ^ 64);
      const unsigned off = (2 * grp + (qq >> 1)) * KV_TILE + l15 * 16 +
                           (chain ? 1024 + (qq & 1) * 640 + (g4 >> 1) * 320 + (g4 & 1) * 8 : (qq & 1) * 256 + (g4 & 1) * 512 + (g4 >> 1) * 8);
      MAKE_TAG();
      asm volatile("s_waitcnt vmcnt(0)" ::: "memory");
      STAMP(1);
      f32x4 acc = {0, 0, 0, 0}, acd = {0, 0, 0, 0};
#define KVSTEP(A, B) _Pragma("unroll") for (int s = 0; s < 16; s += 2) { \
        const u32x4 x0 = LD128(xe + (s >> 3) * 8192 + ((s & 7) >> 1) * 1024), x1 = LD128(xd + (s >> 3) * 8192 + ((s & 7) >> 1) * 1024); \
        const u32x4 w0 = trfrag(wa + s * 1024, wa + s * 1024 + 128), w1 = trfrag(wa + s * 1024 + 1024, wa + s * 1024 + 1152); \
        acc = mfma16(A##0, B##0, acc); acd = mfma16(A##1, B##1, acd); }
      if (chain) { KVSTEP(x, w) } else { KVSTEP(w, x) }
      acc = (acc + acd) * (chain ? 1.0f : 0.25f * 1.44269504088896f);
      const u32x2 pk = {pkbf(acc[0], acc[1]), pkbf(acc[2], acc[3])};
      const __amdgpu_buffer_rsrc_t rs = __builtin_amdgcn_make_buffer_rsrc(ws + W_KV, 0, KV_BYTES, 0x00020000);
      __builtin_amdgcn_raw_buffer_store_b64(pk, rs, off, 0, 16);
      __builtin_amdgcn_raw_buffer_store_b64((u32x2){0x3f803f80u, 0x3f803f80u}, rs, (chain && l15 == 0) ? off + 256 : 0x80000000u, 0, 16);
      const float n2 = sq2(pk[0], sq2(pk[1], 0.f));
      const unsigned kn2 = row_umax(__float_as_uint(sum32(sum16(n2, n2))));
      asm volatile("s_waitcnt vmcnt(0)" ::: "memory");
      STAMP(2);
      if (lane == 0) __hip_atomic_store((gu64*)(ws + W_F1) + chain * 128 + 4 * grp + qq, ((unsigned long long)tag << 32) | kn2, RLX_AGENT);
      __builtin_amdgcn_s_setprio(0);
    }
#pragma unroll
    for (int j = 0; j < 53; ++j) asm volatile("" : "=v"(T[j]));
  } else {
    unsigned pAr, pAi, pBr, pBi, pCr, pCi;
    const unsigned po = (unsigned)((2 * cp + tch) * 256 + lane * 4);
    GLD1(pAr, po, Are); GLD1(pAi, po, Aim); GLD1(pBr, po, Bre); GLD1(pBi, po, Bim); GLD1(pCr, po, Cre); GLD1(pCi, po, Cim);
    LD32(sb + L_M + M_QMAX) = 0;
    asm volatile("s_barrier" ::: "memory");
    gdma_rows4(wq0 + (384 + 32 * w) * 1024, (wq0 ^ 64) + (392 + 32 * w) * 1024, wq, sb + L_WQ + (384 + 32 * w) * 128);
    MAKE_TAG();
    const float ldt = tch ? bfhi(ld2) : bflo(ld2);
    const float eh = ldt * 1.44269502162933349609f, el = fmaf(ldt, 1.44269502162933349609f, -eh) + ldt * 1.925963033500011e-8f;
    const float e0 = __builtin_amdgcn_exp2f(eh);
    const float dt = bflo(pkbf(fmaf(e0, el * 0.693147180559945f, e0), 0.f));
    asm volatile("s_waitcnt vmcnt(4)" : "+v"(pAr), "+v"(pAi), "+v"(pBr), "+v"(pBi), "+v"(pCr), "+v"(pCi) :: "memory");
    STAMP(1);
    const float ar0 = dt * bflo(pAr), ar1 = dt * bfhi(pAr);
    float lam = 0.f;
    if (__any(fmaxf(ar0, ar1) > 0.f)) lam = __uint_as_float(wave_umax(__float_as_uint(fmaxf(fmaxf(ar0, ar1), 0.f))));
    const float ar = (par ? ar1 : ar0) - lam, ai = dt * (par ? bfhi(pAi) : bflo(pAi));
    {
      cf c0 = cmul({bflo(pCr), bflo(pCi)}, {bflo(pBr), bflo(pBi)});
      cf c1 = cmul({bfhi(pCr), bfhi(pCi)}, {bfhi(pBr), bfhi(pBi)});
      int ex = (int)(wave_umax(__float_as_uint(fmaxf(fmaxf(fabsf(c0.x), fabsf(c0.y)), fmaxf(fabsf(c1.x), fabsf(c1.y))))) >> 23);
      ex = ex < 8 ? 8 : (ex > 245 ? 245 : ex);
      const float sdn = __uint_as_float((unsigned)(252 - ex) << 23), sup = __uint_as_float((unsigned)(ex + 2) << 23) * dt;
      LD64(sb + L_M + M_SCALE + tch * 8) = (u32x2){__float_as_uint(sup), __float_as_uint(lam * 1.44269504088896f)};
      cf c = par ? c1 : c0; c.x *= sdn; c.y *= sdn;
      const cf w1 = cexp_(ar, ai), W = cexp_(256.f * ar, 256.f * ai);
      const cf w2 = cmul(w1, w1), w4 = cmul(w2, w2), w6 = cmul(w4, w2);
      cf v = c;
#pragma unroll
      for (int j = 0; j < 8; ++j) { T[32 + j] = pkh(v.x, v.y); if (j < 7) v = cmul(v, w1); }
      v = cmul(c, W); T[40] = pkh(v.x, v.y);
      cf e = cmul(v, w2); T[41] = pkh(e.x, e.y);
      e = cmul(v, w4); T[42] = pkh(e.x, e.y);
      e = cmul(v, w6); T[43] = pkh(e.x, e.y);
      v = cmul(v, W); T[44] = pkh(v.x, v.y);
      e = cmul(v, w4); T[45] = pkh(e.x, e.y);
      v = cmul(v, W); T[46] = pkh(v.x, v.y);
      e = cmul(v, w4); T[47] = pkh(e.x, e.y);
#pragma unroll
      for (int j = 16; j < 21; ++j) { v = cmul(v, W); T[32 + j] = pkh(v.x, v.y); }
    }
    {
      cf p[16];
      cf g = cexp_(8.f * ar, -8.f * ai);
      p[0] = {1.f, 0.f}; p[1] = g;
      g = cmul(g, g); p[2] = g; p[3] = cmul(p[1], g);
      g = cmul(g, g);
      p[4] = g;
#pragma unroll
      for (int i = 1; i < 4; ++i) p[4 + i] = cmul(p[i], g);
      g = cmul(g, g);
      p[8] = g;
#pragma unroll
      for (int i = 1; i < 8; ++i) p[8 + i] = cmul(p[i], g);
      g = cmul(g, g);
#pragma unroll
      for (int i = 0; i < 16; ++i) {
        T[i] = pkh(p[i].x, p[i].y);
        cf q = i ? cmul(p[i], g) : g;
        T[16 + i] = pkh(q.x, q.y);
      }
    }
#pragma unroll
    for (int j = 0; j < 53; ++j) asm volatile("" : "+v"(T[j]));
    STAMP(2);
    asm volatile("s_waitcnt vmcnt(0)" ::: "memory");
  }
  STAMP(3);
  BAR();
  STAMP(4);

  const bool watcher = kh && vl == 0;
  u32x4 fl0, fl1;
  const unsigned fo = W_F1 + lane * 16;
#define FLAGS_LOAD(W) asm volatile("global_load_dwordx4 %0, %2, %3 sc1\n\tglobal_load_dwordx4 %1, %2, %3 offset:1024 sc1" W : "=&v"(fl0), "=&v"(fl1) : "v"(fo), "s"(ws) : "memory")
#define FLAGS_OK() __all(fl0[1] == tag && fl0[3] == tag && fl1[1] == tag && fl1[3] == tag)
  if (watcher) FLAGS_LOAD("");
  f32x4 acc[4];
  {
    const unsigned r0 = 256 * kh + 8 * g4 + (l15 >> 2);
    const unsigned A0 = sb + L_WQ + r0 * 128 + (((4 * kh + ((lane & 3) >> 1)) ^ wswz(r0)) << 4) + (lane & 1) * 8;
    const unsigned xe = xa0 + kh * 8192;
#define Q_TR(B, s, e, P, Q) "ds_read_b64_tr_b16 a[" #B "+4+4*" #e ":" #B "+5+4*" #e "], " P " offset:4096*" #s "\n\t" \
                            "ds_read_b64_tr_b16 a[" #B "+6+4*" #e ":" #B "+7+4*" #e "], " Q " offset:4096*" #s "+512\n\t"
#define Q_LD(B, s, X) "ds_read_b128 a[" #B ":" #B "+3], " X " offset:1024*(" #s "/2)\n\t" \
                      Q_TR(B, s, 0, "%4", "%6") Q_TR(B, s, 1, "%5", "%7") Q_TR(B, s, 2, "%6", "%4") Q_TR(B, s, 3, "%7", "%5")
#define Q_MM(B, N, C0, C1, C2, C3) "s_waitcnt lgkmcnt(" #N ")\n\tv_mfma_f32_16x16x32_bf16 %0, a[" #B "+4:" #B "+7], a[" #B ":" #B "+3], " C0 "\n\t" \
   "v_mfma_f32_16x16x32_bf16 %1, a[" #B "+8:" #B "+11], a[" #B ":" #B "+3], " C1 "\n\tv_mfma_f32_16x16x32_bf16 %2, a[" #B "+12:" #B "+15], a[" #B ":" #B "+3], " C2 "\n\t" \
   "v_mfma_f32_16x16x32_bf16 %3, a[" #B "+16:" #B "+19], a[" #B ":" #B "+3], " C3 "\n\t"
#define Q_MA(B, N) Q_MM(B, N, "%0", "%1", "%2", "%3")
    asm volatile(Q_LD(64, 0, "%8") Q_LD(84, 1, "%9") Q_MM(64, 9, "0", "0", "0", "0") Q_LD(104, 2, "%8") Q_MA(84, 9) Q_LD(64, 3, "%9") Q_MA(104, 9)
                 Q_LD(84, 4, "%8") Q_MA(64, 9) Q_LD(104, 5, "%9") Q_MA(84, 9) Q_LD(64, 6, "%8") Q_MA(104, 9) Q_LD(84, 7, "%9") Q_MA(64, 9) Q_MA(84, 0) "s_nop 7"
                 : "=&v"(acc[0]), "=&v"(acc[1]), "=&v"(acc[2]), "=&v"(acc[3])
                 : "v"(A0), "v"(A0 ^ 32), "v"(A0 ^ 64), "v"(A0 ^ 96), "v"(xe), "v"(xe ^ 64) : "memory", CLOB_A60_127);
  }
  STAMP(19);
  const unsigned xa = opaque(sb + L_R2 + lane * 16 + w * 2048);
  LDF4(xa) = acc[2];
  LDF4(xa + 1024) = acc[3];
  if (watcher) {
    asm volatile("s_waitcnt vmcnt(0)" : "+v"(fl0), "+v"(fl1) :: "memory");
    LD32(sb + L_M + M_RDY) = FLAGS_OK();
    LD32(sb + L_M + M_KMAX) = wave_umax(umax(fl0[0], fl0[2]));
  }
  BAR();
  STAMP(20);
  const bool ready = __builtin_amdgcn_readfirstlane(LD32(sb + L_M + M_RDY));
  const i32x4 rsKV = make_srd(ws + W_KV, KV_BYTES);
  const unsigned ko = lane * 16, vo = l31 <= 16 ? 1024 + h * 320 + l31 * 16 : 0x80000000u;
  const unsigned nt = kh ? 32 - N_SLOW : N_SLOW;
  const unsigned t0 = ((kh ? 0 : 64 - 2 * N_SLOW) + (w & 1) * nt + 2 * bi) & 63;
  unsigned tl = t0;
  if (ready && kh) tl = kv_first(tl, ko, vo, rsKV);
  {
    const f32x4 q0 = acc[0] + LDF4(xa ^ 8192), q1 = acc[1] + LDF4((xa ^ 8192) + 1024);
    const u32x2 o0 = {pkbf(q0[0], q0[1]), pkbf(q0[2], q0[3])}, o1 = {pkbf(q1[0], q1[1]), pkbf(q1[2], q1[3])};
    const unsigned qa = sb + L_Q + ((qq >> 1) * 4 + 2 * kh) * 1024 + (g4 & 1) * 512 + (16 * (qq & 1) + l15) * 16 + (g4 >> 1) * 8;
    LD64(qa) = o0; LD64(qa + 1024) = o1;
    unsigned n2 = row_umax(__float_as_uint(sum32(sum16(sq2(o0[0], sq2(o0[1], 0.f)), sq2(o1[0], sq2(o1[1], 0.f))))));
    if ((lane & 47) == 0) __hip_atomic_fetch_max((LAS unsigned*)(size_t)(sb + L_M + M_QMAX), n2, __ATOMIC_RELAXED, __HIP_MEMORY_SCOPE_WORKGROUP);
  }
  STAMP(5);
  if (!ready && watcher) {
    _Pragma("unroll 1") for (unsigned spins = 0; spins < kSpinCap; ++spins) {
      FLAGS_LOAD("\n\ts_waitcnt vmcnt(0)");
      if (FLAGS_OK()) break;
    }
    LD32(sb + L_M + M_KMAX) = wave_umax(umax(fl0[0], fl0[2]));
  }
  BAR();
  STAMP(6);
  if (!ready && kh) tl = kv_first(tl, ko, vo, rsKV);

  const u32x2 qk2 = LD64(sb + L_M + M_QMAX);
  const float bound = fmaf(__builtin_amdgcn_sqrtf(__uint_as_float(qk2[0]) * __uint_as_float(qk2[1])), 1.002f, 1e-3f);
  const unsigned exact = __builtin_amdgcn_readfirstlane(bound <= 32.0f ? 0u : 1u);
  const unsigned qaddr = sb + L_Q + (qq >> 1) * 4096 + lane * 16;
  if (exact) {
    const unsigned maddr = sb + L_MX + (qq >> 1) * 512 + l31 * 4;
    if (tid < 256) LDF(sb + L_MX + tid * 4) = -3.0e38f;
    BAR();
    float m[4] = {-3.0e38f, -3.0e38f, -3.0e38f, -3.0e38f};
    const __amdgpu_buffer_rsrc_t rs = __builtin_amdgcn_make_buffer_rsrc(ws + W_KV, 0, KV_BYTES, 0x00020000);
#pragma unroll 1
    for (unsigned i = 0; i < nt; ++i) {
      u32x4 kf = __builtin_amdgcn_raw_buffer_load_b128(rs, ko, ((t0 + i) & 63) * KV_TILE, 16);
#pragma unroll
      for (int t = 0; t < 4; ++t) {
        const u32x4 q = LD128(qaddr + t * 1024);
        f32x16 s_;
        asm volatile("s_nop 1\n\tv_mfma_f32_32x32x16_bf16 %0, %1, %2, 0\n\ts_nop 15" : "=&v"(s_) : "v"(kf), "v"(q) : CLOB_A60_127);
#pragma unroll
        for (int r = 0; r < 16; ++r) m[t] = fmaxf(m[t], s_[r]);
      }
    }
#pragma unroll
    for (int t = 0; t < 4; ++t) __builtin_amdgcn_ds_fmaxf((LAS float*)(size_t)(maddr + t * 128), m[t], __ATOMIC_RELAXED, __HIP_MEMORY_SCOPE_WORKGROUP, false);
    BAR();
    asm volatile("v_accvgpr_write_b32 a60, %0\n\tv_accvgpr_write_b32 a61, %1\n\tv_accvgpr_write_b32 a62, %2\n\tv_accvgpr_write_b32 a63, %3"
                 :: "v"(-LDF(maddr)), "v"(-LDF(maddr + 128)), "v"(-LDF(maddr + 256)), "v"(-LDF(maddr + 384)) : CLOB_A60_127);
  }
  STAMP(7);

  unsigned yv[8];
  if (kh == 0) {
    __builtin_amdgcn_s_setprio(2);
    {
      const unsigned c4 = (unsigned)(par * 64 + lane) * 4, tlo = sb + L_TLO + tch * TLO_CH + c4, thi = sb + L_THI + tch * THI_CH + c4;
#pragma unroll
      for (int i = 0; i < 32; ++i) LD32(tlo + i * 528) = T[i];
#pragma unroll
      for (int i = 0; i < 21; ++i) LD32(thi + i * 528) = T[32 + i];
    }
    unsigned ug[8];
    const unsigned go = (unsigned)(tid * 8192 + cp * 4);
#define GATHER4(k, base) asm volatile("global_load_dword %0, %4, %5\n\tglobal_load_dword %1, %4, %5 offset:1024\n\tglobal_load_dword %2, %4, %5 offset:2048\n\t" \
      "global_load_dword %3, %4, %5 offset:3072" : "=&v"(ug[k]), "=&v"(ug[k + 1]), "=&v"(ug[k + 2]), "=&v"(ug[k + 3]) : "v"(go), "s"(base) : "memory")
    post(sb + L_M + M_SW + w * 4, tag);
    GATHER4(0, u);
    wait4(sb + L_M + M_SW, tag);
    STAMP(8);
    const unsigned KT = sb + L_P;
    {
      const int it = w & 1;
      const unsigned aa = sb + L_TLO + tch * TLO_CH + (16 * it + l15) * 528 + g4 * 16;
      const unsigned b0 = sb + L_THI + tch * THI_CH + l15 * 528 + g4 * 16, b1 = sb + L_THI + tch * THI_CH + (l15 < 4 ? 16 + l15 : 20) * 528 + g4 * 16;
      const u32x2 sc = LD64(sb + L_M + M_SCALE + tch * 8);
      f32x4 k0 = {0, 0, 0, 0}, k1 = {0, 0, 0, 0};
      asm volatile("s_nop 1" : "+v"(k0), "+v"(k1));
#pragma unroll
      for (int ks = 0; ks < 8; ++ks) {
        const u32x4 fa = LD128(aa + ks * 64), fb = LD128(b0 + ks * 64), fc = LD128(b1 + ks * 64);
        asm volatile("v_mfma_f32_16x16x32_f16 %0, %2, %3, %0\n\tv_mfma_f32_16x16x32_f16 %1, %2, %4, %1" : "+v"(k0), "+v"(k1) : "v"(fa), "v"(fb), "v"(fc) : CLOB_A60_127);
      }
      asm volatile("s_nop 7" : "+v"(k0), "+v"(k1));
      const int lb = 128 * it + 32 * g4;
      const int l0 = lb + (l15 < 8 ? l15 : l15 < 12 ? 240 + 2 * l15 : 512 + 4 * (l15 & 1) + 128 * (l15 & 2)), l1 = lb + 1024 + 256 * (l15 < 4 ? l15 : 4);
      k0 *= __uint_as_float(sc[0]); k1 *= __uint_as_float(sc[0]);
      if (__builtin_amdgcn_readfirstlane(sc[1])) {
#pragma unroll
        for (int r = 0; r < 4; ++r) {
          k0[r] *= __builtin_amdgcn_exp2f(__uint_as_float(sc[1]) * (float)(l0 + 8 * r));
          k1[r] *= __builtin_amdgcn_exp2f(__uint_as_float(sc[1]) * (float)(l1 + 8 * r));
        }
      }
#define KWR(l, k) asm volatile("ds_write_b16 %0, %1\n\tds_write_b16_d16_hi %0, %1 offset:32\n\tds_write_b16 %0, %2 offset:64\n\tds_write_b16_d16_hi %0, %2 offset:96" \
                 :: "v"(KT + 16 + (l) * 4 + 2 * tch), "v"(pkbf(k[0], k[1])), "v"(pkbf(k[2], k[3])) : "memory")
      KWR(l0, k0); KWR(l1, k1);
      if (w == 0) LD128(KT) = (u32x4){0u, 0u, 0u, 0u};
      if (tid < 248) { const int c = tid >= 124, i = tid - 124 * c; LD128(sb + L_U + c * U_CH + (i >> 2) * 80 + (i & 3) * 16) = (u32x4){0u, 0u, 0u, 0u}; }
    }
    post(sb + L_M + M_SW + 16 + w * 4, tag);
    GATHER4(4, u + 2048);
    wait4(sb + L_M + M_SW + 16, tag);
    STAMP(9);
#pragma unroll
    for (int k = 0; k < 2; ++k) {
      const int id = tid + 256 * k;
      unsigned d[8];
      const float f0 = (float)(4 * id - 3);
#pragma unroll
      for (int i = 0; i < 4; ++i) {
        const unsigned r = pkbf(f0 + (float)(2 * i), f0 + (float)(2 * i + 1));
        d[2 * i] = LD32(KT + 16 + 4 * (int)bflo(r));
        if (i < 3) d[2 * i + 1] = LD32(KT + 16 + 4 * (int)bfhi(r));
      }
      const unsigned ka = sb + L_KC + 224 + id * 32;
#pragma unroll
      for (int c = 0; c < 2; ++c) {
        unsigned P[6];
#pragma unroll
        for (int i = 0; i < 6; ++i) P[i] = __builtin_amdgcn_perm(d[i], d[i + 1], c ? 0x07060302u : 0x05040100u);
        LD128(ka + c * KC_CH) = (u32x4){P[2], P[0], P[3], P[1]};
        LD128(ka + c * KC_CH + 16) = (u32x4){P[4], P[2], P[5], P[3]};
      }
    }
    if (tid < 28) LD128(sb + L_KC + (tid >= 14) * (KC_CH - 224) + tid * 16) = (u32x4){0u, 0u, 0u, 0u};
    STAMP(10);
    asm volatile("s_waitcnt vmcnt(0)" : "+v"(ug[0]), "+v"(ug[1]), "+v"(ug[2]), "+v"(ug[3]), "+v"(ug[4]), "+v"(ug[5]), "+v"(ug[6]), "+v"(ug[7]) :: "memory");
    {
      u32x4 c0, c1;
#pragma unroll
      for (int m = 0; m < 4; ++m) { c0[m] = __builtin_amdgcn_perm(ug[2 * m + 1], ug[2 * m], 0x05040100u); c1[m] = __builtin_amdgcn_perm(ug[2 * m + 1], ug[2 * m], 0x07060302u); }
      const unsigned ua = sb + L_U + (31 + (tid >> 2)) * 80 + (tid & 3) * 16;
      LD128(ua) = c0; LD128(ua + U_CH) = c1;
    }
    tl = kv_first(tl, ko, vo, rsKV);
    sync4(sb, w, 2, tag);
    STAMP(11);
    const int ch = w >> 1, pi = w & 1;
    unsigned aA = sb + L_KC + ch * KC_CH + (unsigned)(8 * (16 * (32 * pi - 1) + l31 - 8 * h + 24));
    unsigned aF = sb + L_U + ch * U_CH + (unsigned)(80 * (l31 + 31 - 16 * pi) + 16 * h);
    f32x16 y0, y1;
    unsigned cl;
    const u32x2 zz = {0u, 0u};
#define CLD(R0, R1, R2, R3, R4, R5, OA, OF) \
      "ds_read_b64 v[" #R0 "], %3 offset:" #OA "+32\n\tds_read_b64 v[" #R1 "], %3 offset:" #OA "\n\tds_read_b128 v[" #R4 "], %4 offset:" #OF "\n\t" \
      "ds_read_b128 v[" #R5 "], %4 offset:" #OF "+2560\n\tds_read_b64 v[" #R2 "], %3 offset:" #OA "+8224\n\tds_read_b64 v[" #R3 "], %3 offset:" #OA "+8192\n\t"
#define CMM(A0, A1, F0, F1) "s_waitcnt lgkmcnt(6)\n\tv_mfma_f32_32x32x16_bf16 %0, v[" #A0 "], v[" #F0 "], %0\n\t" \
      "v_mfma_f32_32x32x16_bf16 %1, v[" #A0 "], v[" #F1 "], %1\n\tv_mfma_f32_32x32x16_bf16 %1, v[" #A1 "], v[" #F0 "], %1\n\t"
    asm volatile(
        "s_setprio " STR(CONV_PRIO) "\n\tv_mfma_f32_32x32x8_bf16 %0, %5, %5, 0\n\tv_mfma_f32_32x32x8_bf16 %1, %5, %5, 0\n\ts_mov_b32 %2, 16\n\t"
        CLD(96:97, 98:99, 100:101, 102:103, 104:107, 108:111, 0, 32)
        "1:\n\t"
        CLD(112:113, 114:115, 116:117, 118:119, 120:123, 124:127, 128, 0)
        CMM(96:99, 100:103, 104:107, 108:111)
        "v_add_u32 %3, 0x100, %3\n\tv_add_u32 %4, 0xffffffb0, %4\n\t"
        CLD(96:97, 98:99, 100:101, 102:103, 104:107, 108:111, 0, 32)
        CMM(112:115, 116:119, 120:123, 124:127)
        "s_sub_u32 %2, %2, 1\n\ts_cmp_lg_u32 %2, 0\n\ts_cbranch_scc1 1b\n\t"
        "s_setprio 2\n\ts_waitcnt lgkmcnt(0)\n\ts_nop 15"
        : "=&v"(y0), "=&v"(y1), "=&s"(cl), "+v"(aA), "+v"(aF) : "v"(zz)
        : "memory", "scc", CLOB_V96_127, CLOB_A60_127);
    const unsigned pa = opaque(sb + L_P + lane * 16), pw = opaque(pa + w * 8192);
#pragma unroll
    for (int q = 0; q < 4; ++q) {
      LDF4(pw + q * 1024) = (f32x4){y0[4 * q], y0[4 * q + 1], y0[4 * q + 2], y0[4 * q + 3]};
      LDF4(pw + 4096 + q * 1024) = (f32x4){y1[4 * q], y1[4 * q + 1], y1[4 * q + 2], y1[4 * q + 3]};
    }
    STAMP(12);
    sync4(sb, w, 3, tag);
    gdma_rows16(wq0 + w * 131072, (wq0 ^ 64) + w * 131072 + 8192, wo, sb + L_WO + w * 16384);
    const float d0 = bflo(d2), d1 = bfhi(d2);
    const unsigned pr = opaque(pa + ch * 4096 + pi * 2048), uo = opaque(sb + L_U + (31 + 32 * ch + l31) * 80 + 32 * pi + 8 * h);
#pragma unroll
    for (int qi = 0; qi < 2; ++qi) {
      const unsigned o = pr + qi * 1024;
      const f32x4 ya = LDF4(o) + LDF4(o + 8192), yb = LDF4(o + 16384) + LDF4(o + 24576);
      const u32x2 ua = LD64(uo + 16 * qi), ub = LD64(uo + 16 * qi + U_CH);
#pragma unroll
      for (int e = 0; e < 4; ++e) {
        const float u0 = (e & 1) ? bfhi(ua[e >> 1]) : bflo(ua[e >> 1]), u1 = (e & 1) ? bfhi(ub[e >> 1]) : bflo(ub[e >> 1]);
        const unsigned yy = pkbf(ya[e], yb[e]), ud = pkbf(u0 * d0, u1 * d1);
        yv[4 * qi + e] = pkbf(bflo(yy) + bflo(ud), bfhi(yy) + bfhi(ud));
      }
    }
    post(sb + L_M + M_SW + 64 + w * 4, tag);
  } else {
    gdma1((64 * bi + 16 * qq + r8) * 1024 + xcd * 128 + c8 * 16, u, sb + L_DUMP);
    gdma1((64 * bi + 16 * qq + 8 + r8) * 1024 + xcd * 128 + c8 * 16, u, sb + L_DUMP);
#pragma unroll
    for (int i = 0; i < 8; ++i) asm volatile("" : "=a"(yv[i]));
  }
  STAMP(13);

  {
    f32x16 O[4];
    const i32x4 rsY = make_srd(out, 4194304);
    const unsigned yo = (1024 * (w >> 1) + 32 * l31 + 16 * (w & 1) + 4 * h) * 2048 + cp * 4;
    if (exact) attend<true>(O, tl, nt, kh ^ 1, qaddr, -bound, ko, vo, rsKV, yo, rsY, yv, sb + L_MX + w * 128 + 88);
    else attend<false>(O, tl, nt, kh ^ 1, qaddr, -bound, ko, vo, rsKV, yo, rsY, yv, sb + L_MX + w * 128 + 88);
    STAMP(21);
    if (kh == 0) wait4(sb + L_M + M_SW + 64, tag);
    const unsigned oa = opaque(sb + L_OA + (w ^ 4) * 4 * OA_SLOT + lane * 16), ol = opaque(sb + L_OA + (w ^ 4) * 4 * OA_SLOT + 2048 + lane * 4);
#pragma unroll
    for (int t = 0; t < 4; ++t) {
      LDF4(oa + t * OA_SLOT) = (f32x4){O[t][0], O[t][1], O[t][2], O[t][3]};
      LDF4(oa + t * OA_SLOT + 1024) = (f32x4){O[t][4], O[t][5], O[t][6], O[t][7]};
      LDF(ol + t * OA_SLOT) = O[t][8];
    }
  }
  asm volatile("s_waitcnt vmcnt(0)" ::: "memory");
  BAR();
  STAMP(14);

  int lane2 = lane; asm volatile("" : "+v"(lane2));
  {
    const int lane = lane2, g4 = lane >> 4, l15 = lane & 15;
    const __amdgpu_buffer_rsrc_t rsO = __builtin_amdgcn_make_buffer_rsrc(ws + W_OX, 0, 2097152, 0x00020000);
    {
      const unsigned s0 = opaque(sb + L_OA + (8 * kh + qq) * OA_SLOT + lane * 16), s1 = opaque(sb + L_OA + (8 * kh + qq) * OA_SLOT + 2048 + lane * 4);
      constexpr unsigned W1 = 4 * OA_SLOT, W4 = 16 * OA_SLOT;
      f32x4 o0 = (LDF4(s0) + LDF4(s0 + W1)) + (LDF4(s0 + W4) + LDF4(s0 + W4 + W1));
      f32x4 o1 = (LDF4(s0 + 1024) + LDF4(s0 + W1 + 1024)) + (LDF4(s0 + W4 + 1024) + LDF4(s0 + W4 + W1 + 1024));
      const float inv = __builtin_amdgcn_rcpf(sum32((LDF(s1) + LDF(s1 + W1)) + (LDF(s1 + W4) + LDF(s1 + W4 + W1))));
      o0 *= inv; o1 *= inv;
      u32x4 pk = {pkbf(o0[0], o0[1]), pkbf(o0[2], o0[3]), pkbf(o1[0], o1[1]), pkbf(o1[2], o1[3])};
      __builtin_amdgcn_raw_buffer_store_b128(pk, rsO, lane * 16 + ((2 * grp + kh) * 32 + 4 * hg + qq) * 1024, 0, 16);
    }
    const unsigned r0 = 256 * kh + 16 * (g4 >> 1) + 4 * (g4 & 1) + (l15 >> 2);
    const unsigned A0 = sb + L_WO + r0 * 128 + (((4 * kh + ((lane & 3) >> 1)) ^ wswz(r0)) << 4) + (lane & 1) * 8;
#define A_WF1(s, e, A, B) "ds_read_b64_tr_b16 a[16*" #s "+4*" #e ":16*" #s "+4*" #e "+1], " A " offset:4096*" #s "\n\t" \
                          "ds_read_b64_tr_b16 a[16*" #s "+4*" #e "+2:16*" #s "+4*" #e "+3], " B " offset:4096*" #s "+1024\n\t"
#define A_WF(s) A_WF1(s, 0, "%0", "%2") A_WF1(s, 1, "%1", "%3") A_WF1(s, 2, "%2", "%0") A_WF1(s, 3, "%3", "%1")
    asm volatile(A_WF(0) A_WF(1) A_WF(2) A_WF(3) "s_waitcnt vmcnt(0)\n\ts_barrier"
                 :: "v"(A0), "v"(A0 ^ 32), "v"(A0 ^ 64), "v"(A0 ^ 96) : "memory", CLOB_A_ALL);
    if (tid == 0) __hip_atomic_store((gu32*)(ws + W_F2) + grp * 32 + hg, tag, RLX_AGENT);
    STAMP(15);
    asm volatile(A_WF(4) A_WF(5) A_WF(6) A_WF(7) "s_waitcnt lgkmcnt(0)"
                 :: "v"(A0), "v"(A0 ^ 32), "v"(A0 ^ 64), "v"(A0 ^ 96) : "memory", CLOB_A_ALL);
    _Pragma("unroll 1") for (unsigned spins = 0; spins < kSpinCap; ++spins) {
      unsigned f = __hip_atomic_load((gu32*)(ws + W_F2) + grp * 32 + 4 * kh + (lane & 3), RLX_AGENT);
      if (__all(f == tag)) break;
    }
    __builtin_amdgcn_fence(__ATOMIC_ACQUIRE, "wavefront");
    STAMP(16);
    const int oo = (g4 >> 1) * 1024 + ((g4 & 1) * 32 + 16 * (qq & 1) + l15) * 16, os = ((2 * grp + (qq >> 1)) * 32 + 16 * kh) * 1024;
    u32x4 of[8];
#pragma unroll
    for (int s = 0; s < 8; ++s) of[s] = __builtin_amdgcn_raw_buffer_load_b128(rsO, oo + (s & 1) * 2048, os + (s >> 1) * 4096, 16);
    __builtin_amdgcn_sched_barrier(0);
    f32x4 acc[4];
    asm volatile("v_mfma_f32_16x16x32_bf16 %0, a[0:3], %4, 0\n\tv_mfma_f32_16x16x32_bf16 %1, a[4:7], %4, 0\n\t"
                 "v_mfma_f32_16x16x32_bf16 %2, a[8:11], %4, 0\n\tv_mfma_f32_16x16x32_bf16 %3, a[12:15], %4, 0"
                 : "=&v"(acc[0]), "=&v"(acc[1]), "=&v"(acc[2]), "=&v"(acc[3]) : "v"(of[0]) : CLOB_A_ALL);
#define OPJ(s, T) asm volatile("v_mfma_f32_16x16x32_bf16 %0, " AR4(16 * s) ", %4, %0\n\tv_mfma_f32_16x16x32_bf16 %1, " AR4(16 * s + 4) ", %4, %1\n\t" \
                 "v_mfma_f32_16x16x32_bf16 %2, " AR4(16 * s + 8) ", %4, %2\n\tv_mfma_f32_16x16x32_bf16 %3, " AR4(16 * s + 12) ", %4, %3" T \
                 : "+v"(acc[0]), "+v"(acc[1]), "+v"(acc[2]), "+v"(acc[3]) : "v"(of[s]) : CLOB_A_ALL)
    OPJ(1, ""); OPJ(2, ""); OPJ(3, ""); OPJ(4, ""); OPJ(5, ""); OPJ(6, ""); OPJ(7, "\n\ts_nop 7");
    STAMP(17);
    const unsigned xa = opaque(sb + L_U + lane * 16 + w * 2048), xb = opaque(sb + L_U + lane * 16 + (w ^ 4) * 2048);
    LDF4(xa) = acc[2];
    LDF4(xa + 1024) = acc[3];
    BAR();
    const f32x4 q0 = acc[0] + LDF4(xb), q1 = acc[1] + LDF4(xb + 1024);
    auto sa = __builtin_amdgcn_permlane16_swap(pkbf(q0[0], q0[1]), pkbf(q1[0], q1[1]), false, false);
    auto sc = __builtin_amdgcn_permlane16_swap(pkbf(q0[2], q0[3]), pkbf(q1[2], q1[3]), false, false);
    u32x4 v = {sa[0], sc[0], sa[1], sc[1]};
    *(u32x4*)(out + (64 * grp + 16 * qq + l15) * 1024 + 512 + 64 * hg + 32 * kh + 16 * (g4 & 1) + 8 * (g4 >> 1)) = v;
    STAMP(18);
    STAMP_OUT();
    if (b == 0 && w == 0) {
      _Pragma("unroll 1") for (unsigned spins = 0; spins < kSpinCap; ++spins) {
        bool ok = true;
#pragma unroll
        for (int k = 0; k < 4; ++k) {
          const int i = lane + 64 * k;
          ok &= __hip_atomic_load((gu32*)(ws + W_F2) + (i >> 3) * 32 + (i & 7), RLX_AGENT) == tag;
        }
        if (__all(ok)) break;
        __builtin_amdgcn_s_sleep(1);
      }
      if (lane == 0) __hip_atomic_store((gu32*)(ws + W_E), tag, RLX_AGENT);
    }
  }
}

extern "C" void kernel_launch(void* const* d_in, const int* in_sizes, int n_in, void* d_out, int out_size, void* d_ws,
                              size_t ws_size, hipStream_t stream) {
  typedef const unsigned short* P;
  fused_kernel<<<256, 512, 0, stream>>>((P)d_in[0], (P)d_in[1], (P)d_in[2], (P)d_in[3], (P)d_in[4], (P)d_in[5], (P)d_in[6],
                                        (const unsigned*)d_in[7], (const unsigned*)d_in[8], (P)d_in[9], (P)d_in[10], (P)d_in[11],
                                        (P)d_in[12], (P)d_in[13], (unsigned short*)d_out, (unsigned char*)d_ws);
}
```

```cpp
#include <hip/hip_runtime.h>
#include <hip/hip_bf16.h>
#include <stdint.h>

typedef float f32x16 __attribute__((ext_vector_type(16)));
typedef float f32x8 __attribute__((ext_vector_type(8)));
typedef float f32x4 __attribute__((ext_vector_type(4)));
typedef float f32x2 __attribute__((ext_vector_type(2)));
typedef __bf16 bf16x8 __attribute__((ext_vector_type(8)));
typedef __bf16 bf16x2 __attribute__((ext_vector_type(2)));
typedef _Float16 f16x8 __attribute__((ext_vector_type(8)));
typedef _Float16 f16x2 __attribute__((ext_vector_type(2)));
typedef short s16x4 __attribute__((ext_vector_type(4)));
typedef unsigned u32x4 __attribute__((ext_vector_type(4)));
typedef unsigned u32x2 __attribute__((ext_vector_type(2)));
#define LAS __attribute__((address_space(3)))
#define GAS __attribute__((address_space(1)))
typedef GAS unsigned gu32;
typedef GAS unsigned long long gu64;
#define RLX_AGENT __ATOMIC_RELAXED, __HIP_MEMORY_SCOPE_AGENT

constexpr unsigned L_X = 0;
constexpr unsigned L_TLO = 0, TLO_CH = 16896, L_THI = 33792, THI_CH = 11088;
constexpr unsigned L_KC = 0, KC_CH = 16640;
constexpr unsigned L_WO = 0;
constexpr unsigned L_B = 65536;
constexpr unsigned L_WQ = L_B;
constexpr unsigned L_R2 = L_B + 65536;
constexpr unsigned L_DUMP = L_B;
constexpr unsigned L_OA = L_B, OA_SLOT = 2304;
constexpr unsigned L_P = L_B + 40960;
constexpr unsigned L_U = L_B + 73728, U_CH = 7600;
constexpr unsigned L_Q = L_B + 88928;
constexpr unsigned L_MX = L_Q + 8192;
constexpr unsigned L_M = L_MX + 1024;
constexpr unsigned LDS_BYTES = L_M + 128;
static_assert(LDS_BYTES <= 163840, "lds");
constexpr unsigned M_TAG = 0, M_RDY = 4, M_QMAX = 8, M_KMAX = 12, M_SCALE = 16  , M_SW = 32  ;
#define CONV_PRIO 0
#define N_SLOW 8
static_assert(N_SLOW >= 4 && N_SLOW <= 28 && N_SLOW % 2 == 0, "the convolution's stores ride on the first four key tiles");

constexpr unsigned KV_TILE = 2304, KV_BYTES = 64 * KV_TILE;
constexpr unsigned W_E = 0, W_F1 = 2048, W_F2 = 4096, W_KV = 8192, W_OX = W_KV + KV_BYTES, W_END = W_OX + 2097152;
constexpr unsigned W_DBG = W_END;

constexpr int CODE_LINES = 208;
constexpr unsigned kSpinCap = 1u << 20;

#define LD32(a) (*(LAS unsigned*)(size_t)(a))
#define LDF(a) (*(LAS float*)(size_t)(a))
#define LD64(a) (*(LAS u32x2*)(size_t)(a))
#define LD128(a) (*(LAS u32x4*)(size_t)(a))
#define LDF4(a) (*(LAS f32x4*)(size_t)(a))
#define BAR() asm volatile("s_waitcnt lgkmcnt(0)\n\ts_barrier" ::: "memory")

#define STAMP(i)
#define STAMP_INIT()
#define STAMP_OUT()

__device__ __forceinline__ float bflo(unsigned v) { return __uint_as_float(v << 16); }
__device__ __forceinline__ float bfhi(unsigned v) { return __uint_as_float(v & 0xffff0000u); }
__device__ __forceinline__ unsigned pkbf(float a, float b) { f32x2 x = {a, b}; return __builtin_bit_cast(unsigned, __builtin_convertvector(x, bf16x2)); }
__device__ __forceinline__ unsigned pkh(float a, float b) { f32x2 x = {a, b}; return __builtin_bit_cast(unsigned, __builtin_convertvector(x, f16x2)); }
template <int CTRL> __device__ __forceinline__ unsigned dppu(unsigned v) { return __builtin_amdgcn_update_dpp(0u, v, CTRL, 0xf, 0xf, true); }
__device__ __forceinline__ unsigned umax(unsigned a, unsigned b) { return a > b ? a : b; }
__device__ __forceinline__ unsigned row_umax(unsigned v) {
  v = umax(v, dppu<0x128>(v)); v = umax(v, dppu<0x124>(v)); v = umax(v, dppu<0x122>(v)); return umax(v, dppu<0x121>(v));
}
__device__ __forceinline__ unsigned wave_umax(unsigned v) {
  v = row_umax(v);
  auto a = __builtin_amdgcn_permlane16_swap(v, v, false, false);
  v = umax(a[0], a[1]);
  auto c = __builtin_amdgcn_permlane32_swap(v, v, false, false);
  return umax(c[0], c[1]);
}
__device__ __forceinline__ float sum16(float a, float b) {
  auto r = __builtin_amdgcn_permlane16_swap(__float_as_uint(a), __float_as_uint(b), false, false);
  return __uint_as_float(r[0]) + __uint_as_float(r[1]);
}
__device__ __forceinline__ float sum32(float v) {
  auto c = __builtin_amdgcn_permlane32_swap(__float_as_uint(v), __float_as_uint(v), false, false);
  return __uint_as_float(c[0]) + __uint_as_float(c[1]);
}
__device__ __forceinline__ float sq2(unsigned v, float c) {
  return __builtin_amdgcn_fdot2_f32_bf16(__builtin_bit_cast(bf16x2, v), __builtin_bit_cast(bf16x2, v), c, false);
}
__device__ __forceinline__ unsigned opaque(unsigned v) { asm volatile("" : "+v"(v)); return v; }
struct cf { float x, y; };
__device__ __forceinline__ cf cmul(cf a, cf b) { return {a.x * b.x - a.y * b.y, a.x * b.y + a.y * b.x}; }
__device__ __forceinline__ cf cexp_(float re, float im) {
  float m = __builtin_amdgcn_exp2f(re * 1.44269504088896f);
  float f = __builtin_amdgcn_fractf(im * 0.159154943091895f);
  return {m * __builtin_amdgcn_cosf(f), m * __builtin_amdgcn_sinf(f)};
}
__device__ __forceinline__ u32x4 trfrag(unsigned a0, unsigned a1) {
  s16x4 lo = __builtin_amdgcn_ds_read_tr16_b64_v4i16((LAS s16x4*)(size_t)a0);
  s16x4 hi = __builtin_amdgcn_ds_read_tr16_b64_v4i16((LAS s16x4*)(size_t)a1);
  u32x2 l = __builtin_bit_cast(u32x2, lo), h = __builtin_bit_cast(u32x2, hi);
  return (u32x4){l[0], l[1], h[0], h[1]};
}
__device__ __forceinline__ f32x16 mfma32(u32x4 a, u32x4 b, f32x16 c) {
  return __builtin_amdgcn_mfma_f32_32x32x16_bf16(__builtin_bit_cast(bf16x8, a), __builtin_bit_cast(bf16x8, b), c, 0, 0, 0);
}
__device__ __forceinline__ f32x4 mfma16(u32x4 a, u32x4 b, f32x4 c) {
  return __builtin_amdgcn_mfma_f32_16x16x32_bf16(__builtin_bit_cast(bf16x8, a), __builtin_bit_cast(bf16x8, b), c, 0, 0, 0);
}
#define GLD1(dst, voff, base) asm volatile("global_load_dword %0, %1, %2" : "=v"(dst) : "v"(voff), "s"(base))
#define GLD4(dst, voff, base, imm) asm volatile("global_load_dwordx4 %0, %1, %2 offset:%3" : "=v"(dst) : "v"(voff), "s"(base), "i"(imm))
__device__ __forceinline__ void gdma4(unsigned voff, const void* base, unsigned lds_dst) {
  unsigned keep;
  asm volatile("s_mov_b32 %0, m0\n\ts_mov_b32 m0, %3\n\ts_nop 0\n\tglobal_load_lds_dwordx4 %1, %2\n\tglobal_load_lds_dwordx4 %1, %2 offset:1024\n\t"
               "global_load_lds_dwordx4 %1, %2 offset:2048\n\tglobal_load_lds_dwordx4 %1, %2 offset:3072\n\ts_mov_b32 m0, %0"
               : "=&s"(keep) : "v"(voff), "s"(base), "s"(lds_dst) : "memory");
}
#define DMA_PAIR "global_load_lds_dwordx4 %2, %1\n\ts_add_u32 m0, m0, 0x400\n\ts_nop 0\n\tglobal_load_lds_dwordx4 %3, %1\n\ts_add_u32 m0, m0, 0x400\n\tv_add_u32 %2, 0x4000, %2\n\tv_add_u32 %3, 0x4000, %3\n\t"
__device__ __forceinline__ void gdma_rows16(unsigned v0, unsigned v1, const void* base, unsigned lds_dst) {
  unsigned keep;
  asm volatile("s_mov_b32 %0, m0\n\ts_mov_b32 m0, %4\n\ts_nop 0\n\t" DMA_PAIR DMA_PAIR DMA_PAIR DMA_PAIR DMA_PAIR DMA_PAIR DMA_PAIR DMA_PAIR "s_mov_b32 m0, %0"
               : "=&s"(keep), "+s"(base), "+v"(v0), "+v"(v1) : "s"(lds_dst) : "memory", "scc");
}
#define DMA_X(v, o) "global_load_lds_dwordx4 " v ", %1 offset:" #o "\n\ts_add_u32 m0, m0, "
__device__ __forceinline__ void gdma_x(unsigned v0, unsigned v1, const void* base, unsigned lds_dst) {
  unsigned keep;
  asm volatile("s_mov_b32 %0, m0\n\ts_mov_b32 m0, %4\n\ts_nop 0\n\t"
               DMA_X("%2", 0) "896\n\ts_nop 0\n\t" DMA_X("%2", 128) "896\n\ts_nop 0\n\t" DMA_X("%2", 256) "896\n\ts_nop 0\n\t" DMA_X("%2", 384) "1408\n\ts_nop 0\n\t"
               DMA_X("%3", 0) "896\n\ts_nop 0\n\t" DMA_X("%3", 128) "896\n\ts_nop 0\n\t" DMA_X("%3", 256) "896\n\ts_nop 0\n\t" DMA_X("%3", 384) "0\n\t"
               "s_mov_b32 m0, %0"
               : "=&s"(keep) : "s"(base), "v"(v0), "v"(v1), "s"(lds_dst) : "memory", "scc");
}
__device__ __forceinline__ void gdma_rows4(unsigned v0, unsigned v1, const void* base, unsigned lds_dst) {
  unsigned keep;
  asm volatile("s_mov_b32 %0, m0\n\ts_mov_b32 m0, %4\n\ts_nop 0\n\t" DMA_PAIR DMA_PAIR "s_mov_b32 m0, %0"
               : "=&s"(keep), "+s"(base), "+v"(v0), "+v"(v1) : "s"(lds_dst) : "memory", "scc");
}
__device__ __forceinline__ void gdma1(unsigned voff, const void* base, unsigned lds_dst) {
  unsigned keep;
  asm volatile("s_mov_b32 %0, m0\n\ts_mov_b32 m0, %3\n\ts_nop 0\n\tglobal_load_lds_dwordx4 %1, %2\n\ts_mov_b32 m0, %0"
               : "=&s"(keep) : "v"(voff), "s"(base), "s"(lds_dst) : "memory");
}
__device__ __forceinline__ unsigned wswz(unsigned row) { return ((((row >> 1) & 3u) ^ (((row >> 3) & 1u) << 1)) << 1); }

__device__ __forceinline__ void wait4(unsigned addr, unsigned want) {
  _Pragma("unroll 1") for (unsigned spins = 0; spins < kSpinCap; ++spins) {
    u32x4 v;
    asm volatile("ds_read_b128 %0, %1\n\ts_waitcnt lgkmcnt(0)" : "=v"(v) : "v"(addr) : "memory");
    if (__all(v[0] == want && v[1] == want && v[2] == want && v[3] == want)) break;
    __builtin_amdgcn_s_sleep(1);
  }
}
__device__ __forceinline__ void post(unsigned addr, unsigned val) {
  asm volatile("ds_write_b32 %0, %1" :: "v"(addr), "v"(val) : "memory");
}

__device__ __forceinline__ void sync4(unsigned sb, int w, int k, unsigned tag) {
  post(sb + L_M + M_SW + k * 16 + w * 4, tag);
  wait4(sb + L_M + M_SW + k * 16, tag);
}

#define STR_(x) #x
#define STR(x) STR_(x)
#define VR(b, o) "v[" STR(b) "+" STR(o) "]"
#define VRR(b, o, n) "v[" STR(b) "+" STR(o) ":" STR(b) "+" STR(o) "+" STR(n) "-1]"
#define AR4(i) "a[" STR(i) ":" STR(i) "+3]"
#define A_EX(S, i) "v_exp_f32 " VR(S, i) ", " VR(S, i) "\n\t"
#define A_QK(S, r, q) "v_mfma_f32_32x32x16_bf16 " VRR(S, 0, 16) ", " AR4(80 + 12 * r) ", " AR4(64 + 4 * q) ", v[80:95]\n\t"
#define A_PV0(O, r, S) "v_smfmac_f32_32x32x32_bf16 " O ", " AR4(84 + 12 * r) ", " VRR(S, 0, 8) ", v79\n\t"
#define A_PV1(O, r, S) "v_smfmac_f32_32x32x32_bf16 " O ", " AR4(88 + 12 * r) ", " VRR(S, 8, 8) ", v79\n\t"
#define A_RD4(o, q) "v_accvgpr_read_b32 " VR(80, o) ", a[60+" #q "]\n\tv_accvgpr_read_b32 " VR(81, o) ", a[60+" #q "]\n\t" \
                    "v_accvgpr_read_b32 " VR(82, o) ", a[60+" #q "]\n\tv_accvgpr_read_b32 " VR(83, o) ", a[60+" #q "]\n\t"
#define A_SHIFT_EXACT(q) A_RD4(0, q) A_RD4(4, q) A_RD4(8, q) A_RD4(12, q) "s_nop 1\n\t"
#define A_SHIFT_NONE(q) ""
#define A_BLOCK(Sc, Sp, Op, rp, rn, qn, Oc, rc, SH, XM, X2, X3, X4, X5, X6, X7, X8, X9) \
  A_EX(Sc, 0) A_EX(Sc, 1) A_PV1(Op, rp, Sp) XM A_EX(Sc, 2) X2 A_EX(Sc, 3) X3 A_EX(Sc, 4) X4 A_EX(Sc, 5) X5 SH(qn) A_QK(Sp, rn, qn) \
  A_EX(Sc, 6) X6 A_EX(Sc, 7) X7 A_EX(Sc, 8) X8 A_EX(Sc, 9) X9 A_EX(Sc, 10) A_PV0(Oc, rc, Sc) \
  A_EX(Sc, 11) A_EX(Sc, 12) A_EX(Sc, 13) A_EX(Sc, 14) A_EX(Sc, 15)
#define A_KV0 "s_mul_i32 %[so], %[tl], 2304\n\t"
#define A_KV1 "s_add_u32 %[tl], %[tl], 1\n\t"
#define A_KV2 "s_and_b32 %[tl], %[tl], 63\n\t"
#define A_KV3(r) "buffer_load_dwordx4 " AR4(80 + 12 * r) ", %[ko], %[rk], %[so] offen sc1\n\t"
#define A_KV4(r) "buffer_load_dwordx4 " AR4(84 + 12 * r) ", %[vo], %[rk], %[so] offen sc1\n\t"
#define A_KV5(r) "buffer_load_dwordx4 " AR4(88 + 12 * r) ", %[vo], %[rk], %[so] offen offset:640 sc1\n\t"
#define A_KV(r) A_KV0 A_KV1 A_KV2 A_KV3(r) A_KV4(r) A_KV5(r)
#define A_STEP(r, rp, rn, SH, XM, Y0, Y1, Y2, W, E0, E1) \
  A_BLOCK(96, 112, "%[o3]", rp, r, 1, "%[o0]", r, SH, XM, A_KV0, A_KV1, A_KV2, "", A_KV3(rp), A_KV4(rp), A_KV5(rp), "") \
  A_BLOCK(112, 96, "%[o0]", r, r, 2, "%[o1]", r, SH, "", Y0, Y1, "", "", "", "", "", "") \
  A_BLOCK(96, 112, "%[o1]", r, r, 3, "%[o2]", r, SH, "", "", Y2, "", "", "", "", "", "") \
  A_BLOCK(112, 96, "%[o2]", r, rn, 0, "%[o3]", r, SH, "", "", "", "", "s_waitcnt vmcnt(" #W ")\n\t", E0, E1, "", "")
#define A_YSO(so) "s_movk_i32 %[so], " #so "\n\t"
#define A_YA(y) "buffer_store_dword " y ", %[yo], %[ry], %[so] offen\n\t"
#define A_YB(y) "buffer_store_dword " y ", %[yo], %[ry], %[so] offen offset:2048\n\t"
#define A_CNT0 "s_sub_u32 %[n], %[n], 2\n\t"
#define A_CNT1 "s_cmp_eq_u32 %[n], 0\n\t"
#define A_EXIT(r) A_PV1("%[o3]", r, 112)
#define A_MOV4(o) "v_mov_b32 " VR(80, o) ", %[nb]\n\tv_mov_b32 " VR(81, o) ", %[nb]\n\tv_mov_b32 " VR(82, o) ", %[nb]\n\tv_mov_b32 " VR(83, o) ", %[nb]\n\t"
#define A_ZERO(O) "v_mfma_f32_32x32x8_bf16 " O ", v[78:79], v[78:79], 0\n\t"
#define ITSTAMP
#define ITOPS
#define ITCLOB
#define A_ATT(SH) \
  "ds_read_b128 a[64:67], %[qa]\n\tds_read_b128 a[68:71], %[qa] offset:1024\n\tds_read_b128 a[72:75], %[qa] offset:2048\n\tds_read_b128 a[76:79], %[qa] offset:3072\n\t" \
  "v_mov_b32 v78, 0\n\tv_mov_b32 v79, 0\n\t" A_MOV4(0) A_MOV4(4) A_ZERO("%[o0]") A_MOV4(8) A_MOV4(12) A_ZERO("%[o1]") A_ZERO("%[o2]") A_ZERO("%[o3]") \
  "s_waitcnt vmcnt(6) lgkmcnt(0)\n\t" SH(0) A_QK(96, 0, 0) "v_mov_b32 v79, 0xdddd\n\ts_setprio 1\n\ts_nop 13\n\t" \
  "s_cmp_eq_u32 %[fl], 0\n\t" A_EX(96, 0) A_EX(96, 1) "s_cbranch_scc1 11f\n\ts_branch 12f\n\t" \
  A_STEP(0, 3, 1, SH, "12:\n\t", A_YSO(0), A_YA("%[y0]"), A_YB("%[y1]"), 8, "", "") \
  A_STEP(1, 0, 2, SH, "", A_YSO(0x1000), A_YA("%[y2]"), A_YB("%[y3]"), 10, "", "") \
  A_STEP(2, 1, 3, SH, "", A_YSO(0x4000), A_YA("%[y4]"), A_YB("%[y5]"), 12, A_CNT0, "") \
  A_STEP(3, 2, 0, SH, "", A_YSO(0x5000), A_YA("%[y6]"), A_YB("%[y7]"), 12, A_CNT0, A_CNT1) ITSTAMP "s_cbranch_scc1 43f\n\t" \
  "10:\n\t" \
  A_STEP(0, 3, 1, SH, "11:\n\t", "", "", "", 6, "", "") \
  A_STEP(1, 0, 2, SH, "", "", "", "", 6, A_CNT0, A_CNT1) "s_cbranch_scc1 41f\n\t" \
  A_STEP(2, 1, 3, SH, "", "", "", "", 6, "", "") \
  A_STEP(3, 2, 0, SH, "", "", "", "", 6, A_CNT0, A_CNT1) ITSTAMP "s_cbranch_scc0 10b\n\t" \
  "43:\n\ts_nop 0\n\t" A_EXIT(3) "s_branch 99f\n\t41:\n\ts_nop 0\n\t" A_EXIT(1) \
  "99:\n\ts_setprio 0\n\ts_nop 15"
#define C8(p, a, b, c, d, e, f, g, h) p #a, p #b, p #c, p #d, p #e, p #f, p #g, p #h
#define CLOB_V78_127 "v78", "v79", C8("v", 80, 81, 82, 83, 84, 85, 86, 87), C8("v", 88, 89, 90, 91, 92, 93, 94, 95), CLOB_V96_127
#define CLOB_V96_127 C8("v", 96, 97, 98, 99, 100, 101, 102, 103), C8("v", 104, 105, 106, 107, 108, 109, 110, 111), \
  C8("v", 112, 113, 114, 115, 116, 117, 118, 119), C8("v", 120, 121, 122, 123, 124, 125, 126, 127)
#define CLOB_V112_127 C8("v", 112, 113, 114, 115, 116, 117, 118, 119), C8("v", 120, 121, 122, 123, 124, 125, 126, 127)
#define CLOB_A0_31 C8("a", 0, 1, 2, 3, 4, 5, 6, 7), C8("a", 8, 9, 10, 11, 12, 13, 14, 15), C8("a", 16, 17, 18, 19, 20, 21, 22, 23), C8("a", 24, 25, 26, 27, 28, 29, 30, 31)
#define CLOB_A32_59 C8("a", 32, 33, 34, 35, 36, 37, 38, 39), C8("a", 40, 41, 42, 43, 44, 45, 46, 47), C8("a", 48, 49, 50, 51, 52, 53, 54, 55), "a56", "a57", "a58", "a59"
#define CLOB_A60_127 "a60", "a61", "a62", "a63", C8("a", 64, 65, 66, 67, 68, 69, 70, 71), C8("a", 72, 73, 74, 75, 76, 77, 78, 79), \
  C8("a", 80, 81, 82, 83, 84, 85, 86, 87), C8("a", 88, 89, 90, 91, 92, 93, 94, 95), \
  C8("a", 96, 97, 98, 99, 100, 101, 102, 103), C8("a", 104, 105, 106, 107, 108, 109, 110, 111), \
  C8("a", 112, 113, 114, 115, 116, 117, 118, 119), C8("a", 120, 121, 122, 123, 124, 125, 126, 127)
#define CLOB_A_ALL CLOB_A0_31, CLOB_A32_59, CLOB_A60_127
typedef int i32x4 __attribute__((ext_vector_type(4)));
__device__ __forceinline__ i32x4 make_srd(const void* p, unsigned bytes) {
  const unsigned long long a = (unsigned long long)p;
  return (i32x4){(int)(unsigned)a, (int)((unsigned)(a >> 32) & 0xffffu), (int)bytes, 0x00020000};
}

__device__ __forceinline__ unsigned kv_first(unsigned tl, unsigned ko, unsigned vo, i32x4 rk) {
  unsigned so;
  asm volatile(A_KV(0) A_KV(1) A_KV(2) : [tl] "+s"(tl), [so] "=&s"(so) : [ko] "v"(ko), [vo] "v"(vo), [rk] "s"(rk) : "memory", "scc", CLOB_A60_127);
  return tl;
}
template <bool EXACT>
__device__ __forceinline__ void attend(f32x16 (&O)[4], unsigned tl, unsigned n, unsigned fl, unsigned qa, float nb, unsigned ko, unsigned vo, i32x4 rk,
                                       unsigned yo, i32x4 ry, const unsigned (&y)[8], unsigned sa) {
  unsigned so;
#define ATT_OPS : [o0] "=&v"(O[0]), [o1] "=&v"(O[1]), [o2] "=&v"(O[2]), [o3] "=&v"(O[3]), [tl] "+s"(tl), [n] "+s"(n), [so] "=&s"(so) ITOPS \
    : [fl] "s"(fl), [qa] "v"(qa), [nb] "v"(nb), [ko] "v"(ko), [vo] "v"(vo), [rk] "s"(rk), [yo] "v"(yo), [ry] "s"(ry), [y0] "a"(y[0]), [y1] "a"(y[1]), [y2] "a"(y[2]), \
      [y3] "a"(y[3]), [y4] "a"(y[4]), [y5] "a"(y[5]), [y6] "a"(y[6]), [y7] "a"(y[7]) : "memory", "scc", CLOB_V78_127, CLOB_A60_127 ITCLOB
  if (EXACT) asm volatile(A_ATT(A_SHIFT_EXACT) ATT_OPS);
  else asm volatile(A_ATT(A_SHIFT_NONE) ATT_OPS);
}

__global__ __launch_bounds__(512, 2) void fused_kernel(
    const unsigned short* __restrict__ u, const unsigned short* __restrict__ Are, const unsigned short* __restrict__ Aim,
    const unsigned short* __restrict__ Bre, const unsigned short* __restrict__ Bim, const unsigned short* __restrict__ Cre,
    const unsigned short* __restrict__ Cim, const unsigned* __restrict__ logdt2, const unsigned* __restrict__ dskip2,
    const unsigned short* __restrict__ x, const unsigned short* __restrict__ wq, const unsigned short* __restrict__ wk,
    const unsigned short* __restrict__ wv, const unsigned short* __restrict__ wo, unsigned short* __restrict__ out,
    unsigned char* __restrict__ ws) {
  __shared__ __attribute__((aligned(128))) unsigned char smem[LDS_BYTES];
  unsigned long long pc;
  asm volatile("s_getpc_b64 %0" : "=s"(pc));
  const unsigned sb = (unsigned)(size_t)(LAS unsigned char*)smem;
  const int tid = threadIdx.x, lane = tid & 63;
  const int w = __builtin_amdgcn_readfirstlane(tid >> 6);
  const int b = blockIdx.x, xcd = b & 7, bi = b >> 3;
  const int grp = xcd * 4 + (bi >> 3), hg = bi & 7, cp = xcd * 32 + bi;
  const int h = lane >> 5, g4 = lane >> 4, l15 = lane & 15, l31 = lane & 31;
  const int qq = w & 3, kh = w >> 2;
  const int chain = hg >> 2;
  const int vl = (qq - hg - 1) & 3;
  STAMP_INIT();
  asm volatile("" :: "s"(u), "s"(Are), "s"(Aim), "s"(Bre), "s"(Bim), "s"(Cre), "s"(Cim), "s"(logdt2), "s"(dskip2), "s"(x), "s"(wq),
               "s"(wk), "s"(wv), "s"(wo), "s"(out), "s"(ws));
  const unsigned r8 = lane >> 3, c8 = lane & 7;
  const unsigned wq0 = r8 * 1024 + hg * 128 + ((c8 ^ ((r8 & 2) << 1)) << 4);
  const int tch = (w >> 1) & 1, par = w & 1;
  unsigned T[53], d2;

  if (kh) {
    const unsigned xo = (64 * grp + 16 * qq + r8) * 1024 + ((c8 ^ (r8 >> 1)) << 4);
    if (vl != 3) asm volatile("s_barrier" ::: "memory");
    else __builtin_amdgcn_s_setprio(3);
    gdma_x(xo, (xo ^ 64) + 8192, x, sb + L_X + qq * 16384);
    gdma_x(xo + 512, (xo ^ 64) + 8704, x, sb + L_X + qq * 16384 + 8192);
    if (vl != 3) {
      gdma_rows16(wq0 + vl * 131072, wq0 + vl * 131072 + 8192, wq, sb + L_WQ + vl * 16384);
      asm volatile("s_waitcnt vmcnt(0)" ::: "memory");
    } else {
      const unsigned short* wkv = chain ? wv : wk;
#pragma unroll
      for (int i = 0; i < 4; ++i) gdma4((unsigned)(i * 4096 + lane * 16), wkv, sb + L_R2 + i * 4096);
      asm volatile("s_barrier" ::: "memory");
      unsigned E;
      asm volatile("s_load_dword %0, %1, 0x0\n\ts_waitcnt lgkmcnt(0)" : "=s"(E) : "s"(ws) : "memory");
      const unsigned tg = E + (((E * 0x9E3779B1u ^ (E >> 15) ^ 0x5bd1e995u) & 0x7fffffffu) | 0x101u);
      LD32(sb + L_M + M_TAG) = tg;
      const unsigned wa = sb + L_R2 + (8 * g4 + (l15 >> 2)) * 32 + (lane & 3) * 8;
      const unsigned xe = sb + L_X + qq * 16384 + (l15 >> 3) * 4096 + (l15 & 7) * 128 + ((g4 ^ (l15 >> 1)) << 4);
      const unsigned off = (2 * grp + (qq >> 1)) * KV_TILE + l15 * 16 +
                           (chain ? 1024 + (qq & 1) * 640 + (g4 >> 1) * 320 + (g4 & 1) * 8 : (qq & 1) * 256 + (g4 & 1) * 512 + (g4 >> 1) * 8);
      f32x4 acc, acd;
#define K_LD(s, X) "ds_read_b128 a[8*" #s ":8*" #s "+3], " X " offset:8192*(" #s "/8)+1024*((" #s "&7)/2)\n\t" \
      "ds_read_b64_tr_b16 a[8*" #s "+4:8*" #s "+5], %4 offset:1024*" #s "\n\tds_read_b64_tr_b16 a[8*" #s "+6:8*" #s "+7], %4 offset:1024*" #s "+128\n\t"
#define K_LD2(s, t) K_LD(s, "%2") K_LD(t, "%3")
#define K_MM(N, s, t, A, B, C0, C1) "s_waitcnt lgkmcnt(" #N ")\n\tv_mfma_f32_16x16x32_bf16 %0, a[8*" #s "+" #A ":8*" #s "+" #A "+3], a[8*" #s "+" #B ":8*" #s "+" #B "+3], " C0 "\n\t" \
      "v_mfma_f32_16x16x32_bf16 %1, a[8*" #t "+" #A ":8*" #t "+" #A "+3], a[8*" #t "+" #B ":8*" #t "+" #B "+3], " C1 "\n\t"
#define K_ALL(A, B) asm volatile("s_waitcnt vmcnt(0) lgkmcnt(0)\n\t" K_LD2(0, 1) K_LD2(2, 3) K_LD2(4, 5) K_MM(12, 0, 1, A, B, "0", "0") K_LD2(6, 7) K_MM(12, 2, 3, A, B, "%0", "%1") \
        K_LD2(8, 9) K_MM(12, 4, 5, A, B, "%0", "%1") K_LD2(10, 11) K_MM(12, 6, 7, A, B, "%0", "%1") K_LD2(12, 13) K_MM(12, 8, 9, A, B, "%0", "%1") \
        K_LD2(14, 15) K_MM(12, 10, 11, A, B, "%0", "%1") K_MM(6, 12, 13, A, B, "%0", "%1") K_MM(0, 14, 15, A, B, "%0", "%1") "s_nop 7" \
        : "=&v"(acc), "=&v"(acd) : "v"(xe), "v"(xe ^ 64), "v"(wa) : "memory", CLOB_A_ALL)
      if (chain) K_ALL(0, 4); else K_ALL(4, 0);
      STAMP(1);
      acc = (acc + acd) * (chain ? 1.0f : 0.25f * 1.44269504088896f);
      const u32x2 pk = {pkbf(acc[0], acc[1]), pkbf(acc[2], acc[3])};
      const __amdgpu_buffer_rsrc_t rs = __builtin_amdgcn_make_buffer_rsrc(ws + W_KV, 0, KV_BYTES, 0x00020000);
      __builtin_amdgcn_raw_buffer_store_b64(pk, rs, off, 0, 16);
      __builtin_amdgcn_raw_buffer_store_b64((u32x2){0x3f803f80u, 0x3f803f80u}, rs, (chain && l15 == 0) ? off + 256 : 0x80000000u, 0, 16);
      const float n2 = sq2(pk[0], sq2(pk[1], 0.f));
      const unsigned kn2 = row_umax(__float_as_uint(sum32(sum16(n2, n2))));
      asm volatile("s_waitcnt vmcnt(0)" ::: "memory");
      if (lane == 0) __hip_atomic_store((gu64*)(ws + W_F1) + chain * 128 + 4 * grp + qq, ((unsigned long long)tg << 32) | kn2, RLX_AGENT);
      __builtin_amdgcn_s_setprio(0);
      STAMP(2);
    }
#pragma unroll
    for (int j = 0; j < 53; ++j) asm volatile("" : "=v"(T[j]));
    asm volatile("" : "=s"(d2));
  } else {
    unsigned pAr, pAi, pBr, pBi, pCr, pCi, ld2;
    const unsigned po = (unsigned)((2 * cp + tch) * 256 + lane * 4);
    unsigned cpf = 0;
    GLD1(cpf, (unsigned)(tid < CODE_LINES ? tid * 128 : 0), pc & ~255ull);
    GLD1(pAr, po, Are); GLD1(pAi, po, Aim); GLD1(pBr, po, Bre); GLD1(pBi, po, Bim); GLD1(pCr, po, Cre); GLD1(pCi, po, Cim);
    LD32(sb + L_M + M_QMAX) = 0;
    asm volatile("s_load_dword %0, %2, %4\n\ts_load_dword %1, %3, %4\n\ts_waitcnt lgkmcnt(0)\n\ts_barrier"
                 : "=&s"(ld2), "=&s"(d2) : "s"(logdt2), "s"(dskip2), "s"(cp * 4) : "memory");
    gdma_rows4(wq0 + (384 + 32 * w) * 1024, wq0 + (392 + 32 * w) * 1024, wq, sb + L_WQ + (384 + 32 * w) * 128);
    const float ldt = tch ? bfhi(ld2) : bflo(ld2);
    const float eh = ldt * 1.44269502162933349609f, el = fmaf(ldt, 1.44269502162933349609f, -eh) + ldt * 1.925963033500011e-8f;
    const float e0 = __builtin_amdgcn_exp2f(eh);
    const float dt = bflo(pkbf(fmaf(e0, el * 0.693147180559945f, e0), 0.f));
    asm volatile("s_waitcnt vmcnt(4)" : "+v"(pAr), "+v"(pAi), "+v"(pBr), "+v"(pBi), "+v"(pCr), "+v"(pCi), "+v"(cpf) :: "memory");
    STAMP(1);
    const float ar0 = dt * bflo(pAr), ar1 = dt * bfhi(pAr);
    float lam = 0.f;
    if (__any(fmaxf(ar0, ar1) > 0.f)) lam = __uint_as_float(wave_umax(__float_as_uint(fmaxf(fmaxf(ar0, ar1), 0.f))));
    const float ar = (par ? ar1 : ar0) - lam, ai = dt * (par ? bfhi(pAi) : bflo(pAi));
    {
      cf c0 = cmul({bflo(pCr), bflo(pCi)}, {bflo(pBr), bflo(pBi)});
      cf c1 = cmul({bfhi(pCr), bfhi(pCi)}, {bfhi(pBr), bfhi(pBi)});
      int ex = (int)(wave_umax(__float_as_uint(fmaxf(fmaxf(fabsf(c0.x), fabsf(c0.y)), fmaxf(fabsf(c1.x), fabsf(c1.y))))) >> 23);
      ex = ex < 8 ? 8 : (ex > 245 ? 245 : ex);
      const float sdn = __uint_as_float((unsigned)(252 - ex) << 23), sup = __uint_as_float((unsigned)(ex + 2) << 23) * dt;
      LD64(sb + L_M + M_SCALE + tch * 8) = (u32x2){__float_as_uint(sup), __float_as_uint(lam * 1.44269504088896f)};
      cf c = par ? c1 : c0; c.x *= sdn; c.y *= sdn;
      const cf w1 = cexp_(ar, ai), W = cexp_(256.f * ar, 256.f * ai);
      const cf w2 = cmul(w1, w1), w4 = cmul(w2, w2), w6 = cmul(w4, w2);
      cf v = c;
#pragma unroll
      for (int j = 0; j < 8; ++j) { T[32 + j] = pkh(v.x, v.y); if (j < 7) v = cmul(v, w1); }
      v = cmul(c, W); T[40] = pkh(v.x, v.y);
      cf e = cmul(v, w2); T[41] = pkh(e.x, e.y);
      e = cmul(v, w4); T[42] = pkh(e.x, e.y);
      e = cmul(v, w6); T[43] = pkh(e.x, e.y);
      v = cmul(v, W); T[44] = pkh(v.x, v.y);
      e = cmul(v, w4); T[45] = pkh(e.x, e.y);
      v = cmul(v, W); T[46] = pkh(v.x, v.y);
      e = cmul(v, w4); T[47] = pkh(e.x, e.y);
#pragma unroll
      for (int j = 16; j < 21; ++j) { v = cmul(v, W); T[32 + j] = pkh(v.x, v.y); }
    }
    {
      cf p[16];
      cf g = cexp_(8.f * ar, -8.f * ai);
      p[0] = {1.f, 0.f}; p[1] = g;
      g = cmul(g, g); p[2] = g; p[3] = cmul(p[1], g);
      g = cmul(g, g);
      p[4] = g;
#pragma unroll
      for (int i = 1; i < 4; ++i) p[4 + i] = cmul(p[i], g);
      g = cmul(g, g);
      p[8] = g;
#pragma unroll
      for (int i = 1; i < 8; ++i) p[8 + i] = cmul(p[i], g);
      g = cmul(g, g);
#pragma unroll
      for (int i = 0; i < 16; ++i) {
        T[i] = pkh(p[i].x, p[i].y);
        cf q = i ? cmul(p[i], g) : g;
        T[16 + i] = pkh(q.x, q.y);
      }
    }
#pragma unroll
    for (int j = 0; j < 53; ++j) asm volatile("" : "+v"(T[j]));
    STAMP(2);
    asm volatile("s_waitcnt vmcnt(0)" ::: "memory");
  }
  STAMP(3);
  BAR();
  STAMP(4);

  const unsigned tag = __builtin_amdgcn_readfirstlane(LD32(sb + L_M + M_TAG));
  const bool watcher = kh && vl == 0;
  const unsigned fo = W_F1 + lane * 16;
  f32x16 qa16;
  {
    const unsigned A0 = sb + L_WQ + (256 * kh + 8 * h + (l15 >> 2)) * 128 + (((4 * (qq & 1) + 2 * ((g4 & 1) ^ kh) + ((lane & 3) >> 1)) ^ ((l15 >> 3) << 2)) << 4) + (lane & 1) * 8;
    const unsigned X0 = sb + L_X + ((qq & 2) + (g4 & 1)) * 16384 + kh * 8192 + (l15 >> 3) * 4096 + (l15 & 7) * 128 + ((h ^ (l15 >> 1)) << 4);
#define Q_LD(s, X) "ds_read_b128 a[64+8*(" #s "&7):64+8*(" #s "&7)+3], " X " offset:1024*(" #s "/4)\n\t" \
      "ds_read_b64_tr_b16 a[64+8*(" #s "&7)+4:64+8*(" #s "&7)+5], %1 offset:2048*" #s "\n\tds_read_b64_tr_b16 a[64+8*(" #s "&7)+6:64+8*(" #s "&7)+7], %1 offset:2048*" #s "+512\n\t"
#define Q_MM(N, s, C) "s_waitcnt lgkmcnt(" #N ")\n\tv_mfma_f32_32x32x16_bf16 %0, a[64+8*(" #s "&7)+4:64+8*(" #s "&7)+7], a[64+8*(" #s "&7):64+8*(" #s "&7)+3], " C "\n\t"
#define Q_OPS : "v"(A0), "v"(X0), "v"(X0 ^ 32), "v"(X0 ^ 64), "v"(X0 ^ 96) : "memory", CLOB_A60_127
    asm volatile(Q_LD(0, "%2") Q_LD(1, "%3") Q_LD(2, "%4") Q_LD(3, "%5") Q_LD(4, "%2") Q_MM(12, 0, "0") Q_LD(5, "%3") Q_MM(12, 1, "%0") Q_LD(6, "%4") Q_MM(12, 2, "%0")
                 Q_LD(7, "%5") Q_MM(12, 3, "%0") Q_LD(8, "%2") Q_MM(12, 4, "%0") Q_LD(9, "%3") Q_MM(12, 5, "%0") Q_LD(10, "%4") Q_MM(12, 6, "%0") Q_LD(11, "%5") Q_MM(12, 7, "%0")
                 : "=&v"(qa16) Q_OPS);
    asm volatile(Q_LD(12, "%2") Q_MM(12, 8, "%0") Q_LD(13, "%3") Q_MM(12, 9, "%0") Q_LD(14, "%4") Q_MM(12, 10, "%0") Q_LD(15, "%5") Q_MM(12, 11, "%0")
                 Q_MM(9, 12, "%0") Q_MM(6, 13, "%0") Q_MM(3, 14, "%0") Q_MM(0, 15, "%0") "s_nop 11"
                 : "+v"(qa16) Q_OPS);
  }
  STAMP(19);
  const unsigned xa = opaque(sb + L_R2 + lane * 16 + w * 2048);
  LDF4(xa) = __builtin_shufflevector(qa16, qa16, 8, 9, 10, 11);
  LDF4(xa + 1024) = __builtin_shufflevector(qa16, qa16, 12, 13, 14, 15);
#define F_LD(R) "global_load_dwordx4 v[" #R ":" #R "+3], %[fo], %[ws] sc1\n\tglobal_load_dwordx4 v[" #R "+4:" #R "+7], %[fo], %[ws] offset:1024 sc1\n\t"
#define F_CK(R) "s_waitcnt vmcnt(2)\n\tv_xor_b32 %[a], %[tg], v[" #R "+1]\n\tv_xor_b32 %[b], %[tg], v[" #R "+3]\n\tv_xor_b32 v[" #R "+5], %[tg], v[" #R "+5]\n\t" \
    "v_xor_b32 v[" #R "+7], %[tg], v[" #R "+7]\n\tv_or3_b32 %[a], %[a], %[b], v[" #R "+5]\n\tv_or_b32 %[a], %[a], v[" #R "+7]\n\t" \
    "v_max_u32 %[b], v[" #R "], v[" #R "+2]\n\tv_cmp_ne_u32 vcc, 0, %[a]\n\ts_cbranch_vccz 9f\n\t"
  if (watcher) asm volatile(F_LD(112) :: [fo] "v"(fo), [ws] "s"(ws) : "memory", CLOB_V112_127);
  BAR();
  STAMP(20);
  const i32x4 rsKV = make_srd(ws + W_KV, KV_BYTES);
  const unsigned ko = lane * 16, vo = l31 <= 16 ? 1024 + h * 320 + l31 * 16 : 0x80000000u;
  const unsigned nt = kh ? 32 - N_SLOW : N_SLOW;
  const unsigned t0 = ((kh ? 0 : 64 - 2 * N_SLOW) + (w & 1) * nt + 2 * bi) & 63;
  unsigned tl = t0;
  {
    const f32x4 q0 = __builtin_shufflevector(qa16, qa16, 0, 1, 2, 3) + LDF4(xa ^ 8192), q1 = __builtin_shufflevector(qa16, qa16, 4, 5, 6, 7) + LDF4((xa ^ 8192) + 1024);
    const u32x4 o = {pkbf(q0[0], q0[1]), pkbf(q0[2], q0[3]), pkbf(q1[0], q1[1]), pkbf(q1[2], q1[3])};
    LD128(sb + L_Q + (2 * qq + kh) * 1024 + lane * 16) = o;
    const unsigned n2 = row_umax(__float_as_uint(sum32(sq2(o[0], sq2(o[1], sq2(o[2], sq2(o[3], 0.f)))))));
    asm volatile("s_mov_b64 exec, 0x10001\n\tds_max_u32 %0, %1\n\ts_mov_b64 exec, -1" :: "v"(sb + L_M + M_QMAX), "v"(n2) : "memory");
  }
  STAMP(5);
  if (watcher) {
    unsigned fa, km, spins = kSpinCap;
    asm volatile("1:\n\t" F_LD(120) F_CK(112) F_LD(112) F_CK(120) "s_sub_u32 %[n], %[n], 1\n\ts_cbranch_scc0 1b\n\t9:"
                 : [a] "=&v"(fa), [b] "=&v"(km), [n] "+s"(spins) : [fo] "v"(fo), [ws] "s"(ws), [tg] "s"(tag) : "memory", "vcc", "scc", CLOB_V112_127);
    LD32(sb + L_M + M_KMAX) = wave_umax(km);
    if (b == 0 && lane == 0) __hip_atomic_store((gu32*)(ws + W_E), tag, RLX_AGENT);
  }
  BAR();
  STAMP(6);
  if (kh) tl = kv_first(tl, ko, vo, rsKV);

  const u32x2 qk2 = LD64(sb + L_M + M_QMAX);
  const float bound = fmaf(__builtin_amdgcn_sqrtf(__uint_as_float(qk2[0]) * __uint_as_float(qk2[1])), 1.002f, 1e-3f);
  const unsigned exact = __builtin_amdgcn_readfirstlane(bound <= 32.0f ? 0u : 1u);
  const unsigned qaddr = sb + L_Q + (qq >> 1) * 4096 + lane * 16;
  if (exact) {
    const unsigned maddr = sb + L_MX + (qq >> 1) * 512 + l31 * 4;
    if (tid < 256) LDF(sb + L_MX + tid * 4) = -3.0e38f;
    BAR();
    float m[4] = {-3.0e38f, -3.0e38f, -3.0e38f, -3.0e38f};
    const __amdgpu_buffer_rsrc_t rs = __builtin_amdgcn_make_buffer_rsrc(ws + W_KV, 0, KV_BYTES, 0x00020000);
#pragma unroll 1
    for (unsigned i = 0; i < nt; ++i) {
      u32x4 kf = __builtin_amdgcn_raw_buffer_load_b128(rs, ko, ((t0 + i) & 63) * KV_TILE, 16);
#pragma unroll
      for (int t = 0; t < 4; ++t) {
        const u32x4 q = LD128(qaddr + t * 1024);
        f32x16 s_;
        asm volatile("s_nop 1\n\tv_mfma_f32_32x32x16_bf16 %0, %1, %2, 0\n\ts_nop 15" : "=&v"(s_) : "v"(kf), "v"(q) : CLOB_A60_127);
#pragma unroll
        for (int r = 0; r < 16; ++r) m[t] = fmaxf(m[t], s_[r]);
      }
    }
#pragma unroll
    for (int t = 0; t < 4; ++t) __builtin_amdgcn_ds_fmaxf((LAS float*)(size_t)(maddr + t * 128), m[t], __ATOMIC_RELAXED, __HIP_MEMORY_SCOPE_WORKGROUP, false);
    BAR();
    asm volatile("v_accvgpr_write_b32 a60, %0\n\tv_accvgpr_write_b32 a61, %1\n\tv_accvgpr_write_b32 a62, %2\n\tv_accvgpr_write_b32 a63, %3"
                 :: "v"(-LDF(maddr)), "v"(-LDF(maddr + 128)), "v"(-LDF(maddr + 256)), "v"(-LDF(maddr + 384)) : CLOB_A60_127);
  }
  STAMP(7);

  unsigned yv[8];
  if (kh == 0) {
    __builtin_amdgcn_s_setprio(2);
    {
      const unsigned c4 = (unsigned)(par * 64 + lane) * 4, tlo = sb + L_TLO + tch * TLO_CH + c4, thi = sb + L_THI + tch * THI_CH + c4;
#pragma unroll
      for (int i = 0; i < 32; ++i) LD32(tlo + i * 528) = T[i];
#pragma unroll
      for (int i = 0; i < 21; ++i) LD32(thi + i * 528) = T[32 + i];
    }
    unsigned ug[8];
    const unsigned go = (unsigned)(tid * 8192 + cp * 4);
#define GATHER4(k, base) asm volatile("global_load_dword %0, %4, %5\n\tglobal_load_dword %1, %4, %5 offset:1024\n\tglobal_load_dword %2, %4, %5 offset:2048\n\t" \
      "global_load_dword %3, %4, %5 offset:3072" : "=&v"(ug[k]), "=&v"(ug[k + 1]), "=&v"(ug[k + 2]), "=&v"(ug[k + 3]) : "v"(go), "s"(base) : "memory")
    post(sb + L_M + M_SW + w * 4, tag);
    GATHER4(0, u);
    wait4(sb + L_M + M_SW, tag);
    STAMP(8);
    const unsigned KT = sb + L_P;
    {
      const int it = w & 1;
      const unsigned aa = sb + L_TLO + tch * TLO_CH + (16 * it + l15) * 528 + g4 * 16;
      const unsigned b0 = sb + L_THI + tch * THI_CH + l15 * 528 + g4 * 16, b1 = sb + L_THI + tch * THI_CH + (l15 < 4 ? 16 + l15 : 20) * 528 + g4 * 16;
      const u32x2 sc = LD64(sb + L_M + M_SCALE + tch * 8);
      f32x4 k0 = {0, 0, 0, 0}, k1 = {0, 0, 0, 0};
      asm volatile("s_nop 1" : "+v"(k0), "+v"(k1));
#pragma unroll
      for (int ks = 0; ks < 8; ++ks) {
        const u32x4 fa = LD128(aa + ks * 64), fb = LD128(b0 + ks * 64), fc = LD128(b1 + ks * 64);
        asm volatile("v_mfma_f32_16x16x32_f16 %0, %2, %3, %0\n\tv_mfma_f32_16x16x32_f16 %1, %2, %4, %1" : "+v"(k0), "+v"(k1) : "v"(fa), "v"(fb), "v"(fc) : CLOB_A60_127);
      }
      asm volatile("s_nop 7" : "+v"(k0), "+v"(k1));
      const int lb = 128 * it + 32 * g4;
      const int l0 = lb + (l15 < 8 ? l15 : l15 < 12 ? 240 + 2 * l15 : 512 + 4 * (l15 & 1) + 128 * (l15 & 2)), l1 = lb + 1024 + 256 * (l15 < 4 ? l15 : 4);
      k0 *= __uint_as_float(sc[0]); k1 *= __uint_as_float(sc[0]);
      if (__builtin_amdgcn_readfirstlane(sc[1])) {
#pragma unroll
        for (int r = 0; r < 4; ++r) {
          k0[r] *= __builtin_amdgcn_exp2f(__uint_as_float(sc[1]) * (float)(l0 + 8 * r));
          k1[r] *= __builtin_amdgcn_exp2f(__uint_as_float(sc[1]) * (float)(l1 + 8 * r));
        }
      }
#define KWR(l, k) asm volatile("ds_write_b16 %0, %1\n\tds_write_b16_d16_hi %0, %1 offset:32\n\tds_write_b16 %0, %2 offset:64\n\tds_write_b16_d16_hi %0, %2 offset:96" \
                 :: "v"(KT + 16 + (l) * 4 + 2 * tch), "v"(pkbf(k[0], k[1])), "v"(pkbf(k[2], k[3])) : "memory")
      KWR(l0, k0); KWR(l1, k1);
      if (w == 0) LD128(KT) = (u32x4){0u, 0u, 0u, 0u};
      if (tid < 248) { const int c = tid >= 124, i = tid - 124 * c; LD128(sb + L_U + c * U_CH + (i >> 2) * 80 + (i & 3) * 16) = (u32x4){0u, 0u, 0u, 0u}; }
    }
    post(sb + L_M + M_SW + 16 + w * 4, tag);
    GATHER4(4, u + 2048);
    wait4(sb + L_M + M_SW + 16, tag);
    STAMP(9);
#pragma unroll
    for (int k = 0; k < 2; ++k) {
      const int id = tid + 256 * k;
      unsigned d[8];
      const float f0 = (float)(4 * id - 3);
#pragma unroll
      for (int i = 0; i < 4; ++i) {
        const unsigned r = pkbf(f0 + (float)(2 * i), f0 + (float)(2 * i + 1));
        d[2 * i] = LD32(KT + 16 + 4 * (int)bflo(r));
        if (i < 3) d[2 * i + 1] = LD32(KT + 16 + 4 * (int)bfhi(r));
      }
      const unsigned ka = sb + L_KC + 224 + id * 32;
#pragma unroll
      for (int c = 0; c < 2; ++c) {
        unsigned P[6];
#pragma unroll
        for (int i = 0; i < 6; ++i) P[i] = __builtin_amdgcn_perm(d[i], d[i + 1], c ? 0x07060302u : 0x05040100u);
        LD128(ka + c * KC_CH) = (u32x4){P[2], P[0], P[3], P[1]};
        LD128(ka + c * KC_CH + 16) = (u32x4){P[4], P[2], P[5], P[3]};
      }
    }
    if (tid < 28) LD128(sb + L_KC + (tid >= 14) * (KC_CH - 224) + tid * 16) = (u32x4){0u, 0u, 0u, 0u};
    STAMP(10);
    asm volatile("s_waitcnt vmcnt(0)" : "+v"(ug[0]), "+v"(ug[1]), "+v"(ug[2]), "+v"(ug[3]), "+v"(ug[4]), "+v"(ug[5]), "+v"(ug[6]), "+v"(ug[7]) :: "memory");
    {
      u32x4 c0, c1;
#pragma unroll
      for (int m = 0; m < 4; ++m) { c0[m] = __builtin_amdgcn_perm(ug[2 * m + 1], ug[2 * m], 0x05040100u); c1[m] = __builtin_amdgcn_perm(ug[2 * m + 1], ug[2 * m], 0x07060302u); }
      const unsigned ua = sb + L_U + (31 + (tid >> 2)) * 80 + (tid & 3) * 16;
      LD128(ua) = c0; LD128(ua + U_CH) = c1;
    }
    tl = kv_first(tl, ko, vo, rsKV);
    sync4(sb, w, 2, tag);
    STAMP(11);
    const int ch = w >> 1, pi = w & 1;
    unsigned aA = sb + L_KC + ch * KC_CH + (unsigned)(8 * (16 * (32 * pi - 1) + l31 - 8 * h + 24));
    unsigned aF = sb + L_U + ch * U_CH + (unsigned)(80 * (l31 + 31 - 16 * pi) + 16 * h);
    f32x16 y0, y1;
    unsigned cl;
    const u32x2 zz = {0u, 0u};
#define CLD(R0, R1, R2, R3, R4, R5, OA, OF) \
      "ds_read_b64 v[" #R0 "], %3 offset:" #OA "+32\n\tds_read_b64 v[" #R1 "], %3 offset:" #OA "\n\tds_read_b128 v[" #R4 "], %4 offset:" #OF "\n\t" \
      "ds_read_b128 v[" #R5 "], %4 offset:" #OF "+2560\n\tds_read_b64 v[" #R2 "], %3 offset:" #OA "+8224\n\tds_read_b64 v[" #R3 "], %3 offset:" #OA "+8192\n\t"
#define CMM(A0, A1, F0, F1) "s_waitcnt lgkmcnt(6)\n\tv_mfma_f32_32x32x16_bf16 %0, v[" #A0 "], v[" #F0 "], %0\n\t" \
      "v_mfma_f32_32x32x16_bf16 %1, v[" #A0 "], v[" #F1 "], %1\n\tv_mfma_f32_32x32x16_bf16 %1, v[" #A1 "], v[" #F0 "], %1\n\t"
    asm volatile(
        "s_setprio " STR(CONV_PRIO) "\n\tv_mfma_f32_32x32x8_bf16 %0, %5, %5, 0\n\tv_mfma_f32_32x32x8_bf16 %1, %5, %5, 0\n\ts_mov_b32 %2, 16\n\t"
        CLD(96:97, 98:99, 100:101, 102:103, 104:107, 108:111, 0, 32)
        "1:\n\t"
        CLD(112:113, 114:115, 116:117, 118:119, 120:123, 124:127, 128, 0)
        CMM(96:99, 100:103, 104:107, 108:111)
        "v_add_u32 %3, 0x100, %3\n\tv_add_u32 %4, 0xffffffb0, %4\n\t"
        CLD(96:97, 98:99, 100:101, 102:103, 104:107, 108:111, 0, 32)
        CMM(112:115, 116:119, 120:123, 124:127)
        "s_sub_u32 %2, %2, 1\n\ts_cmp_lg_u32 %2, 0\n\ts_cbranch_scc1 1b\n\t"
        "s_setprio 2\n\ts_waitcnt lgkmcnt(0)\n\ts_nop 15"
        : "=&v"(y0), "=&v"(y1), "=&s"(cl), "+v"(aA), "+v"(aF) : "v"(zz)
        : "memory", "scc", CLOB_V96_127, CLOB_A60_127);
    const unsigned pa = opaque(sb + L_P + lane * 16), pw = opaque(pa + w * 8192);
#pragma unroll
    for (int q = 0; q < 4; ++q) {
      LDF4(pw + q * 1024) = (f32x4){y0[4 * q], y0[4 * q + 1], y0[4 * q + 2], y0[4 * q + 3]};
      LDF4(pw + 4096 + q * 1024) = (f32x4){y1[4 * q], y1[4 * q + 1], y1[4 * q + 2], y1[4 * q + 3]};
    }
    STAMP(12);
    sync4(sb, w, 3, tag);
    const unsigned wo0 = (128 * w + (lane >> 3)) * 1024 + hg * 128 + (((lane & 7) ^ wswz(lane >> 3)) << 4);
    gdma_rows16(wo0, (wo0 ^ 64) + 8192, wo, sb + L_WO + w * 16384);
    const float d0 = bflo(d2), d1 = bfhi(d2);
    const unsigned pr = opaque(pa + ch * 4096 + pi * 2048), uo = opaque(sb + L_U + (31 + 32 * ch + l31) * 80 + 32 * pi + 8 * h);
#pragma unroll
    for (int qi = 0; qi < 2; ++qi) {
      const unsigned o = pr + qi * 1024;
      const f32x4 ya = LDF4(o) + LDF4(o + 8192), yb = LDF4(o + 16384) + LDF4(o + 24576);
      const u32x2 ua = LD64(uo + 16 * qi), ub = LD64(uo + 16 * qi + U_CH);
#pragma unroll
      for (int e = 0; e < 4; ++e) {
        const float u0 = (e & 1) ? bfhi(ua[e >> 1]) : bflo(ua[e >> 1]), u1 = (e & 1) ? bfhi(ub[e >> 1]) : bflo(ub[e >> 1]);
        const unsigned yy = pkbf(ya[e], yb[e]), ud = pkbf(u0 * d0, u1 * d1);
        yv[4 * qi + e] = pkbf(bflo(yy) + bflo(ud), bfhi(yy) + bfhi(ud));
      }
    }
    post(sb + L_M + M_SW + 64 + w * 4, tag);
  } else {
    gdma1((64 * bi + 16 * qq + r8) * 1024 + xcd * 128 + c8 * 16, u, sb + L_DUMP);
    gdma1((64 * bi + 16 * qq + 8 + r8) * 1024 + xcd * 128 + c8 * 16, u, sb + L_DUMP);
#pragma unroll
    for (int i = 0; i < 8; ++i) asm volatile("" : "=a"(yv[i]));
  }
  STAMP(13);

  {
    f32x16 O[4];
    const i32x4 rsY = make_srd(out, 4194304);
    const unsigned yo = (1024 * (w >> 1) + 32 * l31 + 16 * (w & 1) + 4 * h) * 2048 + cp * 4;
    if (exact) attend<true>(O, tl, nt, kh ^ 1, qaddr, -bound, ko, vo, rsKV, yo, rsY, yv, sb + L_MX + w * 128 + 88);
    else attend<false>(O, tl, nt, kh ^ 1, qaddr, -bound, ko, vo, rsKV, yo, rsY, yv, sb + L_MX + w * 128 + 88);
    STAMP(21);
    if (kh == 0) wait4(sb + L_M + M_SW + 64, tag);
    const unsigned oa = opaque(sb + L_OA + (w ^ 4) * 4 * OA_SLOT + lane * 16), ol = opaque(sb + L_OA + (w ^ 4) * 4 * OA_SLOT + 2048 + lane * 4);
#pragma unroll
    for (int t = 0; t < 4; ++t) {
      LDF4(oa + t * OA_SLOT) = (f32x4){O[t][0], O[t][1], O[t][2], O[t][3]};
      LDF4(oa + t * OA_SLOT + 1024) = (f32x4){O[t][4], O[t][5], O[t][6], O[t][7]};
      LDF(ol + t * OA_SLOT) = O[t][8];
    }
  }
  asm volatile("s_waitcnt vmcnt(0)" ::: "memory");
  BAR();
  STAMP(14);

  int lane2 = lane; asm volatile("" : "+v"(lane2));
  {
    const int lane = lane2, g4 = lane >> 4, l15 = lane & 15;
    const __amdgpu_buffer_rsrc_t rsO = __builtin_amdgcn_make_buffer_rsrc(ws + W_OX, 0, 2097152, 0x00020000);
    {
      const unsigned s0 = opaque(sb + L_OA + (8 * kh + qq) * OA_SLOT + lane * 16), s1 = opaque(sb + L_OA + (8 * kh + qq) * OA_SLOT + 2048 + lane * 4);
      constexpr unsigned W1 = 4 * OA_SLOT, W4 = 16 * OA_SLOT;
      f32x4 o0 = (LDF4(s0) + LDF4(s0 + W1)) + (LDF4(s0 + W4) + LDF4(s0 + W4 + W1));
      f32x4 o1 = (LDF4(s0 + 1024) + LDF4(s0 + W1 + 1024)) + (LDF4(s0 + W4 + 1024) + LDF4(s0 + W4 + W1 + 1024));
      const float inv = __builtin_amdgcn_rcpf(sum32((LDF(s1) + LDF(s1 + W1)) + (LDF(s1 + W4) + LDF(s1 + W4 + W1))));
      o0 *= inv; o1 *= inv;
      u32x4 pk = {pkbf(o0[0], o0[1]), pkbf(o0[2], o0[3]), pkbf(o1[0], o1[1]), pkbf(o1[2], o1[3])};
      __builtin_amdgcn_raw_buffer_store_b128(pk, rsO, lane * 16 + ((2 * grp + kh) * 32 + 4 * hg + qq) * 1024, 0, 16);
    }
    const unsigned r0 = 256 * kh + 16 * (g4 >> 1) + 4 * (g4 & 1) + (l15 >> 2);
    const unsigned A0 = sb + L_WO + r0 * 128 + (((4 * kh + ((lane & 3) >> 1)) ^ wswz(r0)) << 4) + (lane & 1) * 8;
#define A_WF1(s, e, A, B) "ds_read_b64_tr_b16 a[16*" #s "+4*" #e ":16*" #s "+4*" #e "+1], " A " offset:4096*" #s "\n\t" \
                          "ds_read_b64_tr_b16 a[16*" #s "+4*" #e "+2:16*" #s "+4*" #e "+3], " B " offset:4096*" #s "+1024\n\t"
#define A_WF(s) A_WF1(s, 0, "%0", "%2") A_WF1(s, 1, "%1", "%3") A_WF1(s, 2, "%2", "%0") A_WF1(s, 3, "%3", "%1")
    asm volatile(A_WF(0) A_WF(1) A_WF(2) A_WF(3) "s_waitcnt vmcnt(0)\n\ts_barrier"
                 :: "v"(A0), "v"(A0 ^ 32), "v"(A0 ^ 64), "v"(A0 ^ 96) : "memory", CLOB_A_ALL);
    if (tid == 0) __hip_atomic_store((gu32*)(ws + W_F2) + grp * 32 + hg, tag, RLX_AGENT);
    STAMP(15);
    asm volatile(A_WF(4) A_WF(5) A_WF(6) A_WF(7) "s_waitcnt lgkmcnt(0)"
                 :: "v"(A0), "v"(A0 ^ 32), "v"(A0 ^ 64), "v"(A0 ^ 96) : "memory", CLOB_A_ALL);
    _Pragma("unroll 1") for (unsigned spins = 0; spins < kSpinCap; ++spins) {
      unsigned f = __hip_atomic_load((gu32*)(ws + W_F2) + grp * 32 + 4 * kh + (lane & 3), RLX_AGENT);
      if (__all(f == tag)) break;
    }
    __builtin_amdgcn_fence(__ATOMIC_ACQUIRE, "wavefront");
    STAMP(16);
    const int oo = (g4 >> 1) * 1024 + ((g4 & 1) * 32 + 16 * (qq & 1) + l15) * 16, os = ((2 * grp + (qq >> 1)) * 32 + 16 * kh) * 1024;
    u32x4 of[8];
#pragma unroll
    for (int s = 0; s < 8; ++s) of[s] = __builtin_amdgcn_raw_buffer_load_b128(rsO, oo + (s & 1) * 2048, os + (s >> 1) * 4096, 16);
    __builtin_amdgcn_sched_barrier(0);
    f32x4 acc[4];
    asm volatile("v_mfma_f32_16x16x32_bf16 %0, a[0:3], %4, 0\n\tv_mfma_f32_16x16x32_bf16 %1, a[4:7], %4, 0\n\t"
                 "v_mfma_f32_16x16x32_bf16 %2, a[8:11], %4, 0\n\tv_mfma_f32_16x16x32_bf16 %3, a[12:15], %4, 0"
                 : "=&v"(acc[0]), "=&v"(acc[1]), "=&v"(acc[2]), "=&v"(acc[3]) : "v"(of[0]) : CLOB_A_ALL);
#define OPJ(s, T) asm volatile("v_mfma_f32_16x16x32_bf16 %0, " AR4(16 * s) ", %4, %0\n\tv_mfma_f32_16x16x32_bf16 %1, " AR4(16 * s + 4) ", %4, %1\n\t" \
                 "v_mfma_f32_16x16x32_bf16 %2, " AR4(16 * s + 8) ", %4, %2\n\tv_mfma_f32_16x16x32_bf16 %3, " AR4(16 * s + 12) ", %4, %3" T \
                 : "+v"(acc[0]), "+v"(acc[1]), "+v"(acc[2]), "+v"(acc[3]) : "v"(of[s]) : CLOB_A_ALL)
    OPJ(1, ""); OPJ(2, ""); OPJ(3, ""); OPJ(4, ""); OPJ(5, ""); OPJ(6, ""); OPJ(7, "\n\ts_nop 7");
    STAMP(17);
    const unsigned xa = opaque(sb + L_U + lane * 16 + w * 2048), xb = opaque(sb + L_U + lane * 16 + (w ^ 4) * 2048);
    LDF4(xa) = acc[2];
    LDF4(xa + 1024) = acc[3];
    BAR();
    const f32x4 q0 = acc[0] + LDF4(xb), q1 = acc[1] + LDF4(xb + 1024);
    auto sa = __builtin_amdgcn_permlane16_swap(pkbf(q0[0], q0[1]), pkbf(q1[0], q1[1]), false, false);
    auto sc = __builtin_amdgcn_permlane16_swap(pkbf(q0[2], q0[3]), pkbf(q1[2], q1[3]), false, false);
    u32x4 v = {sa[0], sc[0], sa[1], sc[1]};
    *(u32x4*)(out + (64 * grp + 16 * qq + l15) * 1024 + 512 + 64 * hg + 32 * kh + 16 * (g4 & 1) + 8 * (g4 >> 1)) = v;
    STAMP(18);
    STAMP_OUT();
  }
}

extern "C" void kernel_launch(void* const* d_in, const int* in_sizes, int n_in, void* d_out, int out_size, void* d_ws,
                              size_t ws_size, hipStream_t stream) {
  typedef const unsigned short* P;
  fused_kernel<<<256, 512, 0, stream>>>((P)d_in[0], (P)d_in[1], (P)d_in[2], (P)d_in[3], (P)d_in[4], (P)d_in[5], (P)d_in[6],
                                        (const unsigned*)d_in[7], (const unsigned*)d_in[8], (P)d_in[9], (P)d_in[10], (P)d_in[11],
                                        (P)d_in[12], (P)d_in[13], (unsigned short*)d_out, (unsigned char*)d_ws);
}
```

```cpp
#include <hip/hip_runtime.h>
#include <hip/hip_bf16.h>
#include <stdint.h>

typedef float f32x16 __attribute__((ext_vector_type(16)));
typedef float f32x8 __attribute__((ext_vector_type(8)));
typedef float f32x4 __attribute__((ext_vector_type(4)));
typedef float f32x2 __attribute__((ext_vector_type(2)));
typedef __bf16 bf16x8 __attribute__((ext_vector_type(8)));
typedef __bf16 bf16x2 __attribute__((ext_vector_type(2)));
typedef _Float16 f16x8 __attribute__((ext_vector_type(8)));
typedef _Float16 f16x2 __attribute__((ext_vector_type(2)));
typedef short s16x4 __attribute__((ext_vector_type(4)));
typedef unsigned u32x4 __attribute__((ext_vector_type(4)));
typedef unsigned u32x2 __attribute__((ext_vector_type(2)));
#define LAS __attribute__((address_space(3)))
#define GAS __attribute__((address_space(1)))
typedef GAS unsigned gu32;
typedef GAS unsigned long long gu64;
#define RLX_AGENT __ATOMIC_RELAXED, __HIP_MEMORY_SCOPE_AGENT

constexpr unsigned L_X = 0;
constexpr unsigned L_TLO = 0, TLO_CH = 16896, L_THI = 33792, THI_CH = 11088;
constexpr unsigned L_KC = 0, KC_CH = 16640;
constexpr unsigned L_WO = 0;
constexpr unsigned L_B = 65536;
constexpr unsigned L_WQ = L_B;
constexpr unsigned L_R2 = L_B + 65536;
constexpr unsigned L_DUMP = L_B;
constexpr unsigned L_OA = L_B, OA_SLOT = 2304;
constexpr unsigned L_P = L_B + 40960;
constexpr unsigned L_U = L_B + 73728, U_CH = 7600;
constexpr unsigned L_Q = L_B + 88928;
constexpr unsigned L_MX = L_Q + 8192;
constexpr unsigned L_M = L_MX + 1024;
constexpr unsigned LDS_BYTES = L_M + 128;
static_assert(LDS_BYTES <= 163840, "lds");
constexpr unsigned M_TAG = 0, M_RDY = 4, M_QMAX = 8, M_KMAX = 12, M_SCALE = 16  , M_SW = 32  ;
#define CONV_PRIO 0
#define N_SLOW 8
static_assert(N_SLOW >= 4 && N_SLOW <= 28 && N_SLOW % 2 == 0, "the convolution's stores ride on the first four key tiles");

constexpr unsigned KV_TILE = 2304, KV_BYTES = 64 * KV_TILE;
constexpr unsigned W_E = 0, W_F1 = 2048, W_F2 = 4096, W_KV = 8192, W_OX = W_KV + KV_BYTES, W_END = W_OX + 2097152;
constexpr unsigned W_DBG = W_END;

constexpr int CODE_LINES = 208;
constexpr unsigned kSpinCap = 1u << 20;

#define LD32(a) (*(LAS unsigned*)(size_t)(a))
#define LDF(a) (*(LAS float*)(size_t)(a))
#define LD64(a) (*(LAS u32x2*)(size_t)(a))
#define LD128(a) (*(LAS u32x4*)(size_t)(a))
#define LDF4(a) (*(LAS f32x4*)(size_t)(a))
#define BAR() asm volatile("s_waitcnt lgkmcnt(0)\n\ts_barrier" ::: "memory")

#define STAMP(i)
#define STAMP_INIT()
#define STAMP_OUT()

__device__ __forceinline__ float bflo(unsigned v) { return __uint_as_float(v << 16); }
__device__ __forceinline__ float bfhi(unsigned v) { return __uint_as_float(v & 0xffff0000u); }
__device__ __forceinline__ unsigned pkbf(float a, float b) { f32x2 x = {a, b}; return __builtin_bit_cast(unsigned, __builtin_convertvector(x, bf16x2)); }
__device__ __forceinline__ unsigned pkh(float a, float b) { f32x2 x = {a, b}; return __builtin_bit_cast(unsigned, __builtin_convertvector(x, f16x2)); }
template <int CTRL> __device__ __forceinline__ unsigned dppu(unsigned v) { return __builtin_amdgcn_update_dpp(0u, v, CTRL, 0xf, 0xf, true); }
__device__ __forceinline__ unsigned umax(unsigned a, unsigned b) { return a > b ? a : b; }
__device__ __forceinline__ unsigned row_umax(unsigned v) {
  v = umax(v, dppu<0x128>(v)); v = umax(v, dppu<0x124>(v)); v = umax(v, dppu<0x122>(v)); return umax(v, dppu<0x121>(v));
}
__device__ __forceinline__ unsigned wave_umax(unsigned v) {
  v = row_umax(v);
  auto a = __builtin_amdgcn_permlane16_swap(v, v, false, false);
  v = umax(a[0], a[1]);
  auto c = __builtin_amdgcn_permlane32_swap(v, v, false, false);
  return umax(c[0], c[1]);
}
__device__ __forceinline__ float sum16(float a, float b) {
  auto r = __builtin_amdgcn_permlane16_swap(__float_as_uint(a), __float_as_uint(b), false, false);
  return __uint_as_float(r[0]) + __uint_as_float(r[1]);
}
__device__ __forceinline__ float sum32(float v) {
  auto c = __builtin_amdgcn_permlane32_swap(__float_as_uint(v), __float_as_uint(v), false, false);
  return __uint_as_float(c[0]) + __uint_as_float(c[1]);
}
__device__ __forceinline__ float sq2(unsigned v, float c) {
  return __builtin_amdgcn_fdot2_f32_bf16(__builtin_bit_cast(bf16x2, v), __builtin_bit_cast(bf16x2, v), c, false);
}
__device__ __forceinline__ unsigned opaque(unsigned v) { asm volatile("" : "+v"(v)); return v; }
struct cf { float x, y; };
__device__ __forceinline__ cf cmul(cf a, cf b) { return {a.x * b.x - a.y * b.y, a.x * b.y + a.y * b.x}; }
__device__ __forceinline__ cf cexp_(float re, float im) {
  float m = __builtin_amdgcn_exp2f(re * 1.44269504088896f);
  float f = __builtin_amdgcn_fractf(im * 0.159154943091895f);
  return {m * __builtin_amdgcn_cosf(f), m * __builtin_amdgcn_sinf(f)};
}
__device__ __forceinline__ u32x4 trfrag(unsigned a0, unsigned a1) {
  s16x4 lo = __builtin_amdgcn_ds_read_tr16_b64_v4i16((LAS s16x4*)(size_t)a0);
  s16x4 hi = __builtin_amdgcn_ds_read_tr16_b64_v4i16((LAS s16x4*)(size_t)a1);
  u32x2 l = __builtin_bit_cast(u32x2, lo), h = __builtin_bit_cast(u32x2, hi);
  return (u32x4){l[0], l[1], h[0], h[1]};
}
__device__ __forceinline__ f32x16 mfma32(u32x4 a, u32x4 b, f32x16 c) {
  return __builtin_amdgcn_mfma_f32_32x32x16_bf16(__builtin_bit_cast(bf16x8, a), __builtin_bit_cast(bf16x8, b), c, 0, 0, 0);
}
__device__ __forceinline__ f32x4 mfma16(u32x4 a, u32x4 b, f32x4 c) {
  return __builtin_amdgcn_mfma_f32_16x16x32_bf16(__builtin_bit_cast(bf16x8, a), __builtin_bit_cast(bf16x8, b), c, 0, 0, 0);
}
#define GLD1(dst, voff, base) asm volatile("global_load_dword %0, %1, %2" : "=v"(dst) : "v"(voff), "s"(base))
#define GLD4(dst, voff, base, imm) asm volatile("global_load_dwordx4 %0, %1, %2 offset:%3" : "=v"(dst) : "v"(voff), "s"(base), "i"(imm))
__device__ __forceinline__ void gdma4(unsigned voff, const void* base, unsigned lds_dst) {
  unsigned keep;
  asm volatile("s_mov_b32 %0, m0\n\ts_mov_b32 m0, %3\n\ts_nop 0\n\tglobal_load_lds_dwordx4 %1, %2\n\tglobal_load_lds_dwordx4 %1, %2 offset:1024\n\t"
               "global_load_lds_dwordx4 %1, %2 offset:2048\n\tglobal_load_lds_dwordx4 %1, %2 offset:3072\n\ts_mov_b32 m0, %0"
               : "=&s"(keep) : "v"(voff), "s"(base), "s"(lds_dst) : "memory");
}
#define DMA_PAIR "global_load_lds_dwordx4 %2, %1\n\ts_add_u32 m0, m0, 0x400\n\ts_nop 0\n\tglobal_load_lds_dwordx4 %3, %1\n\ts_add_u32 m0, m0, 0x400\n\tv_add_u32 %2, 0x4000, %2\n\tv_add_u32 %3, 0x4000, %3\n\t"
__device__ __forceinline__ void gdma_rows16(unsigned v0, unsigned v1, const void* base, unsigned lds_dst) {
  unsigned keep;
  asm volatile("s_mov_b32 %0, m0\n\ts_mov_b32 m0, %4\n\ts_nop 0\n\t" DMA_PAIR DMA_PAIR DMA_PAIR DMA_PAIR DMA_PAIR DMA_PAIR DMA_PAIR DMA_PAIR "s_mov_b32 m0, %0"
               : "=&s"(keep), "+s"(base), "+v"(v0), "+v"(v1) : "s"(lds_dst) : "memory", "scc");
}
#define DMA_X(v, o) "global_load_lds_dwordx4 " v ", %1 offset:" #o "\n\ts_add_u32 m0, m0, "
__device__ __forceinline__ void gdma_x(unsigned v0, unsigned v1, const void* base, unsigned lds_dst) {
  unsigned keep;
  asm volatile("s_mov_b32 %0, m0\n\ts_mov_b32 m0, %4\n\ts_nop 0\n\t"
               DMA_X("%2", 0) "896\n\ts_nop 0\n\t" DMA_X("%2", 128) "896\n\ts_nop 0\n\t" DMA_X("%2", 256) "896\n\ts_nop 0\n\t" DMA_X("%2", 384) "1408\n\ts_nop 0\n\t"
               DMA_X("%3", 0) "896\n\ts_nop 0\n\t" DMA_X("%3", 128) "896\n\ts_nop 0\n\t" DMA_X("%3", 256) "896\n\ts_nop 0\n\t" DMA_X("%3", 384) "0\n\t"
               "s_mov_b32 m0, %0"
               : "=&s"(keep) : "s"(base), "v"(v0), "v"(v1), "s"(lds_dst) : "memory", "scc");
}
__device__ __forceinline__ void gdma_rows4(unsigned v0, unsigned v1, const void* base, unsigned lds_dst) {
  unsigned keep;
  asm volatile("s_mov_b32 %0, m0\n\ts_mov_b32 m0, %4\n\ts_nop 0\n\t" DMA_PAIR DMA_PAIR "s_mov_b32 m0, %0"
               : "=&s"(keep), "+s"(base), "+v"(v0), "+v"(v1) : "s"(lds_dst) : "memory", "scc");
}
__device__ __forceinline__ void gdma1(unsigned voff, const void* base, unsigned lds_dst) {
  unsigned keep;
  asm volatile("s_mov_b32 %0, m0\n\ts_mov_b32 m0, %3\n\ts_nop 0\n\tglobal_load_lds_dwordx4 %1, %2\n\ts_mov_b32 m0, %0"
               : "=&s"(keep) : "v"(voff), "s"(base), "s"(lds_dst) : "memory");
}
__device__ __forceinline__ unsigned wswz(unsigned row) { return ((((row >> 1) & 3u) ^ (((row >> 3) & 1u) << 1)) << 1); }

__device__ __forceinline__ void wait4(unsigned addr, unsigned want) {
  _Pragma("unroll 1") for (unsigned spins = 0; spins < kSpinCap; ++spins) {
    u32x4 v;
    asm volatile("ds_read_b128 %0, %1\n\ts_waitcnt lgkmcnt(0)" : "=v"(v) : "v"(addr) : "memory");
    if (__builtin_amdgcn_readfirstlane(v[0] & v[1] & v[2] & v[3]) == want) break;
    __builtin_amdgcn_s_sleep(1);
  }
}
__device__ __forceinline__ void post(unsigned addr, unsigned val) {
  asm volatile("ds_write_b32 %0, %1" :: "v"(addr), "v"(val) : "memory");
}

__device__ __forceinline__ void sync4(unsigned sb, int w, int k) {
  post(sb + L_M + M_SW + k * 16 + w * 4, 1u);
  wait4(sb + L_M + M_SW + k * 16, 1u);
}

#define STR_(x) #x
#define STR(x) STR_(x)
#define VR(b, o) "v[" STR(b) "+" STR(o) "]"
#define VRR(b, o, n) "v[" STR(b) "+" STR(o) ":" STR(b) "+" STR(o) "+" STR(n) "-1]"
#define AR4(i) "a[" STR(i) ":" STR(i) "+3]"
#define A_EX(S, i) "v_exp_f32 " VR(S, i) ", " VR(S, i) "\n\t"
#define A_QK(S, r, q) "v_mfma_f32_32x32x16_bf16 " VRR(S, 0, 16) ", " AR4(80 + 12 * r) ", " AR4(64 + 4 * q) ", v[80:95]\n\t"
#define A_PV0(O, r, S) "v_smfmac_f32_32x32x32_bf16 " O ", " AR4(84 + 12 * r) ", " VRR(S, 0, 8) ", v79\n\t"
#define A_PV1(O, r, S) "v_smfmac_f32_32x32x32_bf16 " O ", " AR4(88 + 12 * r) ", " VRR(S, 8, 8) ", v79\n\t"
#define A_RD4(o, q) "v_accvgpr_read_b32 " VR(80, o) ", a[60+" #q "]\n\tv_accvgpr_read_b32 " VR(81, o) ", a[60+" #q "]\n\t" \
                    "v_accvgpr_read_b32 " VR(82, o) ", a[60+" #q "]\n\tv_accvgpr_read_b32 " VR(83, o) ", a[60+" #q "]\n\t"
#define A_SHIFT_EXACT(q) A_RD4(0, q) A_RD4(4, q) A_RD4(8, q) A_RD4(12, q) "s_nop 1\n\t"
#define A_SHIFT_NONE(q) ""
#define A_BLOCK(Sc, Sp, Op, rp, rn, qn, Oc, rc, SH, XM, X2, X3, X4, X5, X6, X7, X8, X9) \
  A_EX(Sc, 0) A_EX(Sc, 1) A_PV1(Op, rp, Sp) XM A_EX(Sc, 2) X2 A_EX(Sc, 3) X3 A_EX(Sc, 4) X4 A_EX(Sc, 5) X5 SH(qn) A_QK(Sp, rn, qn) \
  A_EX(Sc, 6) X6 A_EX(Sc, 7) X7 A_EX(Sc, 8) X8 A_EX(Sc, 9) X9 A_EX(Sc, 10) A_PV0(Oc, rc, Sc) \
  A_EX(Sc, 11) A_EX(Sc, 12) A_EX(Sc, 13) A_EX(Sc, 14) A_EX(Sc, 15)
#define A_KV0 "s_mul_i32 %[so], %[tl], 2304\n\t"
#define A_KV1 "s_add_u32 %[tl], %[tl], 1\n\t"
#define A_KV2 "s_and_b32 %[tl], %[tl], 63\n\t"
#define A_KV3(r) "buffer_load_dwordx4 " AR4(80 + 12 * r) ", %[ko], %[rk], %[so] offen sc1\n\t"
#define A_KV4(r) "buffer_load_dwordx4 " AR4(84 + 12 * r) ", %[vo], %[rk], %[so] offen sc1\n\t"
#define A_KV5(r) "buffer_load_dwordx4 " AR4(88 + 12 * r) ", %[vo], %[rk], %[so] offen offset:640 sc1\n\t"
#define A_KV(r) A_KV0 A_KV1 A_KV2 A_KV3(r) A_KV4(r) A_KV5(r)
#define A_STEP(r, rp, rn, SH, XM, Y0, Y1, Y2, W, E0, E1) \
  A_BLOCK(96, 112, "%[o3]", rp, r, 1, "%[o0]", r, SH, XM, A_KV0, A_KV1, A_KV2, "", A_KV3(rp), A_KV4(rp), A_KV5(rp), "") \
  A_BLOCK(112, 96, "%[o0]", r, r, 2, "%[o1]", r, SH, "", Y0, Y1, "", "", "", "", "", "") \
  A_BLOCK(96, 112, "%[o1]", r, r, 3, "%[o2]", r, SH, "", "", Y2, "", "", "", "", "", "") \
  A_BLOCK(112, 96, "%[o2]", r, rn, 0, "%[o3]", r, SH, "", "", "", "", "s_waitcnt vmcnt(" #W ")\n\t", E0, E1, "", "")
#define A_YSO(so) "s_movk_i32 %[so], " #so "\n\t"
#define A_YA(y) "buffer_store_dword " y ", %[yo], %[ry], %[so] offen\n\t"
#define A_YB(y) "buffer_store_dword " y ", %[yo], %[ry], %[so] offen offset:2048\n\t"
#define A_CNT0 "s_sub_u32 %[n], %[n], 2\n\t"
#define A_CNT1 "s_cmp_eq_u32 %[n], 0\n\t"
#define A_EXIT(r) A_PV1("%[o3]", r, 112)
#define A_MOV4(o) "v_mov_b32 " VR(80, o) ", %[nb]\n\tv_mov_b32 " VR(81, o) ", %[nb]\n\tv_mov_b32 " VR(82, o) ", %[nb]\n\tv_mov_b32 " VR(83, o) ", %[nb]\n\t"
#define A_ZERO(O) "v_mfma_f32_32x32x8_bf16 " O ", v[78:79], v[78:79], 0\n\t"
#define ITSTAMP
#define ITOPS
#define ITCLOB
#define A_ATT(SH) \
  "ds_read_b128 a[64:67], %[qa]\n\tds_read_b128 a[68:71], %[qa] offset:1024\n\tds_read_b128 a[72:75], %[qa] offset:2048\n\tds_read_b128 a[76:79], %[qa] offset:3072\n\t" \
  "v_mov_b32 v78, 0\n\tv_mov_b32 v79, 0\n\t" A_MOV4(0) A_MOV4(4) A_ZERO("%[o0]") A_MOV4(8) A_MOV4(12) A_ZERO("%[o1]") A_ZERO("%[o2]") A_ZERO("%[o3]") \
  "s_waitcnt vmcnt(6) lgkmcnt(0)\n\t" SH(0) A_QK(96, 0, 0) "v_mov_b32 v79, 0xdddd\n\ts_setprio 1\n\ts_nop 13\n\t" \
  "s_cmp_eq_u32 %[fl], 0\n\t" A_EX(96, 0) A_EX(96, 1) "s_cbranch_scc1 11f\n\ts_branch 12f\n\t" \
  A_STEP(0, 3, 1, SH, "12:\n\t", A_YSO(0), A_YA("%[y0]"), A_YB("%[y1]"), 8, "", "") \
  A_STEP(1, 0, 2, SH, "", A_YSO(0x1000), A_YA("%[y2]"), A_YB("%[y3]"), 10, "", "") \
  A_STEP(2, 1, 3, SH, "", A_YSO(0x4000), A_YA("%[y4]"), A_YB("%[y5]"), 12, A_CNT0, "") \
  A_STEP(3, 2, 0, SH, "", A_YSO(0x5000), A_YA("%[y6]"), A_YB("%[y7]"), 12, A_CNT0, A_CNT1) ITSTAMP "s_cbranch_scc1 43f\n\t" \
  "10:\n\t" \
  A_STEP(0, 3, 1, SH, "11:\n\t", "", "", "", 6, "", "") \
  A_STEP(1, 0, 2, SH, "", "", "", "", 6, A_CNT0, A_CNT1) "s_cbranch_scc1 41f\n\t" \
  A_STEP(2, 1, 3, SH, "", "", "", "", 6, "", "") \
  A_STEP(3, 2, 0, SH, "", "", "", "", 6, A_CNT0, A_CNT1) ITSTAMP "s_cbranch_scc0 10b\n\t" \
  "43:\n\ts_nop 0\n\t" A_EXIT(3) "s_branch 99f\n\t41:\n\ts_nop 0\n\t" A_EXIT(1) \
  "99:\n\ts_setprio 0\n\ts_nop 15"
#define C8(p, a, b, c, d, e, f, g, h) p #a, p #b, p #c, p #d, p #e, p #f, p #g, p #h
#define CLOB_V78_127 "v78", "v79", C8("v", 80, 81, 82, 83, 84, 85, 86, 87), C8("v", 88, 89, 90, 91, 92, 93, 94, 95), CLOB_V96_127
#define CLOB_V96_127 C8("v", 96, 97, 98, 99, 100, 101, 102, 103), C8("v", 104, 105, 106, 107, 108, 109, 110, 111), \
  C8("v", 112, 113, 114, 115, 116, 117, 118, 119), C8("v", 120, 121, 122, 123, 124, 125, 126, 127)
#define CLOB_V112_127 C8("v", 112, 113, 114, 115, 116, 117, 118, 119), C8("v", 120, 121, 122, 123, 124, 125, 126, 127)
#define CLOB_A0_31 C8("a", 0, 1, 2, 3, 4, 5, 6, 7), C8("a", 8, 9, 10, 11, 12, 13, 14, 15), C8("a", 16, 17, 18, 19, 20, 21, 22, 23), C8("a", 24, 25, 26, 27, 28, 29, 30, 31)
#define CLOB_A32_59 C8("a", 32, 33, 34, 35, 36, 37, 38, 39), C8("a", 40, 41, 42, 43, 44, 45, 46, 47), C8("a", 48, 49, 50, 51, 52, 53, 54, 55), "a56", "a57", "a58", "a59"
#define CLOB_A60_127 "a60", "a61", "a62", "a63", C8("a", 64, 65, 66, 67, 68, 69, 70, 71), C8("a", 72, 73, 74, 75, 76, 77, 78, 79), \
  C8("a", 80, 81, 82, 83, 84, 85, 86, 87), C8("a", 88, 89, 90, 91, 92, 93, 94, 95), \
  C8("a", 96, 97, 98, 99, 100, 101, 102, 103), C8("a", 104, 105, 106, 107, 108, 109, 110, 111), \
  C8("a", 112, 113, 114, 115, 116, 117, 118, 119), C8("a", 120, 121, 122, 123, 124, 125, 126, 127)
#define CLOB_A_ALL CLOB_A0_31, CLOB_A32_59, CLOB_A60_127
typedef int i32x4 __attribute__((ext_vector_type(4)));
__device__ __forceinline__ i32x4 make_srd(const void* p, unsigned bytes) {
  const unsigned long long a = (unsigned long long)p;
  return (i32x4){(int)(unsigned)a, (int)((unsigned)(a >> 32) & 0xffffu), (int)bytes, 0x00020000};
}

__device__ __forceinline__ unsigned kv_first(unsigned tl, unsigned ko, unsigned vo, i32x4 rk) {
  unsigned so;
  asm volatile(A_KV(0) A_KV(1) A_KV(2) : [tl] "+s"(tl), [so] "=&s"(so) : [ko] "v"(ko), [vo] "v"(vo), [rk] "s"(rk) : "memory", "scc", CLOB_A60_127);
  return tl;
}
template <bool EXACT>
__device__ __forceinline__ void attend(f32x16 (&O)[4], unsigned tl, unsigned n, unsigned fl, unsigned qa, float nb, unsigned ko, unsigned vo, i32x4 rk,
                                       unsigned yo, i32x4 ry, const unsigned (&y)[8], unsigned sa) {
  unsigned so;
#define ATT_OPS : [o0] "=&v"(O[0]), [o1] "=&v"(O[1]), [o2] "=&v"(O[2]), [o3] "=&v"(O[3]), [tl] "+s"(tl), [n] "+s"(n), [so] "=&s"(so) ITOPS \
    : [fl] "s"(fl), [qa] "v"(qa), [nb] "v"(nb), [ko] "v"(ko), [vo] "v"(vo), [rk] "s"(rk), [yo] "v"(yo), [ry] "s"(ry), [y0] "a"(y[0]), [y1] "a"(y[1]), [y2] "a"(y[2]), \
      [y3] "a"(y[3]), [y4] "a"(y[4]), [y5] "a"(y[5]), [y6] "a"(y[6]), [y7] "a"(y[7]) : "memory", "scc", CLOB_V78_127, CLOB_A60_127 ITCLOB
  if (EXACT) asm volatile(A_ATT(A_SHIFT_EXACT) ATT_OPS);
  else asm volatile(A_ATT(A_SHIFT_NONE) ATT_OPS);
}

__global__ __launch_bounds__(512, 2) void fused_kernel(
    const unsigned short* __restrict__ u, const unsigned short* __restrict__ Are, const unsigned short* __restrict__ Aim,
    const unsigned short* __restrict__ Bre, const unsigned short* __restrict__ Bim, const unsigned short* __restrict__ Cre,
    const unsigned short* __restrict__ Cim, const unsigned* __restrict__ logdt2, const unsigned* __restrict__ dskip2,
    const unsigned short* __restrict__ x, const unsigned short* __restrict__ wq, const unsigned short* __restrict__ wk,
    const unsigned short* __restrict__ wv, const unsigned short* __restrict__ wo, unsigned short* __restrict__ out,
    unsigned char* __restrict__ ws) {
  __shared__ __attribute__((aligned(128))) unsigned char smem[LDS_BYTES];
  unsigned long long pc;
  asm volatile("s_getpc_b64 %0" : "=s"(pc));
  const unsigned sb = (unsigned)(size_t)(LAS unsigned char*)smem;
  const int tid = threadIdx.x, lane = tid & 63;
  const int w = __builtin_amdgcn_readfirstlane(tid >> 6);
  const int b = blockIdx.x, xcd = b & 7, bi = b >> 3;
  const int grp = xcd * 4 + (bi >> 3), hg = bi & 7, cp = xcd * 32 + bi;
  const int h = lane >> 5, g4 = lane >> 4, l15 = lane & 15, l31 = lane & 31;
  const int qq = w & 3, kh = w >> 2;
  const int chain = hg >> 2;
  const int vl = (qq - hg - 1) & 3;
  STAMP_INIT();
  asm volatile("" :: "s"(u), "s"(Are), "s"(Aim), "s"(Bre), "s"(Bim), "s"(Cre), "s"(Cim), "s"(logdt2), "s"(dskip2), "s"(x), "s"(wq),
               "s"(wk), "s"(wv), "s"(wo), "s"(out), "s"(ws));
  STAMP(29);
  const unsigned r8 = lane >> 3, c8 = lane & 7;
  const unsigned wq0 = r8 * 1024 + hg * 128 + ((c8 ^ ((r8 & 2) << 1)) << 4);
  const int tch = (w >> 1) & 1, par = w & 1;
  unsigned T[53], d2;

  if (kh) {
    const unsigned xo = (64 * grp + 16 * qq + r8) * 1024 + ((c8 ^ (r8 >> 1)) << 4);
    if (vl != 3) asm volatile("s_barrier" ::: "memory");
    else __builtin_amdgcn_s_setprio(3);
    gdma_x(xo, (xo ^ 64) + 8192, x, sb + L_X + qq * 16384);
    gdma_x(xo + 512, (xo ^ 64) + 8704, x, sb + L_X + qq * 16384 + 8192);
    if (vl != 3) {
      gdma_rows16(wq0 + vl * 131072, wq0 + vl * 131072 + 8192, wq, sb + L_WQ + vl * 16384);
      asm volatile("s_waitcnt vmcnt(0)" ::: "memory");
    } else {
      const unsigned short* wkv = chain ? wv : wk;
#pragma unroll
      for (int i = 0; i < 4; ++i) gdma4((unsigned)(i * 4096 + lane * 16), wkv, sb + L_R2 + i * 4096);
      asm volatile("s_barrier" ::: "memory");
      unsigned E;
      asm volatile("s_load_dword %0, %1, 0x0\n\ts_waitcnt lgkmcnt(0)" : "=s"(E) : "s"(ws) : "memory");
      const unsigned tg = E + (((E * 0x9E3779B1u ^ (E >> 15) ^ 0x5bd1e995u) & 0x7fffffffu) | 0x101u);
      LD32(sb + L_M + M_TAG) = tg;
      const unsigned wa = sb + L_R2 + (8 * g4 + (l15 >> 2)) * 32 + (lane & 3) * 8;
      const unsigned xe = sb + L_X + qq * 16384 + (l15 >> 3) * 4096 + (l15 & 7) * 128 + ((g4 ^ (l15 >> 1)) << 4);
      const unsigned off = (2 * grp + (qq >> 1)) * KV_TILE + l15 * 16 +
                           (chain ? 1024 + (qq & 1) * 640 + (g4 >> 1) * 320 + (g4 & 1) * 8 : (qq & 1) * 256 + (g4 & 1) * 512 + (g4 >> 1) * 8);
      f32x4 acc, acd;
#define K_LD(s, X) "ds_read_b128 a[8*" #s ":8*" #s "+3], " X " offset:8192*(" #s "/8)+1024*((" #s "&7)/2)\n\t" \
      "ds_read_b64_tr_b16 a[8*" #s "+4:8*" #s "+5], %4 offset:1024*" #s "\n\tds_read_b64_tr_b16 a[8*" #s "+6:8*" #s "+7], %4 offset:1024*" #s "+128\n\t"
#define K_LD2(s, t) K_LD(s, "%2") K_LD(t, "%3")
#define K_MM(N, s, t, A, B, C0, C1) "s_waitcnt lgkmcnt(" #N ")\n\tv_mfma_f32_16x16x32_bf16 %0, a[8*" #s "+" #A ":8*" #s "+" #A "+3], a[8*" #s "+" #B ":8*" #s "+" #B "+3], " C0 "\n\t" \
      "v_mfma_f32_16x16x32_bf16 %1, a[8*" #t "+" #A ":8*" #t "+" #A "+3], a[8*" #t "+" #B ":8*" #t "+" #B "+3], " C1 "\n\t"
#define K_ALL(A, B) asm volatile("s_waitcnt vmcnt(0) lgkmcnt(0)\n\t" K_LD2(0, 1) K_LD2(2, 3) K_LD2(4, 5) K_MM(12, 0, 1, A, B, "0", "0") K_LD2(6, 7) K_MM(12, 2, 3, A, B, "%0", "%1") \
        K_LD2(8, 9) K_MM(12, 4, 5, A, B, "%0", "%1") K_LD2(10, 11) K_MM(12, 6, 7, A, B, "%0", "%1") K_LD2(12, 13) K_MM(12, 8, 9, A, B, "%0", "%1") \
        K_LD2(14, 15) K_MM(12, 10, 11, A, B, "%0", "%1") K_MM(6, 12, 13, A, B, "%0", "%1") K_MM(0, 14, 15, A, B, "%0", "%1") "s_nop 7" \
        : "=&v"(acc), "=&v"(acd) : "v"(xe), "v"(xe ^ 64), "v"(wa) : "memory", CLOB_A_ALL)
      if (chain) K_ALL(0, 4); else K_ALL(4, 0);
      STAMP(1);
      acc = (acc + acd) * (chain ? 1.0f : 0.25f * 1.44269504088896f);
      const u32x2 pk = {pkbf(acc[0], acc[1]), pkbf(acc[2], acc[3])};
      const __amdgpu_buffer_rsrc_t rs = __builtin_amdgcn_make_buffer_rsrc(ws + W_KV, 0, KV_BYTES, 0x00020000);
      __builtin_amdgcn_raw_buffer_store_b64(pk, rs, off, 0, 16);
      __builtin_amdgcn_raw_buffer_store_b64((u32x2){0x3f803f80u, 0x3f803f80u}, rs, (chain && l15 == 0) ? off + 256 : 0x80000000u, 0, 16);
      const float n2 = sq2(pk[0], sq2(pk[1], 0.f));
      const unsigned kn2 = row_umax(__float_as_uint(sum32(sum16(n2, n2))));
      asm volatile("s_waitcnt vmcnt(0)" ::: "memory");
      if (lane == 0) __hip_atomic_store((gu64*)(ws + W_F1) + chain * 128 + 4 * grp + qq, ((unsigned long long)tg << 32) | kn2, RLX_AGENT);
      __builtin_amdgcn_s_setprio(0);
      STAMP(2);
    }
#pragma unroll
    for (int j = 0; j < 53; ++j) asm volatile("" : "=v"(T[j]));
    asm volatile("" : "=s"(d2));
  } else {
    unsigned pAr, pAi, pBr, pBi, pCr, pCi, ld2;
    const unsigned po = (unsigned)((2 * cp + tch) * 256 + lane * 4);
    unsigned cpf = 0;
    GLD1(cpf, (unsigned)(tid < CODE_LINES ? tid * 128 : 0), pc & ~255ull);
    GLD1(pAr, po, Are); GLD1(pAi, po, Aim); GLD1(pBr, po, Bre); GLD1(pBi, po, Bim); GLD1(pCr, po, Cre); GLD1(pCi, po, Cim);
    LD32(sb + L_M + l31 * 4) = 0;
    asm volatile("s_load_dword %0, %2, %4\n\ts_load_dword %1, %3, %4\n\ts_waitcnt lgkmcnt(0)\n\ts_barrier"
                 : "=&s"(ld2), "=&s"(d2) : "s"(logdt2), "s"(dskip2), "s"(cp * 4) : "memory");
    gdma_rows4(wq0 + (384 + 32 * w) * 1024, wq0 + (392 + 32 * w) * 1024, wq, sb + L_WQ + (384 + 32 * w) * 128);
    const float ldt = tch ? bfhi(ld2) : bflo(ld2);
    const float eh = ldt * 1.44269502162933349609f, el = fmaf(ldt, 1.44269502162933349609f, -eh) + ldt * 1.925963033500011e-8f;
    const float e0 = __builtin_amdgcn_exp2f(eh);
    const float dt = bflo(pkbf(fmaf(e0, el * 0.693147180559945f, e0), 0.f));
    asm volatile("s_waitcnt vmcnt(4)" : "+v"(pAr), "+v"(pAi), "+v"(pBr), "+v"(pBi), "+v"(pCr), "+v"(pCi), "+v"(cpf) :: "memory");
    STAMP(1);
    const float ar0 = dt * bflo(pAr), ar1 = dt * bfhi(pAr);
    float lam = 0.f;
    if (__any(fmaxf(ar0, ar1) > 0.f)) lam = __uint_as_float(wave_umax(__float_as_uint(fmaxf(fmaxf(ar0, ar1), 0.f))));
    const float ar = (par ? ar1 : ar0) - lam, ai = dt * (par ? bfhi(pAi) : bflo(pAi));
    {
      cf c0 = cmul({bflo(pCr), bflo(pCi)}, {bflo(pBr), bflo(pBi)});
      cf c1 = cmul({bfhi(pCr), bfhi(pCi)}, {bfhi(pBr), bfhi(pBi)});
      int ex = (int)(wave_umax(__float_as_uint(fmaxf(fmaxf(fabsf(c0.x), fabsf(c0.y)), fmaxf(fabsf(c1.x), fabsf(c1.y))))) >> 23);
      ex = ex < 8 ? 8 : (ex > 245 ? 245 : ex);
      const float sdn = __uint_as_float((unsigned)(252 - ex) << 23), sup = __uint_as_float((unsigned)(ex + 2) << 23) * dt;
      LD64(sb + L_M + M_SCALE + tch * 8) = (u32x2){__float_as_uint(sup), __float_as_uint(lam * 1.44269504088896f)};
      cf c = par ? c1 : c0; c.x *= sdn; c.y *= sdn;
      const cf w1 = cexp_(ar, ai), W = cexp_(256.f * ar, 256.f * ai);
      const cf w2 = cmul(w1, w1), w4 = cmul(w2, w2), w6 = cmul(w4, w2);
      cf v = c;
#pragma unroll
      for (int j = 0; j < 8; ++j) { T[32 + j] = pkh(v.x, v.y); if (j < 7) v = cmul(v, w1); }
      v = cmul(c, W); T[40] = pkh(v.x, v.y);
      cf e = cmul(v, w2); T[41] = pkh(e.x, e.y);
      e = cmul(v, w4); T[42] = pkh(e.x, e.y);
      e = cmul(v, w6); T[43] = pkh(e.x, e.y);
      v = cmul(v, W); T[44] = pkh(v.x, v.y);
      e = cmul(v, w4); T[45] = pkh(e.x, e.y);
      v = cmul(v, W); T[46] = pkh(v.x, v.y);
      e = cmul(v, w4); T[47] = pkh(e.x, e.y);
#pragma unroll
      for (int j = 16; j < 21; ++j) { v = cmul(v, W); T[32 + j] = pkh(v.x, v.y); }
    }
    {
      cf p[16];
      cf g = cexp_(8.f * ar, -8.f * ai);
      p[0] = {1.f, 0.f}; p[1] = g;
      g = cmul(g, g); p[2] = g; p[3] = cmul(p[1], g);
      g = cmul(g, g);
      p[4] = g;
#pragma unroll
      for (int i = 1; i < 4; ++i) p[4 + i] = cmul(p[i], g);
      g = cmul(g, g);
      p[8] = g;
#pragma unroll
      for (int i = 1; i < 8; ++i) p[8 + i] = cmul(p[i], g);
      g = cmul(g, g);
#pragma unroll
      for (int i = 0; i < 16; ++i) {
        T[i] = pkh(p[i].x, p[i].y);
        cf q = i ? cmul(p[i], g) : g;
        T[16 + i] = pkh(q.x, q.y);
      }
    }
#pragma unroll
    for (int j = 0; j < 53; ++j) asm volatile("" : "+v"(T[j]));
    STAMP(2);
    asm volatile("s_waitcnt vmcnt(0)" ::: "memory");
  }
  STAMP(3);
  BAR();
  STAMP(4);

  const unsigned tag = __builtin_amdgcn_readfirstlane(LD32(sb + L_M + M_TAG));
  const bool watcher = kh && vl == 0;
  const unsigned fo = W_F1 + lane * 16;
  f32x16 qa16;
  {
    const unsigned A0 = sb + L_WQ + (256 * kh + 8 * h + (l15 >> 2)) * 128 + (((4 * (qq & 1) + 2 * ((g4 & 1) ^ kh) + ((lane & 3) >> 1)) ^ ((l15 >> 3) << 2)) << 4) + (lane & 1) * 8;
    const unsigned X0 = sb + L_X + ((qq & 2) + (g4 & 1)) * 16384 + kh * 8192 + (l15 >> 3) * 4096 + (l15 & 7) * 128 + ((h ^ (l15 >> 1)) << 4);
#define Q_LD(s, X) "ds_read_b128 a[64+8*(" #s "&7):64+8*(" #s "&7)+3], " X " offset:1024*(" #s "/4)\n\t" \
      "ds_read_b64_tr_b16 a[64+8*(" #s "&7)+4:64+8*(" #s "&7)+5], %1 offset:2048*" #s "\n\tds_read_b64_tr_b16 a[64+8*(" #s "&7)+6:64+8*(" #s "&7)+7], %1 offset:2048*" #s "+512\n\t"
#define Q_MM(N, s, C) "s_waitcnt lgkmcnt(" #N ")\n\tv_mfma_f32_32x32x16_bf16 %0, a[64+8*(" #s "&7)+4:64+8*(" #s "&7)+7], a[64+8*(" #s "&7):64+8*(" #s "&7)+3], " C "\n\t"
#define Q_OPS : "v"(A0), "v"(X0), "v"(X0 ^ 32), "v"(X0 ^ 64), "v"(X0 ^ 96) : "memory", CLOB_A60_127
    asm volatile(Q_LD(0, "%2") Q_LD(1, "%3") Q_LD(2, "%4") Q_LD(3, "%5") Q_LD(4, "%2") Q_MM(12, 0, "0") Q_LD(5, "%3") Q_MM(12, 1, "%0") Q_LD(6, "%4") Q_MM(12, 2, "%0")
                 Q_LD(7, "%5") Q_MM(12, 3, "%0") Q_LD(8, "%2") Q_MM(12, 4, "%0") Q_LD(9, "%3") Q_MM(12, 5, "%0") Q_LD(10, "%4") Q_MM(12, 6, "%0") Q_LD(11, "%5") Q_MM(12, 7, "%0")
                 : "=&v"(qa16) Q_OPS);
    asm volatile(Q_LD(12, "%2") Q_MM(12, 8, "%0") Q_LD(13, "%3") Q_MM(12, 9, "%0") Q_LD(14, "%4") Q_MM(12, 10, "%0") Q_LD(15, "%5") Q_MM(12, 11, "%0")
                 Q_MM(9, 12, "%0") Q_MM(6, 13, "%0") Q_MM(3, 14, "%0") Q_MM(0, 15, "%0") "s_nop 11"
                 : "+v"(qa16) Q_OPS);
  }
  STAMP(19);
  const unsigned xa = opaque(sb + L_R2 + lane * 16 + w * 2048);
  LDF4(xa) = __builtin_shufflevector(qa16, qa16, 8, 9, 10, 11);
  LDF4(xa + 1024) = __builtin_shufflevector(qa16, qa16, 12, 13, 14, 15);
#define F_LD(R) "global_load_dwordx4 v[" #R ":" #R "+3], %[fo], %[ws] sc1\n\tglobal_load_dwordx4 v[" #R "+4:" #R "+7], %[fo], %[ws] offset:1024 sc1\n\t"
#define F_CK(R) "s_waitcnt vmcnt(2)\n\tv_xor_b32 %[a], %[tg], v[" #R "+1]\n\tv_xor_b32 %[b], %[tg], v[" #R "+3]\n\tv_xor_b32 v[" #R "+5], %[tg], v[" #R "+5]\n\t" \
    "v_xor_b32 v[" #R "+7], %[tg], v[" #R "+7]\n\tv_or3_b32 %[a], %[a], %[b], v[" #R "+5]\n\tv_or_b32 %[a], %[a], v[" #R "+7]\n\t" \
    "v_max_u32 %[b], v[" #R "], v[" #R "+2]\n\tv_cmp_ne_u32 vcc, 0, %[a]\n\ts_cbranch_vccz 9f\n\t"
  if (watcher) asm volatile(F_LD(112) :: [fo] "v"(fo), [ws] "s"(ws) : "memory", CLOB_V112_127);
  BAR();
  STAMP(20);
  const i32x4 rsKV = make_srd(ws + W_KV, KV_BYTES);
  const unsigned ko = lane * 16, vo = l31 <= 16 ? 1024 + h * 320 + l31 * 16 : 0x80000000u;
  const unsigned nt = kh ? 32 - N_SLOW : N_SLOW;
  const unsigned t0 = ((kh ? 0 : 64 - 2 * N_SLOW) + (w & 1) * nt + 2 * bi) & 63;
  unsigned tl = t0;
  {
    const f32x4 q0 = __builtin_shufflevector(qa16, qa16, 0, 1, 2, 3) + LDF4(xa ^ 8192), q1 = __builtin_shufflevector(qa16, qa16, 4, 5, 6, 7) + LDF4((xa ^ 8192) + 1024);
    const u32x4 o = {pkbf(q0[0], q0[1]), pkbf(q0[2], q0[3]), pkbf(q1[0], q1[1]), pkbf(q1[2], q1[3])};
    LD128(sb + L_Q + (2 * qq + kh) * 1024 + lane * 16) = o;
    const unsigned n2 = row_umax(__float_as_uint(sum32(sq2(o[0], sq2(o[1], sq2(o[2], sq2(o[3], 0.f)))))));
    asm volatile("s_mov_b64 exec, 0x10001\n\tds_max_u32 %0, %1\n\ts_mov_b64 exec, -1" :: "v"(sb + L_M + M_QMAX), "v"(n2) : "memory");
  }
  STAMP(5);
  if (watcher) {
    unsigned fa, km, spins = kSpinCap;
    asm volatile("1:\n\t" F_LD(120) F_CK(112) F_LD(112) F_CK(120) "s_sub_u32 %[n], %[n], 1\n\ts_cbranch_scc0 1b\n\t9:"
                 : [a] "=&v"(fa), [b] "=&v"(km), [n] "+s"(spins) : [fo] "v"(fo), [ws] "s"(ws), [tg] "s"(tag) : "memory", "vcc", "scc", CLOB_V112_127);
    LD32(sb + L_M + M_KMAX) = wave_umax(km);
    if (b == 0 && lane == 0) __hip_atomic_store((gu32*)(ws + W_E), tag, RLX_AGENT);
  }
  BAR();
  STAMP(6);
  if (kh) tl = kv_first(tl, ko, vo, rsKV);

  const u32x2 qk2 = LD64(sb + L_M + M_QMAX);
  const float bound = fmaf(__builtin_amdgcn_sqrtf(__uint_as_float(qk2[0]) * __uint_as_float(qk2[1])), 1.002f, 1e-3f);
  const unsigned exact = __builtin_amdgcn_readfirstlane(bound <= 32.0f ? 0u : 1u);
  const unsigned qaddr = sb + L_Q + (qq >> 1) * 4096 + lane * 16;
  if (exact) {
    const unsigned maddr = sb + L_MX + (qq >> 1) * 512 + l31 * 4;
    if (tid < 256) LDF(sb + L_MX + tid * 4) = -3.0e38f;
    BAR();
    float m[4] = {-3.0e38f, -3.0e38f, -3.0e38f, -3.0e38f};
    const __amdgpu_buffer_rsrc_t rs = __builtin_amdgcn_make_buffer_rsrc(ws + W_KV, 0, KV_BYTES, 0x00020000);
#pragma unroll 1
    for (unsigned i = 0; i < nt; ++i) {
      u32x4 kf = __builtin_amdgcn_raw_buffer_load_b128(rs, ko, ((t0 + i) & 63) * KV_TILE, 16);
#pragma unroll
      for (int t = 0; t < 4; ++t) {
        const u32x4 q = LD128(qaddr + t * 1024);
        f32x16 s_;
        asm volatile("s_nop 1\n\tv_mfma_f32_32x32x16_bf16 %0, %1, %2, 0\n\ts_nop 15" : "=&v"(s_) : "v"(kf), "v"(q) : CLOB_A60_127);
#pragma unroll
        for (int r = 0; r < 16; ++r) m[t] = fmaxf(m[t], s_[r]);
      }
    }
#pragma unroll
    for (int t = 0; t < 4; ++t) __builtin_amdgcn_ds_fmaxf((LAS float*)(size_t)(maddr + t * 128), m[t], __ATOMIC_RELAXED, __HIP_MEMORY_SCOPE_WORKGROUP, false);
    BAR();
    asm volatile("v_accvgpr_write_b32 a60, %0\n\tv_accvgpr_write_b32 a61, %1\n\tv_accvgpr_write_b32 a62, %2\n\tv_accvgpr_write_b32 a63, %3"
                 :: "v"(-LDF(maddr)), "v"(-LDF(maddr + 128)), "v"(-LDF(maddr + 256)), "v"(-LDF(maddr + 384)) : CLOB_A60_127);
  }
  STAMP(7);

  unsigned yv[8];
  if (kh == 0) {
    __builtin_amdgcn_s_setprio(2);
    {
      const unsigned c4 = (unsigned)(par * 64 + lane) * 4, tlo = sb + L_TLO + tch * TLO_CH + c4, thi = sb + L_THI + tch * THI_CH + c4;
#pragma unroll
      for (int i = 0; i < 32; ++i) LD32(tlo + i * 528) = T[i];
#pragma unroll
      for (int i = 0; i < 21; ++i) LD32(thi + i * 528) = T[32 + i];
    }
    unsigned ug[8];
    const unsigned go = (unsigned)(tid * 8192 + cp * 4);
#define GATHER4(k, base) asm volatile("global_load_dword %0, %4, %5\n\tglobal_load_dword %1, %4, %5 offset:1024\n\tglobal_load_dword %2, %4, %5 offset:2048\n\t" \
      "global_load_dword %3, %4, %5 offset:3072" : "=&v"(ug[k]), "=&v"(ug[k + 1]), "=&v"(ug[k + 2]), "=&v"(ug[k + 3]) : "v"(go), "s"(base) : "memory")
    post(sb + L_M + M_SW + w * 4, 1u);
    GATHER4(0, u);
    wait4(sb + L_M + M_SW, 1u);
    STAMP(8);
    const unsigned KT = sb + L_P;
    {
      const int it = w & 1;
      const unsigned aa = sb + L_TLO + tch * TLO_CH + (16 * it + l15) * 528 + g4 * 16;
      const unsigned b0 = sb + L_THI + tch * THI_CH + l15 * 528 + g4 * 16, b1 = sb + L_THI + tch * THI_CH + (l15 < 4 ? 16 + l15 : 20) * 528 + g4 * 16;
      const u32x2 sc = LD64(sb + L_M + M_SCALE + tch * 8);
      f32x4 k0 = {0, 0, 0, 0}, k1 = {0, 0, 0, 0};
      asm volatile("s_nop 1" : "+v"(k0), "+v"(k1));
#pragma unroll
      for (int ks = 0; ks < 8; ++ks) {
        const u32x4 fa = LD128(aa + ks * 64), fb = LD128(b0 + ks * 64), fc = LD128(b1 + ks * 64);
        asm volatile("v_mfma_f32_16x16x32_f16 %0, %2, %3, %0\n\tv_mfma_f32_16x16x32_f16 %1, %2, %4, %1" : "+v"(k0), "+v"(k1) : "v"(fa), "v"(fb), "v"(fc) : CLOB_A60_127);
      }
      asm volatile("s_nop 7" : "+v"(k0), "+v"(k1));
      const int lb = 128 * it + 32 * g4;
      const int l0 = lb + (l15 < 8 ? l15 : l15 < 12 ? 240 + 2 * l15 : 512 + 4 * (l15 & 1) + 128 * (l15 & 2)), l1 = lb + 1024 + 256 * (l15 < 4 ? l15 : 4);
      k0 *= __uint_as_float(sc[0]); k1 *= __uint_as_float(sc[0]);
      if (__builtin_amdgcn_readfirstlane(sc[1])) {
#pragma unroll
        for (int r = 0; r < 4; ++r) {
          k0[r] *= __builtin_amdgcn_exp2f(__uint_as_float(sc[1]) * (float)(l0 + 8 * r));
          k1[r] *= __builtin_amdgcn_exp2f(__uint_as_float(sc[1]) * (float)(l1 + 8 * r));
        }
      }
#define KWR(l, k) asm volatile("ds_write_b16 %0, %1\n\tds_write_b16_d16_hi %0, %1 offset:32\n\tds_write_b16 %0, %2 offset:64\n\tds_write_b16_d16_hi %0, %2 offset:96" \
                 :: "v"(KT + 16 + (l) * 4 + 2 * tch), "v"(pkbf(k[0], k[1])), "v"(pkbf(k[2], k[3])) : "memory")
      KWR(l0, k0); KWR(l1, k1);
      if (w == 0) LD128(KT) = (u32x4){0u, 0u, 0u, 0u};
      if (tid < 248) { const int c = tid >= 124, i = tid - 124 * c; LD128(sb + L_U + c * U_CH + (i >> 2) * 80 + (i & 3) * 16) = (u32x4){0u, 0u, 0u, 0u}; }
    }
    post(sb + L_M + M_SW + 16 + w * 4, 1u);
    GATHER4(4, u + 2048);
    wait4(sb + L_M + M_SW + 16, 1u);
    STAMP(9);
#pragma unroll
    for (int k = 0; k < 2; ++k) {
      const int id = tid + 256 * k;
      unsigned d[8];
      const float f0 = (float)(4 * id - 3);
#pragma unroll
      for (int i = 0; i < 4; ++i) {
        const unsigned r = pkbf(f0 + (float)(2 * i), f0 + (float)(2 * i + 1));
        d[2 * i] = LD32(KT + 16 + 4 * (int)bflo(r));
        if (i < 3) d[2 * i + 1] = LD32(KT + 16 + 4 * (int)bfhi(r));
      }
      const unsigned ka = sb + L_KC + 224 + id * 32;
#pragma unroll
      for (int c = 0; c < 2; ++c) {
        unsigned P[6];
#pragma unroll
        for (int i = 0; i < 6; ++i) P[i] = __builtin_amdgcn_perm(d[i], d[i + 1], c ? 0x07060302u : 0x05040100u);
        LD128(ka + c * KC_CH) = (u32x4){P[2], P[0], P[3], P[1]};
        LD128(ka + c * KC_CH + 16) = (u32x4){P[4], P[2], P[5], P[3]};
      }
    }
    if (tid < 28) LD128(sb + L_KC + (tid >= 14) * (KC_CH - 224) + tid * 16) = (u32x4){0u, 0u, 0u, 0u};
    STAMP(10);
    asm volatile("s_waitcnt vmcnt(0)" : "+v"(ug[0]), "+v"(ug[1]), "+v"(ug[2]), "+v"(ug[3]), "+v"(ug[4]), "+v"(ug[5]), "+v"(ug[6]), "+v"(ug[7]) :: "memory");
    {
      u32x4 c0, c1;
#pragma unroll
      for (int m = 0; m < 4; ++m) { c0[m] = __builtin_amdgcn_perm(ug[2 * m + 1], ug[2 * m], 0x05040100u); c1[m] = __builtin_amdgcn_perm(ug[2 * m + 1], ug[2 * m], 0x07060302u); }
      const unsigned ua = sb + L_U + (31 + (tid >> 2)) * 80 + (tid & 3) * 16;
      LD128(ua) = c0; LD128(ua + U_CH) = c1;
    }
    tl = kv_first(tl, ko, vo, rsKV);
    sync4(sb, w, 2);
    STAMP(11);
    const int ch = w >> 1, pi = w & 1;
    unsigned aA = sb + L_KC + ch * KC_CH + (unsigned)(8 * (16 * (32 * pi - 1) + l31 - 8 * h + 24));
    unsigned aF = sb + L_U + ch * U_CH + (unsigned)(80 * (l31 + 31 - 16 * pi) + 16 * h);
    f32x16 y0, y1;
    unsigned cl;
    const u32x2 zz = {0u, 0u};
#define CLD(R0, R1, R2, R3, R4, R5, OA, OF) \
      "ds_read_b64 v[" #R0 "], %3 offset:" #OA "+32\n\tds_read_b64 v[" #R1 "], %3 offset:" #OA "\n\tds_read_b128 v[" #R4 "], %4 offset:" #OF "\n\t" \
      "ds_read_b128 v[" #R5 "], %4 offset:" #OF "+2560\n\tds_read_b64 v[" #R2 "], %3 offset:" #OA "+8224\n\tds_read_b64 v[" #R3 "], %3 offset:" #OA "+8192\n\t"
#define CMM(A0, A1, F0, F1) "s_waitcnt lgkmcnt(6)\n\tv_mfma_f32_32x32x16_bf16 %0, v[" #A0 "], v[" #F0 "], %0\n\t" \
      "v_mfma_f32_32x32x16_bf16 %1, v[" #A0 "], v[" #F1 "], %1\n\tv_mfma_f32_32x32x16_bf16 %1, v[" #A1 "], v[" #F0 "], %1\n\t"
    asm volatile(
        "s_setprio " STR(CONV_PRIO) "\n\ts_nop 1\n\tv_mfma_f32_32x32x8_bf16 %0, %5, %5, 0\n\tv_mfma_f32_32x32x8_bf16 %1, %5, %5, 0\n\ts_mov_b32 %2, 16\n\t"
        CLD(96:97, 98:99, 100:101, 102:103, 104:107, 108:111, 0, 32)
        "1:\n\t"
        CLD(112:113, 114:115, 116:117, 118:119, 120:123, 124:127, 128, 0)
        CMM(96:99, 100:103, 104:107, 108:111)
        "v_add_u32 %3, 0x100, %3\n\tv_add_u32 %4, 0xffffffb0, %4\n\t"
        CLD(96:97, 98:99, 100:101, 102:103, 104:107, 108:111, 0, 32)
        CMM(112:115, 116:119, 120:123, 124:127)
        "s_sub_u32 %2, %2, 1\n\ts_cmp_lg_u32 %2, 0\n\ts_cbranch_scc1 1b\n\t"
        "s_setprio 2\n\ts_waitcnt lgkmcnt(0)\n\ts_nop 15"
        : "=&v"(y0), "=&v"(y1), "=&s"(cl), "+v"(aA), "+v"(aF) : "v"(zz)
        : "memory", "scc", CLOB_V96_127, CLOB_A60_127);
    const unsigned pa = opaque(sb + L_P + lane * 16), pw = opaque(pa + w * 8192);
#pragma unroll
    for (int q = 0; q < 4; ++q) {
      LDF4(pw + q * 1024) = (f32x4){y0[4 * q], y0[4 * q + 1], y0[4 * q + 2], y0[4 * q + 3]};
      LDF4(pw + 4096 + q * 1024) = (f32x4){y1[4 * q], y1[4 * q + 1], y1[4 * q + 2], y1[4 * q + 3]};
    }
    STAMP(12);
    sync4(sb, w, 3);
    const unsigned wo0 = (128 * w + (lane >> 3)) * 1024 + hg * 128 + (((lane & 7) ^ wswz(lane >> 3)) << 4);
    gdma_rows16(wo0, (wo0 ^ 64) + 8192, wo, sb + L_WO + w * 16384);
    const float d0 = bflo(d2), d1 = bfhi(d2);
    const unsigned pr = opaque(pa + ch * 4096 + pi * 2048), uo = opaque(sb + L_U + (31 + 32 * ch + l31) * 80 + 32 * pi + 8 * h);
#pragma unroll
    for (int qi = 0; qi < 2; ++qi) {
      const unsigned o = pr + qi * 1024;
      const f32x4 ya = LDF4(o) + LDF4(o + 8192), yb = LDF4(o + 16384) + LDF4(o + 24576);
      const u32x2 ua = LD64(uo + 16 * qi), ub = LD64(uo + 16 * qi + U_CH);
#pragma unroll
      for (int e = 0; e < 4; ++e) {
        const float u0 = (e & 1) ? bfhi(ua[e >> 1]) : bflo(ua[e >> 1]), u1 = (e & 1) ? bfhi(ub[e >> 1]) : bflo(ub[e >> 1]);
        const unsigned yy = pkbf(ya[e], yb[e]), ud = pkbf(u0 * d0, u1 * d1);
        yv[4 * qi + e] = pkbf(bflo(yy) + bflo(ud), bfhi(yy) + bfhi(ud));
      }
    }
    post(sb + L_M + M_SW + 64 + w * 4, 1u);
  } else {
    gdma1((64 * bi + 16 * qq + r8) * 1024 + xcd * 128 + c8 * 16, u, sb + L_DUMP);
    gdma1((64 * bi + 16 * qq + 8 + r8) * 1024 + xcd * 128 + c8 * 16, u, sb + L_DUMP);
#pragma unroll
    for (int i = 0; i < 8; ++i) asm volatile("" : "=a"(yv[i]));
  }
  STAMP(13);

  {
    f32x16 O[4];
    const i32x4 rsY = make_srd(out, 4194304);
    const unsigned yo = (1024 * (w >> 1) + 32 * l31 + 16 * (w & 1) + 4 * h) * 2048 + cp * 4;
    if (exact) attend<true>(O, tl, nt, kh ^ 1, qaddr, -bound, ko, vo, rsKV, yo, rsY, yv, sb + L_MX + w * 128 + 88);
    else attend<false>(O, tl, nt, kh ^ 1, qaddr, -bound, ko, vo, rsKV, yo, rsY, yv, sb + L_MX + w * 128 + 88);
    STAMP(21);
    if (kh == 0) wait4(sb + L_M + M_SW + 64, 1u);
    const unsigned oa = opaque(sb + L_OA + (w ^ 4) * 4 * OA_SLOT + lane * 16), ol = opaque(sb + L_OA + (w ^ 4) * 4 * OA_SLOT + 2048 + lane * 4);
#pragma unroll
    for (int t = 0; t < 4; ++t) {
      LDF4(oa + t * OA_SLOT) = (f32x4){O[t][0], O[t][1], O[t][2], O[t][3]};
      LDF4(oa + t * OA_SLOT + 1024) = (f32x4){O[t][4], O[t][5], O[t][6], O[t][7]};
      LDF(ol + t * OA_SLOT) = O[t][8];
    }
  }
  asm volatile("s_waitcnt vmcnt(0)" ::: "memory");
  BAR();
  STAMP(14);

  int lane2 = lane; asm volatile("" : "+v"(lane2));
  {
    const int lane = lane2, g4 = lane >> 4, l15 = lane & 15;
    const __amdgpu_buffer_rsrc_t rsO = __builtin_amdgcn_make_buffer_rsrc(ws + W_OX, 0, 2097152, 0x00020000);
    {
      const unsigned s0 = opaque(sb + L_OA + (8 * kh + qq) * OA_SLOT + lane * 16), s1 = opaque(sb + L_OA + (8 * kh + qq) * OA_SLOT + 2048 + lane * 4);
      constexpr unsigned W1 = 4 * OA_SLOT, W4 = 16 * OA_SLOT;
      f32x4 o0 = (LDF4(s0) + LDF4(s0 + W1)) + (LDF4(s0 + W4) + LDF4(s0 + W4 + W1));
      f32x4 o1 = (LDF4(s0 + 1024) + LDF4(s0 + W1 + 1024)) + (LDF4(s0 + W4 + 1024) + LDF4(s0 + W4 + W1 + 1024));
      const float inv = __builtin_amdgcn_rcpf(sum32((LDF(s1) + LDF(s1 + W1)) + (LDF(s1 + W4) + LDF(s1 + W4 + W1))));
      o0 *= inv; o1 *= inv;
      u32x4 pk = {pkbf(o0[0], o0[1]), pkbf(o0[2], o0[3]), pkbf(o1[0], o1[1]), pkbf(o1[2], o1[3])};
      __builtin_amdgcn_raw_buffer_store_b128(pk, rsO, lane * 16 + ((2 * grp + kh) * 32 + 4 * hg + qq) * 1024, 0, 16);
    }
    const unsigned r0 = 256 * kh + 16 * (g4 >> 1) + 4 * (g4 & 1) + (l15 >> 2);
    const unsigned A0 = sb + L_WO + r0 * 128 + (((4 * kh + ((lane & 3) >> 1)) ^ wswz(r0)) << 4) + (lane & 1) * 8;
#define A_WF1(s, e, A, B) "ds_read_b64_tr_b16 a[16*" #s "+4*" #e ":16*" #s "+4*" #e "+1], " A " offset:4096*" #s "\n\t" \
                          "ds_read_b64_tr_b16 a[16*" #s "+4*" #e "+2:16*" #s "+4*" #e "+3], " B " offset:4096*" #s "+1024\n\t"
#define A_WF(s) A_WF1(s, 0, "%0", "%2") A_WF1(s, 1, "%1", "%3") A_WF1(s, 2, "%2", "%0") A_WF1(s, 3, "%3", "%1")
    asm volatile(A_WF(0) A_WF(1) A_WF(2) A_WF(3) "s_waitcnt vmcnt(0)\n\ts_barrier"
                 :: "v"(A0), "v"(A0 ^ 32), "v"(A0 ^ 64), "v"(A0 ^ 96) : "memory", CLOB_A_ALL);
    if (tid == 0) __hip_atomic_store((gu32*)(ws + W_F2) + grp * 32 + hg, tag, RLX_AGENT);
    STAMP(15);
    asm volatile(A_WF(4) A_WF(5) A_WF(6) A_WF(7) "s_waitcnt lgkmcnt(0)"
                 :: "v"(A0), "v"(A0 ^ 32), "v"(A0 ^ 64), "v"(A0 ^ 96) : "memory", CLOB_A_ALL);
    _Pragma("unroll 1") for (unsigned spins = 0; spins < kSpinCap; ++spins) {
      unsigned f = __hip_atomic_load((gu32*)(ws + W_F2) + grp * 32 + 4 * kh + (lane & 3), RLX_AGENT);
      if (__all(f == tag)) break;
    }
    __builtin_amdgcn_fence(__ATOMIC_ACQUIRE, "wavefront");
    STAMP(16);
    const int oo = (g4 >> 1) * 1024 + ((g4 & 1) * 32 + 16 * (qq & 1) + l15) * 16, os = ((2 * grp + (qq >> 1)) * 32 + 16 * kh) * 1024;
    u32x4 of[8];
#pragma unroll
    for (int s = 0; s < 8; ++s) of[s] = __builtin_amdgcn_raw_buffer_load_b128(rsO, oo + (s & 1) * 2048, os + (s >> 1) * 4096, 16);
    __builtin_amdgcn_sched_barrier(0);
    f32x4 acc[4];
    asm volatile("v_mfma_f32_16x16x32_bf16 %0, a[0:3], %4, 0\n\tv_mfma_f32_16x16x32_bf16 %1, a[4:7], %4, 0\n\t"
                 "v_mfma_f32_16x16x32_bf16 %2, a[8:11], %4, 0\n\tv_mfma_f32_16x16x32_bf16 %3, a[12:15], %4, 0"
                 : "=&v"(acc[0]), "=&v"(acc[1]), "=&v"(acc[2]), "=&v"(acc[3]) : "v"(of[0]) : CLOB_A_ALL);
#define OPJ(s, T) asm volatile("v_mfma_f32_16x16x32_bf16 %0, " AR4(16 * s) ", %4, %0\n\tv_mfma_f32_16x16x32_bf16 %1, " AR4(16 * s + 4) ", %4, %1\n\t" \
                 "v_mfma_f32_16x16x32_bf16 %2, " AR4(16 * s + 8) ", %4, %2\n\tv_mfma_f32_16x16x32_bf16 %3, " AR4(16 * s + 12) ", %4, %3" T \
                 : "+v"(acc[0]), "+v"(acc[1]), "+v"(acc[2]), "+v"(acc[3]) : "v"(of[s]) : CLOB_A_ALL)
    OPJ(1, ""); OPJ(2, ""); OPJ(3, ""); OPJ(4, ""); OPJ(5, ""); OPJ(6, ""); OPJ(7, "\n\ts_nop 7");
    STAMP(17);
    const unsigned xa = opaque(sb + L_U + lane * 16 + w * 2048), xb = opaque(sb + L_U + lane * 16 + (w ^ 4) * 2048);
    LDF4(xa) = acc[2];
    LDF4(xa + 1024) = acc[3];
    BAR();
    const f32x4 q0 = acc[0] + LDF4(xb), q1 = acc[1] + LDF4(xb + 1024);
    auto sa = __builtin_amdgcn_permlane16_swap(pkbf(q0[0], q0[1]), pkbf(q1[0], q1[1]), false, false);
    auto sc = __builtin_amdgcn_permlane16_swap(pkbf(q0[2], q0[3]), pkbf(q1[2], q1[3]), false, false);
    u32x4 v = {sa[0], sc[0], sa[1], sc[1]};
    *(u32x4*)(out + (64 * grp + 16 * qq + l15) * 1024 + 512 + 64 * hg + 32 * kh + 16 * (g4 & 1) + 8 * (g4 >> 1)) = v;
    STAMP(18);
    STAMP_OUT();
  }
}

extern "C" void kernel_launch(void* const* d_in, const int* in_sizes, int n_in, void* d_out, int out_size, void* d_ws,
                              size_t ws_size, hipStream_t stream) {
  typedef const unsigned short* P;
  fused_kernel<<<256, 512, 0, stream>>>((P)d_in[0], (P)d_in[1], (P)d_in[2], (P)d_in[3], (P)d_in[4], (P)d_in[5], (P)d_in[6],
                                        (const unsigned*)d_in[7], (const unsigned*)d_in[8], (P)d_in[9], (P)d_in[10], (P)d_in[11],
                                        (P)d_in[12], (P)d_in[13], (unsigned short*)d_out, (unsigned char*)d_ws);
}
```

```cpp
#define DIAG 29
#include <hip/hip_runtime.h>
#include <hip/hip_bf16.h>
#include <stdint.h>

typedef float f32x16 __attribute__((ext_vector_type(16)));
typedef float f32x8 __attribute__((ext_vector_type(8)));
typedef float f32x4 __attribute__((ext_vector_type(4)));
typedef float f32x2 __attribute__((ext_vector_type(2)));
typedef __bf16 bf16x8 __attribute__((ext_vector_type(8)));
typedef __bf16 bf16x2 __attribute__((ext_vector_type(2)));
typedef _Float16 f16x8 __attribute__((ext_vector_type(8)));
typedef _Float16 f16x2 __attribute__((ext_vector_type(2)));
typedef short s16x4 __attribute__((ext_vector_type(4)));
typedef unsigned u32x4 __attribute__((ext_vector_type(4)));
typedef unsigned u32x2 __attribute__((ext_vector_type(2)));
#define LAS __attribute__((address_space(3)))
#define GAS __attribute__((address_space(1)))
typedef GAS unsigned gu32;
typedef GAS unsigned long long gu64;
#define RLX_AGENT __ATOMIC_RELAXED, __HIP_MEMORY_SCOPE_AGENT

constexpr unsigned L_X = 0;
constexpr unsigned L_TLO = 0, TLO_CH = 16896, L_THI = 33792, THI_CH = 11088;
constexpr unsigned L_KC = 0, KC_CH = 16640;
constexpr unsigned L_WO = 0;
constexpr unsigned L_B = 65536;
constexpr unsigned L_WQ = L_B;
constexpr unsigned L_R2 = L_B + 65536;
constexpr unsigned L_DUMP = L_B;
constexpr unsigned L_OA = L_B, OA_SLOT = 2304;
constexpr unsigned L_P = L_B + 40960;
constexpr unsigned L_U = L_B + 73728, U_CH = 7600;
constexpr unsigned L_Q = L_B + 88928;
constexpr unsigned L_MX = L_Q + 8192;
constexpr unsigned L_M = L_MX + 1024;
constexpr unsigned LDS_BYTES = L_M + 128;
static_assert(LDS_BYTES <= 163840, "lds");
constexpr unsigned M_TAG = 0, M_RDY = 4, M_QMAX = 8, M_KMAX = 12, M_SCALE = 16  , M_SW = 32  ;
#define CONV_PRIO 0
#define N_SLOW 8
static_assert(N_SLOW >= 4 && N_SLOW <= 28 && N_SLOW % 2 == 0, "the convolution's stores ride on the first four key tiles");

constexpr unsigned KV_TILE = 2304, KV_BYTES = 64 * KV_TILE;
constexpr unsigned W_E = 0, W_F1 = 2048, W_F2 = 4096, W_KV = 8192, W_OX = W_KV + KV_BYTES, W_END = W_OX + 2097152;
constexpr unsigned W_DBG = W_END;

constexpr int CODE_LINES = 208;
constexpr unsigned kSpinCap = 1u << 20;

#define LD32(a) (*(LAS unsigned*)(size_t)(a))
#define LDF(a) (*(LAS float*)(size_t)(a))
#define LD64(a) (*(LAS u32x2*)(size_t)(a))
#define LD128(a) (*(LAS u32x4*)(size_t)(a))
#define LDF4(a) (*(LAS f32x4*)(size_t)(a))
#define BAR() asm volatile("s_waitcnt lgkmcnt(0)\n\ts_barrier" ::: "memory")

#define RT_(v) asm volatile("s_memrealtime %0\n\ts_waitcnt lgkmcnt(0)" : "=s"(v) :: "memory")
#define STAMP(i) do { if ((i) == DIAG) RT_(ti_); } while (0)
#define STAMP_INIT() unsigned long long t0_, ti_ = 0; RT_(t0_)
#define STAMP_OUT() do { if (w == 4) { unsigned long long te_, t_; RT_(te_); te_ += ti_ - t0_; \
  _Pragma("unroll 1") for (int k_ = 0; k_ < 100000; ++k_) { RT_(t_); if (t_ >= te_) break; } } } while (0)

__device__ __forceinline__ float bflo(unsigned v) { return __uint_as_float(v << 16); }
__device__ __forceinline__ float bfhi(unsigned v) { return __uint_as_float(v & 0xffff0000u); }
__device__ __forceinline__ unsigned pkbf(float a, float b) { f32x2 x = {a, b}; return __builtin_bit_cast(unsigned, __builtin_convertvector(x, bf16x2)); }
__device__ __forceinline__ unsigned pkh(float a, float b) { f32x2 x = {a, b}; return __builtin_bit_cast(unsigned, __builtin_convertvector(x, f16x2)); }
template <int CTRL> __device__ __forceinline__ unsigned dppu(unsigned v) { return __builtin_amdgcn_update_dpp(0u, v, CTRL, 0xf, 0xf, true); }
__device__ __forceinline__ unsigned umax(unsigned a, unsigned b) { return a > b ? a : b; }
__device__ __forceinline__ unsigned row_umax(unsigned v) {
  v = umax(v, dppu<0x128>(v)); v = umax(v, dppu<0x124>(v)); v = umax(v, dppu<0x122>(v)); return umax(v, dppu<0x121>(v));
}
__device__ __forceinline__ unsigned wave_umax(unsigned v) {
  v = row_umax(v);
  auto a = __builtin_amdgcn_permlane16_swap(v, v, false, false);
  v = umax(a[0], a[1]);
  auto c = __builtin_amdgcn_permlane32_swap(v, v, false, false);
  return umax(c[0], c[1]);
}
__device__ __forceinline__ float sum16(float a, float b) {
  auto r = __builtin_amdgcn_permlane16_swap(__float_as_uint(a), __float_as_uint(b), false, false);
  return __uint_as_float(r[0]) + __uint_as_float(r[1]);
}
__device__ __forceinline__ float sum32(float v) {
  auto c = __builtin_amdgcn_permlane32_swap(__float_as_uint(v), __float_as_uint(v), false, false);
  return __uint_as_float(c[0]) + __uint_as_float(c[1]);
}
__device__ __forceinline__ float sq2(unsigned v, float c) {
  return __builtin_amdgcn_fdot2_f32_bf16(__builtin_bit_cast(bf16x2, v), __builtin_bit_cast(bf16x2, v), c, false);
}
__device__ __forceinline__ unsigned opaque(unsigned v) { asm volatile("" : "+v"(v)); return v; }
struct cf { float x, y; };
__device__ __forceinline__ cf cmul(cf a, cf b) { return {a.x * b.x - a.y * b.y, a.x * b.y + a.y * b.x}; }
__device__ __forceinline__ cf cexp_(float re, float im) {
  float m = __builtin_amdgcn_exp2f(re * 1.44269504088896f);
  float f = __builtin_amdgcn_fractf(im * 0.159154943091895f);
  return {m * __builtin_amdgcn_cosf(f), m * __builtin_amdgcn_sinf(f)};
}
__device__ __forceinline__ u32x4 trfrag(unsigned a0, unsigned a1) {
  s16x4 lo = __builtin_amdgcn_ds_read_tr16_b64_v4i16((LAS s16x4*)(size_t)a0);
  s16x4 hi = __builtin_amdgcn_ds_read_tr16_b64_v4i16((LAS s16x4*)(size_t)a1);
  u32x2 l = __builtin_bit_cast(u32x2, lo), h = __builtin_bit_cast(u32x2, hi);
  return (u32x4){l[0], l[1], h[0], h[1]};
}
__device__ __forceinline__ f32x16 mfma32(u32x4 a, u32x4 b, f32x16 c) {
  return __builtin_amdgcn_mfma_f32_32x32x16_bf16(__builtin_bit_cast(bf16x8, a), __builtin_bit_cast(bf16x8, b), c, 0, 0, 0);
}
__device__ __forceinline__ f32x4 mfma16(u32x4 a, u32x4 b, f32x4 c) {
  return __builtin_amdgcn_mfma_f32_16x16x32_bf16(__builtin_bit_cast(bf16x8, a), __builtin_bit_cast(bf16x8, b), c, 0, 0, 0);
}
#define GLD1(dst, voff, base) asm volatile("global_load_dword %0, %1, %2" : "=v"(dst) : "v"(voff), "s"(base))
#define GLD4(dst, voff, base, imm) asm volatile("global_load_dwordx4 %0, %1, %2 offset:%3" : "=v"(dst) : "v"(voff), "s"(base), "i"(imm))
__device__ __forceinline__ void gdma4(unsigned voff, const void* base, unsigned lds_dst) {
  unsigned keep;
  asm volatile("s_mov_b32 %0, m0\n\ts_mov_b32 m0, %3\n\ts_nop 0\n\tglobal_load_lds_dwordx4 %1, %2\n\tglobal_load_lds_dwordx4 %1, %2 offset:1024\n\t"
               "global_load_lds_dwordx4 %1, %2 offset:2048\n\tglobal_load_lds_dwordx4 %1, %2 offset:3072\n\ts_mov_b32 m0, %0"
               : "=&s"(keep) : "v"(voff), "s"(base), "s"(lds_dst) : "memory");
}
#define DMA_PAIR "global_load_lds_dwordx4 %2, %1\n\ts_add_u32 m0, m0, 0x400\n\ts_nop 0\n\tglobal_load_lds_dwordx4 %3, %1\n\ts_add_u32 m0, m0, 0x400\n\tv_add_u32 %2, 0x4000, %2\n\tv_add_u32 %3, 0x4000, %3\n\t"
__device__ __forceinline__ void gdma_rows16(unsigned v0, unsigned v1, const void* base, unsigned lds_dst) {
  unsigned keep;
  asm volatile("s_mov_b32 %0, m0\n\ts_mov_b32 m0, %4\n\ts_nop 0\n\t" DMA_PAIR DMA_PAIR DMA_PAIR DMA_PAIR DMA_PAIR DMA_PAIR DMA_PAIR DMA_PAIR "s_mov_b32 m0, %0"
               : "=&s"(keep), "+s"(base), "+v"(v0), "+v"(v1) : "s"(lds_dst) : "memory", "scc");
}
#define DMA_X(v, o) "global_load_lds_dwordx4 " v ", %1 offset:" #o "\n\ts_add_u32 m0, m0, "
__device__ __forceinline__ void gdma_x(unsigned v0, unsigned v1, const void* base, unsigned lds_dst) {
  unsigned keep;
  asm volatile("s_mov_b32 %0, m0\n\ts_mov_b32 m0, %4\n\ts_nop 0\n\t"
               DMA_X("%2", 0) "896\n\ts_nop 0\n\t" DMA_X("%2", 128) "896\n\ts_nop 0\n\t" DMA_X("%2", 256) "896\n\ts_nop 0\n\t" DMA_X("%2", 384) "1408\n\ts_nop 0\n\t"
               DMA_X("%3", 0) "896\n\ts_nop 0\n\t" DMA_X("%3", 128) "896\n\ts_nop 0\n\t" DMA_X("%3", 256) "896\n\ts_nop 0\n\t" DMA_X("%3", 384) "0\n\t"
               "s_mov_b32 m0, %0"
               : "=&s"(keep) : "s"(base), "v"(v0), "v"(v1), "s"(lds_dst) : "memory", "scc");
}
__device__ __forceinline__ void gdma_rows4(unsigned v0, unsigned v1, const void* base, unsigned lds_dst) {
  unsigned keep;
  asm volatile("s_mov_b32 %0, m0\n\ts_mov_b32 m0, %4\n\ts_nop 0\n\t" DMA_PAIR DMA_PAIR "s_mov_b32 m0, %0"
               : "=&s"(keep), "+s"(base), "+v"(v0), "+v"(v1) : "s"(lds_dst) : "memory", "scc");
}
__device__ __forceinline__ void gdma1(unsigned voff, const void* base, unsigned lds_dst) {
  unsigned keep;
  asm volatile("s_mov_b32 %0, m0\n\ts_mov_b32 m0, %3\n\ts_nop 0\n\tglobal_load_lds_dwordx4 %1, %2\n\ts_mov_b32 m0, %0"
               : "=&s"(keep) : "v"(voff), "s"(base), "s"(lds_dst) : "memory");
}
__device__ __forceinline__ unsigned wswz(unsigned row) { return ((((row >> 1) & 3u) ^ (((row >> 3) & 1u) << 1)) << 1); }

__device__ __forceinline__ void wait4(unsigned addr, unsigned want) {
  _Pragma("unroll 1") for (unsigned spins = 0; spins < kSpinCap; ++spins) {
    u32x4 v;
    asm volatile("ds_read_b128 %0, %1\n\ts_waitcnt lgkmcnt(0)" : "=v"(v) : "v"(addr) : "memory");
    if (__builtin_amdgcn_readfirstlane(v[0] & v[1] & v[2] & v[3]) == want) break;
    __builtin_amdgcn_s_sleep(1);
  }
}
__device__ __forceinline__ void post(unsigned addr, unsigned val) {
  asm volatile("ds_write_b32 %0, %1" :: "v"(addr), "v"(val) : "memory");
}

__device__ __forceinline__ void sync4(unsigned sb, int w, int k) {
  post(sb + L_M + M_SW + k * 16 + w * 4, 1u);
  wait4(sb + L_M + M_SW + k * 16, 1u);
}

#define STR_(x) #x
#define STR(x) STR_(x)
#define VR(b, o) "v[" STR(b) "+" STR(o) "]"
#define VRR(b, o, n) "v[" STR(b) "+" STR(o) ":" STR(b) "+" STR(o) "+" STR(n) "-1]"
#define AR4(i) "a[" STR(i) ":" STR(i) "+3]"
#define A_EX(S, i) "v_exp_f32 " VR(S, i) ", " VR(S, i) "\n\t"
#define A_QK(S, r, q) "v_mfma_f32_32x32x16_bf16 " VRR(S, 0, 16) ", " AR4(80 + 12 * r) ", " AR4(64 + 4 * q) ", v[80:95]\n\t"
#define A_PV0(O, r, S) "v_smfmac_f32_32x32x32_bf16 " O ", " AR4(84 + 12 * r) ", " VRR(S, 0, 8) ", v79\n\t"
#define A_PV1(O, r, S) "v_smfmac_f32_32x32x32_bf16 " O ", " AR4(88 + 12 * r) ", " VRR(S, 8, 8) ", v79\n\t"
#define A_RD4(o, q) "v_accvgpr_read_b32 " VR(80, o) ", a[60+" #q "]\n\tv_accvgpr_read_b32 " VR(81, o) ", a[60+" #q "]\n\t" \
                    "v_accvgpr_read_b32 " VR(82, o) ", a[60+" #q "]\n\tv_accvgpr_read_b32 " VR(83, o) ", a[60+" #q "]\n\t"
#define A_SHIFT_EXACT(q) A_RD4(0, q) A_RD4(4, q) A_RD4(8, q) A_RD4(12, q) "s_nop 1\n\t"
#define A_SHIFT_NONE(q) ""
#define A_BLOCK(Sc, Sp, Op, rp, rn, qn, Oc, rc, SH, XM, X2, X3, X4, X5, X6, X7, X8, X9) \
  A_EX(Sc, 0) A_EX(Sc, 1) A_PV1(Op, rp, Sp) XM A_EX(Sc, 2) X2 A_EX(Sc, 3) X3 A_EX(Sc, 4) X4 A_EX(Sc, 5) X5 SH(qn) A_QK(Sp, rn, qn) \
  A_EX(Sc, 6) X6 A_EX(Sc, 7) X7 A_EX(Sc, 8) X8 A_EX(Sc, 9) X9 A_EX(Sc, 10) A_PV0(Oc, rc, Sc) \
  A_EX(Sc, 11) A_EX(Sc, 12) A_EX(Sc, 13) A_EX(Sc, 14) A_EX(Sc, 15)
#define A_KV0 "s_mul_i32 %[so], %[tl], 2304\n\t"
#define A_KV1 "s_add_u32 %[tl], %[tl], 1\n\t"
#define A_KV2 "s_and_b32 %[tl], %[tl], 63\n\t"
#define A_KV3(r) "buffer_load_dwordx4 " AR4(80 + 12 * r) ", %[ko], %[rk], %[so] offen sc1\n\t"
#define A_KV4(r) "buffer_load_dwordx4 " AR4(84 + 12 * r) ", %[vo], %[rk], %[so] offen sc1\n\t"
#define A_KV5(r) "buffer_load_dwordx4 " AR4(88 + 12 * r) ", %[vo], %[rk], %[so] offen offset:640 sc1\n\t"
#define A_KV(r) A_KV0 A_KV1 A_KV2 A_KV3(r) A_KV4(r) A_KV5(r)
#define A_STEP(r, rp, rn, SH, XM, Y0, Y1, Y2, W, E0, E1) \
  A_BLOCK(96, 112, "%[o3]", rp, r, 1, "%[o0]", r, SH, XM, A_KV0, A_KV1, A_KV2, "", A_KV3(rp), A_KV4(rp), A_KV5(rp), "") \
  A_BLOCK(112, 96, "%[o0]", r, r, 2, "%[o1]", r, SH, "", Y0, Y1, "", "", "", "", "", "") \
  A_BLOCK(96, 112, "%[o1]", r, r, 3, "%[o2]", r, SH, "", "", Y2, "", "", "", "", "", "") \
  A_BLOCK(112, 96, "%[o2]", r, rn, 0, "%[o3]", r, SH, "", "", "", "", "s_waitcnt vmcnt(" #W ")\n\t", E0, E1, "", "")
#define A_YSO(so) "s_movk_i32 %[so], " #so "\n\t"
#define A_YA(y) "buffer_store_dword " y ", %[yo], %[ry], %[so] offen\n\t"
#define A_YB(y) "buffer_store_dword " y ", %[yo], %[ry], %[so] offen offset:2048\n\t"
#define A_CNT0 "s_sub_u32 %[n], %[n], 2\n\t"
#define A_CNT1 "s_cmp_eq_u32 %[n], 0\n\t"
#define A_EXIT(r) A_PV1("%[o3]", r, 112)
#define A_MOV4(o) "v_mov_b32 " VR(80, o) ", %[nb]\n\tv_mov_b32 " VR(81, o) ", %[nb]\n\tv_mov_b32 " VR(82, o) ", %[nb]\n\tv_mov_b32 " VR(83, o) ", %[nb]\n\t"
#define A_ZERO(O) "v_mfma_f32_32x32x8_bf16 " O ", v[78:79], v[78:79], 0\n\t"
#define ITSTAMP
#define ITOPS
#define ITCLOB
#define A_ATT(SH) \
  "ds_read_b128 a[64:67], %[qa]\n\tds_read_b128 a[68:71], %[qa] offset:1024\n\tds_read_b128 a[72:75], %[qa] offset:2048\n\tds_read_b128 a[76:79], %[qa] offset:3072\n\t" \
  "v_mov_b32 v78, 0\n\tv_mov_b32 v79, 0\n\t" A_MOV4(0) A_MOV4(4) A_ZERO("%[o0]") A_MOV4(8) A_MOV4(12) A_ZERO("%[o1]") A_ZERO("%[o2]") A_ZERO("%[o3]") \
  "s_waitcnt vmcnt(6) lgkmcnt(0)\n\t" SH(0) A_QK(96, 0, 0) "v_mov_b32 v79, 0xdddd\n\ts_setprio 1\n\ts_nop 13\n\t" \
  "s_cmp_eq_u32 %[fl], 0\n\t" A_EX(96, 0) A_EX(96, 1) "s_cbranch_scc1 11f\n\ts_branch 12f\n\t" \
  A_STEP(0, 3, 1, SH, "12:\n\t", A_YSO(0), A_YA("%[y0]"), A_YB("%[y1]"), 8, "", "") \
  A_STEP(1, 0, 2, SH, "", A_YSO(0x1000), A_YA("%[y2]"), A_YB("%[y3]"), 10, "", "") \
  A_STEP(2, 1, 3, SH, "", A_YSO(0x4000), A_YA("%[y4]"), A_YB("%[y5]"), 12, A_CNT0, "") \
  A_STEP(3, 2, 0, SH, "", A_YSO(0x5000), A_YA("%[y6]"), A_YB("%[y7]"), 12, A_CNT0, A_CNT1) ITSTAMP "s_cbranch_scc1 43f\n\t" \
  "10:\n\t" \
  A_STEP(0, 3, 1, SH, "11:\n\t", "", "", "", 6, "", "") \
  A_STEP(1, 0, 2, SH, "", "", "", "", 6, A_CNT0, A_CNT1) "s_cbranch_scc1 41f\n\t" \
  A_STEP(2, 1, 3, SH, "", "", "", "", 6, "", "") \
  A_STEP(3, 2, 0, SH, "", "", "", "", 6, A_CNT0, A_CNT1) ITSTAMP "s_cbranch_scc0 10b\n\t" \
  "43:\n\ts_nop 0\n\t" A_EXIT(3) "s_branch 99f\n\t41:\n\ts_nop 0\n\t" A_EXIT(1) \
  "99:\n\ts_setprio 0\n\ts_nop 15"
#define C8(p, a, b, c, d, e, f, g, h) p #a, p #b, p #c, p #d, p #e, p #f, p #g, p #h
#define CLOB_V78_127 "v78", "v79", C8("v", 80, 81, 82, 83, 84, 85, 86, 87), C8("v", 88, 89, 90, 91, 92, 93, 94, 95), CLOB_V96_127
#define CLOB_V96_127 C8("v", 96, 97, 98, 99, 100, 101, 102, 103), C8("v", 104, 105, 106, 107, 108, 109, 110, 111), \
  C8("v", 112, 113, 114, 115, 116, 117, 118, 119), C8("v", 120, 121, 122, 123, 124, 125, 126, 127)
#define CLOB_V112_127 C8("v", 112, 113, 114, 115, 116, 117, 118, 119), C8("v", 120, 121, 122, 123, 124, 125, 126, 127)
#define CLOB_A0_31 C8("a", 0, 1, 2, 3, 4, 5, 6, 7), C8("a", 8, 9, 10, 11, 12, 13, 14, 15), C8("a", 16, 17, 18, 19, 20, 21, 22, 23), C8("a", 24, 25, 26, 27, 28, 29, 30, 31)
#define CLOB_A32_59 C8("a", 32, 33, 34, 35, 36, 37, 38, 39), C8("a", 40, 41, 42, 43, 44, 45, 46, 47), C8("a", 48, 49, 50, 51, 52, 53, 54, 55), "a56", "a57", "a58", "a59"
#define CLOB_A60_127 "a60", "a61", "a62", "a63", C8("a", 64, 65, 66, 67, 68, 69, 70, 71), C8("a", 72, 73, 74, 75, 76, 77, 78, 79), \
  C8("a", 80, 81, 82, 83, 84, 85, 86, 87), C8("a", 88, 89, 90, 91, 92, 93, 94, 95), \
  C8("a", 96, 97, 98, 99, 100, 101, 102, 103), C8("a", 104, 105, 106, 107, 108, 109, 110, 111), \
  C8("a", 112, 113, 114, 115, 116, 117, 118, 119), C8("a", 120, 121, 122, 123, 124, 125, 126, 127)
#define CLOB_A_ALL CLOB_A0_31, CLOB_A32_59, CLOB_A60_127
typedef int i32x4 __attribute__((ext_vector_type(4)));
__device__ __forceinline__ i32x4 make_srd(const void* p, unsigned bytes) {
  const unsigned long long a = (unsigned long long)p;
  return (i32x4){(int)(unsigned)a, (int)((unsigned)(a >> 32) & 0xffffu), (int)bytes, 0x00020000};
}

__device__ __forceinline__ unsigned kv_first(unsigned tl, unsigned ko, unsigned vo, i32x4 rk) {
  unsigned so;
  asm volatile(A_KV(0) A_KV(1) A_KV(2) : [tl] "+s"(tl), [so] "=&s"(so) : [ko] "v"(ko), [vo] "v"(vo), [rk] "s"(rk) : "memory", "scc", CLOB_A60_127);
  return tl;
}
template <bool EXACT>
__device__ __forceinline__ void attend(f32x16 (&O)[4], unsigned tl, unsigned n, unsigned fl, unsigned qa, float nb, unsigned ko, unsigned vo, i32x4 rk,
                                       unsigned yo, i32x4 ry, const unsigned (&y)[8], unsigned sa) {
  unsigned so;
#define ATT_OPS : [o0] "=&v"(O[0]), [o1] "=&v"(O[1]), [o2] "=&v"(O[2]), [o3] "=&v"(O[3]), [tl] "+s"(tl), [n] "+s"(n), [so] "=&s"(so) ITOPS \
    : [fl] "s"(fl), [qa] "v"(qa), [nb] "v"(nb), [ko] "v"(ko), [vo] "v"(vo), [rk] "s"(rk), [yo] "v"(yo), [ry] "s"(ry), [y0] "a"(y[0]), [y1] "a"(y[1]), [y2] "a"(y[2]), \
      [y3] "a"(y[3]), [y4] "a"(y[4]), [y5] "a"(y[5]), [y6] "a"(y[6]), [y7] "a"(y[7]) : "memory", "scc", CLOB_V78_127, CLOB_A60_127 ITCLOB
  if (EXACT) asm volatile(A_ATT(A_SHIFT_EXACT) ATT_OPS);
  else asm volatile(A_ATT(A_SHIFT_NONE) ATT_OPS);
}

__global__ __launch_bounds__(512, 2) void fused_kernel(
    const unsigned short* __restrict__ u, const unsigned short* __restrict__ Are, const unsigned short* __restrict__ Aim,
    const unsigned short* __restrict__ Bre, const unsigned short* __restrict__ Bim, const unsigned short* __restrict__ Cre,
    const unsigned short* __restrict__ Cim, const unsigned* __restrict__ logdt2, const unsigned* __restrict__ dskip2,
    const unsigned short* __restrict__ x, const unsigned short* __restrict__ wq, const unsigned short* __restrict__ wk,
    const unsigned short* __restrict__ wv, const unsigned short* __restrict__ wo, unsigned short* __restrict__ out,
    unsigned char* __restrict__ ws) {
  __shared__ __attribute__((aligned(128))) unsigned char smem[LDS_BYTES];
  unsigned long long pc;
  asm volatile("s_getpc_b64 %0" : "=s"(pc));
  const unsigned sb = (unsigned)(size_t)(LAS unsigned char*)smem;
  const int tid = threadIdx.x, lane = tid & 63;
  const int w = __builtin_amdgcn_readfirstlane(tid >> 6);
  const int b = blockIdx.x, xcd = b & 7, bi = b >> 3;
  const int grp = xcd * 4 + (bi >> 3), hg = bi & 7, cp = xcd * 32 + bi;
  const int h = lane >> 5, g4 = lane >> 4, l15 = lane & 15, l31 = lane & 31;
  const int qq = w & 3, kh = w >> 2;
  const int chain = hg >> 2;
  const int vl = (qq - hg - 1) & 3;
  STAMP_INIT();
  asm volatile("" :: "s"(u), "s"(Are), "s"(Aim), "s"(Bre), "s"(Bim), "s"(Cre), "s"(Cim), "s"(logdt2), "s"(dskip2), "s"(x), "s"(wq),
               "s"(wk), "s"(wv), "s"(wo), "s"(out), "s"(ws));
  STAMP(29);
  const unsigned r8 = lane >> 3, c8 = lane & 7;
  const unsigned wq0 = r8 * 1024 + hg * 128 + ((c8 ^ ((r8 & 2) << 1)) << 4);
  const int tch = (w >> 1) & 1, par = w & 1;
  unsigned T[53], d2;

  if (kh) {
    const unsigned xo = (64 * grp + 16 * qq + r8) * 1024 + ((c8 ^ (r8 >> 1)) << 4);
    if (vl != 3) asm volatile("s_barrier" ::: "memory");
    else __builtin_amdgcn_s_setprio(3);
    gdma_x(xo, (xo ^ 64) + 8192, x, sb + L_X + qq * 16384);
    gdma_x(xo + 512, (xo ^ 64) + 8704, x, sb + L_X + qq * 16384 + 8192);
    if (vl != 3) {
      gdma_rows16(wq0 + vl * 131072, wq0 + vl * 131072 + 8192, wq, sb + L_WQ + vl * 16384);
      asm volatile("s_waitcnt vmcnt(0)" ::: "memory");
    } else {
      const unsigned short* wkv = chain ? wv : wk;
#pragma unroll
      for (int i = 0; i < 4; ++i) gdma4((unsigned)(i * 4096 + lane * 16), wkv, sb + L_R2 + i * 4096);
      asm volatile("s_barrier" ::: "memory");
      unsigned E;
      asm volatile("s_load_dword %0, %1, 0x0\n\ts_waitcnt lgkmcnt(0)" : "=s"(E) : "s"(ws) : "memory");
      const unsigned tg = E + (((E * 0x9E3779B1u ^ (E >> 15) ^ 0x5bd1e995u) & 0x7fffffffu) | 0x101u);
      LD32(sb + L_M + M_TAG) = tg;
      const unsigned wa = sb + L_R2 + (8 * g4 + (l15 >> 2)) * 32 + (lane & 3) * 8;
      const unsigned xe = sb + L_X + qq * 16384 + (l15 >> 3) * 4096 + (l15 & 7) * 128 + ((g4 ^ (l15 >> 1)) << 4);
      const unsigned off = (2 * grp + (qq >> 1)) * KV_TILE + l15 * 16 +
                           (chain ? 1024 + (qq & 1) * 640 + (g4 >> 1) * 320 + (g4 & 1) * 8 : (qq & 1) * 256 + (g4 & 1) * 512 + (g4 >> 1) * 8);
      f32x4 acc, acd;
#define K_LD(s, X) "ds_read_b128 a[8*" #s ":8*" #s "+3], " X " offset:8192*(" #s "/8)+1024*((" #s "&7)/2)\n\t" \
      "ds_read_b64_tr_b16 a[8*" #s "+4:8*" #s "+5], %4 offset:1024*" #s "\n\tds_read_b64_tr_b16 a[8*" #s "+6:8*" #s "+7], %4 offset:1024*" #s "+128\n\t"
#define K_LD2(s, t) K_LD(s, "%2") K_LD(t, "%3")
#define K_MM(N, s, t, A, B, C0, C1) "s_waitcnt lgkmcnt(" #N ")\n\tv_mfma_f32_16x16x32_bf16 %0, a[8*" #s "+" #A ":8*" #s "+" #A "+3], a[8*" #s "+" #B ":8*" #s "+" #B "+3], " C0 "\n\t" \
      "v_mfma_f32_16x16x32_bf16 %1, a[8*" #t "+" #A ":8*" #t "+" #A "+3], a[8*" #t "+" #B ":8*" #t "+" #B "+3], " C1 "\n\t"
#define K_ALL(A, B) asm volatile("s_waitcnt vmcnt(0) lgkmcnt(0)\n\t" K_LD2(0, 1) K_LD2(2, 3) K_LD2(4, 5) K_MM(12, 0, 1, A, B, "0", "0") K_LD2(6, 7) K_MM(12, 2, 3, A, B, "%0", "%1") \
        K_LD2(8, 9) K_MM(12, 4, 5, A, B, "%0", "%1") K_LD2(10, 11) K_MM(12, 6, 7, A, B, "%0", "%1") K_LD2(12, 13) K_MM(12, 8, 9, A, B, "%0", "%1") \
        K_LD2(14, 15) K_MM(12, 10, 11, A, B, "%0", "%1") K_MM(6, 12, 13, A, B, "%0", "%1") K_MM(0, 14, 15, A, B, "%0", "%1") "s_nop 7" \
        : "=&v"(acc), "=&v"(acd) : "v"(xe), "v"(xe ^ 64), "v"(wa) : "memory", CLOB_A_ALL)
      if (chain) K_ALL(0, 4); else K_ALL(4, 0);
      STAMP(1);
      acc = (acc + acd) * (chain ? 1.0f : 0.25f * 1.44269504088896f);
      const u32x2 pk = {pkbf(acc[0], acc[1]), pkbf(acc[2], acc[3])};
      const __amdgpu_buffer_rsrc_t rs = __builtin_amdgcn_make_buffer_rsrc(ws + W_KV, 0, KV_BYTES, 0x00020000);
      __builtin_amdgcn_raw_buffer_store_b64(pk, rs, off, 0, 16);
      __builtin_amdgcn_raw_buffer_store_b64((u32x2){0x3f803f80u, 0x3f803f80u}, rs, (chain && l15 == 0) ? off + 256 : 0x80000000u, 0, 16);
      const float n2 = sq2(pk[0], sq2(pk[1], 0.f));
      const unsigned kn2 = row_umax(__float_as_uint(sum32(sum16(n2, n2))));
      asm volatile("s_waitcnt vmcnt(0)" ::: "memory");
      if (lane == 0) __hip_atomic_store((gu64*)(ws + W_F1) + chain * 128 + 4 * grp + qq, ((unsigned long long)tg << 32) | kn2, RLX_AGENT);
      __builtin_amdgcn_s_setprio(0);
      STAMP(2);
    }
#pragma unroll
    for (int j = 0; j < 53; ++j) asm volatile("" : "=v"(T[j]));
    asm volatile("" : "=s"(d2));
  } else {
    unsigned pAr, pAi, pBr, pBi, pCr, pCi, ld2;
    const unsigned po = (unsigned)((2 * cp + tch) * 256 + lane * 4);
    unsigned cpf = 0;
    GLD1(cpf, (unsigned)(tid < CODE_LINES ? tid * 128 : 0), pc & ~255ull);
    GLD1(pAr, po, Are); GLD1(pAi, po, Aim); GLD1(pBr, po, Bre); GLD1(pBi, po, Bim); GLD1(pCr, po, Cre); GLD1(pCi, po, Cim);
    LD32(sb + L_M + l31 * 4) = 0;
    asm volatile("s_load_dword %0, %2, %4\n\ts_load_dword %1, %3, %4\n\ts_waitcnt lgkmcnt(0)\n\ts_barrier"
                 : "=&s"(ld2), "=&s"(d2) : "s"(logdt2), "s"(dskip2), "s"(cp * 4) : "memory");
    gdma_rows4(wq0 + (384 + 32 * w) * 1024, wq0 + (392 + 32 * w) * 1024, wq, sb + L_WQ + (384 + 32 * w) * 128);
    const float ldt = tch ? bfhi(ld2) : bflo(ld2);
    const float eh = ldt * 1.44269502162933349609f, el = fmaf(ldt, 1.44269502162933349609f, -eh) + ldt * 1.925963033500011e-8f;
    const float e0 = __builtin_amdgcn_exp2f(eh);
    const float dt = bflo(pkbf(fmaf(e0, el * 0.693147180559945f, e0), 0.f));
    asm volatile("s_waitcnt vmcnt(4)" : "+v"(pAr), "+v"(pAi), "+v"(pBr), "+v"(pBi), "+v"(pCr), "+v"(pCi), "+v"(cpf) :: "memory");
    STAMP(1);
    const float ar0 = dt * bflo(pAr), ar1 = dt * bfhi(pAr);
    float lam = 0.f;
    if (__any(fmaxf(ar0, ar1) > 0.f)) lam = __uint_as_float(wave_umax(__float_as_uint(fmaxf(fmaxf(ar0, ar1), 0.f))));
    const float ar = (par ? ar1 : ar0) - lam, ai = dt * (par ? bfhi(pAi) : bflo(pAi));
    {
      cf c0 = cmul({bflo(pCr), bflo(pCi)}, {bflo(pBr), bflo(pBi)});
      cf c1 = cmul({bfhi(pCr), bfhi(pCi)}, {bfhi(pBr), bfhi(pBi)});
      int ex = (int)(wave_umax(__float_as_uint(fmaxf(fmaxf(fabsf(c0.x), fabsf(c0.y)), fmaxf(fabsf(c1.x), fabsf(c1.y))))) >> 23);
      ex = ex < 8 ? 8 : (ex > 245 ? 245 : ex);
      const float sdn = __uint_as_float((unsigned)(252 - ex) << 23), sup = __uint_as_float((unsigned)(ex + 2) << 23) * dt;
      LD64(sb + L_M + M_SCALE + tch * 8) = (u32x2){__float_as_uint(sup), __float_as_uint(lam * 1.44269504088896f)};
      cf c = par ? c1 : c0; c.x *= sdn; c.y *= sdn;
      const cf w1 = cexp_(ar, ai), W = cexp_(256.f * ar, 256.f * ai);
      const cf w2 = cmul(w1, w1), w4 = cmul(w2, w2), w6 = cmul(w4, w2);
      cf v = c;
#pragma unroll
      for (int j = 0; j < 8; ++j) { T[32 + j] = pkh(v.x, v.y); if (j < 7) v = cmul(v, w1); }
      v = cmul(c, W); T[40] = pkh(v.x, v.y);
      cf e = cmul(v, w2); T[41] = pkh(e.x, e.y);
      e = cmul(v, w4); T[42] = pkh(e.x, e.y);
      e = cmul(v, w6); T[43] = pkh(e.x, e.y);
      v = cmul(v, W); T[44] = pkh(v.x, v.y);
      e = cmul(v, w4); T[45] = pkh(e.x, e.y);
      v = cmul(v, W); T[46] = pkh(v.x, v.y);
      e = cmul(v, w4); T[47] = pkh(e.x, e.y);
#pragma unroll
      for (int j = 16; j < 21; ++j) { v = cmul(v, W); T[32 + j] = pkh(v.x, v.y); }
    }
    {
      cf p[16];
      cf g = cexp_(8.f * ar, -8.f * ai);
      p[0] = {1.f, 0.f}; p[1] = g;
      g = cmul(g, g); p[2] = g; p[3] = cmul(p[1], g);
      g = cmul(g, g);
      p[4] = g;
#pragma unroll
      for (int i = 1; i < 4; ++i) p[4 + i] = cmul(p[i], g);
      g = cmul(g, g);
      p[8] = g;
#pragma unroll
      for (int i = 1; i < 8; ++i) p[8 + i] = cmul(p[i], g);
      g = cmul(g, g);
#pragma unroll
      for (int i = 0; i < 16; ++i) {
        T[i] = pkh(p[i].x, p[i].y);
        cf q = i ? cmul(p[i], g) : g;
        T[16 + i] = pkh(q.x, q.y);
      }
    }
#pragma unroll
    for (int j = 0; j < 53; ++j) asm volatile("" : "+v"(T[j]));
    STAMP(2);
    asm volatile("s_waitcnt vmcnt(0)" ::: "memory");
  }
  STAMP(3);
  BAR();
  STAMP(4);

  const unsigned tag = __builtin_amdgcn_readfirstlane(LD32(sb + L_M + M_TAG));
  const bool watcher = kh && vl == 0;
  const unsigned fo = W_F1 + lane * 16;
  f32x16 qa16;
  {
    const unsigned A0 = sb + L_WQ + (256 * kh + 8 * h + (l15 >> 2)) * 128 + (((4 * (qq & 1) + 2 * ((g4 & 1) ^ kh) + ((lane & 3) >> 1)) ^ ((l15 >> 3) << 2)) << 4) + (lane & 1) * 8;
    const unsigned X0 = sb + L_X + ((qq & 2) + (g4 & 1)) * 16384 + kh * 8192 + (l15 >> 3) * 4096 + (l15 & 7) * 128 + ((h ^ (l15 >> 1)) << 4);
#define Q_LD(s, X) "ds_read_b128 a[64+8*(" #s "&7):64+8*(" #s "&7)+3], " X " offset:1024*(" #s "/4)\n\t" \
      "ds_read_b64_tr_b16 a[64+8*(" #s "&7)+4:64+8*(" #s "&7)+5], %1 offset:2048*" #s "\n\tds_read_b64_tr_b16 a[64+8*(" #s "&7)+6:64+8*(" #s "&7)+7], %1 offset:2048*" #s "+512\n\t"
#define Q_MM(N, s, C) "s_waitcnt lgkmcnt(" #N ")\n\tv_mfma_f32_32x32x16_bf16 %0, a[64+8*(" #s "&7)+4:64+8*(" #s "&7)+7], a[64+8*(" #s "&7):64+8*(" #s "&7)+3], " C "\n\t"
#define Q_OPS : "v"(A0), "v"(X0), "v"(X0 ^ 32), "v"(X0 ^ 64), "v"(X0 ^ 96) : "memory", CLOB_A60_127
    asm volatile(Q_LD(0, "%2") Q_LD(1, "%3") Q_LD(2, "%4") Q_LD(3, "%5") Q_LD(4, "%2") Q_MM(12, 0, "0") Q_LD(5, "%3") Q_MM(12, 1, "%0") Q_LD(6, "%4") Q_MM(12, 2, "%0")
                 Q_LD(7, "%5") Q_MM(12, 3, "%0") Q_LD(8, "%2") Q_MM(12, 4, "%0") Q_LD(9, "%3") Q_MM(12, 5, "%0") Q_LD(10, "%4") Q_MM(12, 6, "%0") Q_LD(11, "%5") Q_MM(12, 7, "%0")
                 : "=&v"(qa16) Q_OPS);
    asm volatile(Q_LD(12, "%2") Q_MM(12, 8, "%0") Q_LD(13, "%3") Q_MM(12, 9, "%0") Q_LD(14, "%4") Q_MM(12, 10, "%0") Q_LD(15, "%5") Q_MM(12, 11, "%0")
                 Q_MM(9, 12, "%0") Q_MM(6, 13, "%0") Q_MM(3, 14, "%0") Q_MM(0, 15, "%0") "s_nop 11"
                 : "+v"(qa16) Q_OPS);
  }
  STAMP(19);
  const unsigned xa = opaque(sb + L_R2 + lane * 16 + w * 2048);
  LDF4(xa) = __builtin_shufflevector(qa16, qa16, 8, 9, 10, 11);
  LDF4(xa + 1024) = __builtin_shufflevector(qa16, qa16, 12, 13, 14, 15);
#define F_LD(R) "global_load_dwordx4 v[" #R ":" #R "+3], %[fo], %[ws] sc1\n\tglobal_load_dwordx4 v[" #R "+4:" #R "+7], %[fo], %[ws] offset:1024 sc1\n\t"
#define F_CK(R) "s_waitcnt vmcnt(2)\n\tv_xor_b32 %[a], %[tg], v[" #R "+1]\n\tv_xor_b32 %[b], %[tg], v[" #R "+3]\n\tv_xor_b32 v[" #R "+5], %[tg], v[" #R "+5]\n\t" \
    "v_xor_b32 v[" #R "+7], %[tg], v[" #R "+7]\n\tv_or3_b32 %[a], %[a], %[b], v[" #R "+5]\n\tv_or_b32 %[a], %[a], v[" #R "+7]\n\t" \
    "v_max_u32 %[b], v[" #R "], v[" #R "+2]\n\tv_cmp_ne_u32 vcc, 0, %[a]\n\ts_cbranch_vccz 9f\n\t"
  if (watcher) asm volatile(F_LD(112) :: [fo] "v"(fo), [ws] "s"(ws) : "memory", CLOB_V112_127);
  BAR();
  STAMP(20);
  const i32x4 rsKV = make_srd(ws + W_KV, KV_BYTES);
  const unsigned ko = lane * 16, vo = l31 <= 16 ? 1024 + h * 320 + l31 * 16 : 0x80000000u;
  const unsigned nt = kh ? 32 - N_SLOW : N_SLOW;
  const unsigned t0 = ((kh ? 0 : 64 - 2 * N_SLOW) + (w & 1) * nt + 2 * bi) & 63;
  unsigned tl = t0;
  {
    const f32x4 q0 = __builtin_shufflevector(qa16, qa16, 0, 1, 2, 3) + LDF4(xa ^ 8192), q1 = __builtin_shufflevector(qa16, qa16, 4, 5, 6, 7) + LDF4((xa ^ 8192) + 1024);
    const u32x4 o = {pkbf(q0[0], q0[1]), pkbf(q0[2], q0[3]), pkbf(q1[0], q1[1]), pkbf(q1[2], q1[3])};
    LD128(sb + L_Q + (2 * qq + kh) * 1024 + lane * 16) = o;
    const unsigned n2 = row_umax(__float_as_uint(sum32(sq2(o[0], sq2(o[1], sq2(o[2], sq2(o[3], 0.f)))))));
    asm volatile("s_mov_b64 exec, 0x10001\n\tds_max_u32 %0, %1\n\ts_mov_b64 exec, -1" :: "v"(sb + L_M + M_QMAX), "v"(n2) : "memory");
  }
  STAMP(5);
  if (watcher) {
    unsigned fa, km, spins = kSpinCap;
    asm volatile("1:\n\t" F_LD(120) F_CK(112) F_LD(112) F_CK(120) "s_sub_u32 %[n], %[n], 1\n\ts_cbranch_scc0 1b\n\t9:"
                 : [a] "=&v"(fa), [b] "=&v"(km), [n] "+s"(spins) : [fo] "v"(fo), [ws] "s"(ws), [tg] "s"(tag) : "memory", "vcc", "scc", CLOB_V112_127);
    LD32(sb + L_M + M_KMAX) = wave_umax(km);
    if (b == 0 && lane == 0) __hip_atomic_store((gu32*)(ws + W_E), tag, RLX_AGENT);
  }
  BAR();
  STAMP(6);
  if (kh) tl = kv_first(tl, ko, vo, rsKV);

  const u32x2 qk2 = LD64(sb + L_M + M_QMAX);
  const float bound = fmaf(__builtin_amdgcn_sqrtf(__uint_as_float(qk2[0]) * __uint_as_float(qk2[1])), 1.002f, 1e-3f);
  const unsigned exact = __builtin_amdgcn_readfirstlane(bound <= 32.0f ? 0u : 1u);
  const unsigned qaddr = sb + L_Q + (qq >> 1) * 4096 + lane * 16;
  if (exact) {
    const unsigned maddr = sb + L_MX + (qq >> 1) * 512 + l31 * 4;
    if (tid < 256) LDF(sb + L_MX + tid * 4) = -3.0e38f;
    BAR();
    float m[4] = {-3.0e38f, -3.0e38f, -3.0e38f, -3.0e38f};
    const __amdgpu_buffer_rsrc_t rs = __builtin_amdgcn_make_buffer_rsrc(ws + W_KV, 0, KV_BYTES, 0x00020000);
#pragma unroll 1
    for (unsigned i = 0; i < nt; ++i) {
      u32x4 kf = __builtin_amdgcn_raw_buffer_load_b128(rs, ko, ((t0 + i) & 63) * KV_TILE, 16);
#pragma unroll
      for (int t = 0; t < 4; ++t) {
        const u32x4 q = LD128(qaddr + t * 1024);
        f32x16 s_;
        asm volatile("s_nop 1\n\tv_mfma_f32_32x32x16_bf16 %0, %1, %2, 0\n\ts_nop 15" : "=&v"(s_) : "v"(kf), "v"(q) : CLOB_A60_127);
#pragma unroll
        for (int r = 0; r < 16; ++r) m[t] = fmaxf(m[t], s_[r]);
      }
    }
#pragma unroll
    for (int t = 0; t < 4; ++t) __builtin_amdgcn_ds_fmaxf((LAS float*)(size_t)(maddr + t * 128), m[t], __ATOMIC_RELAXED, __HIP_MEMORY_SCOPE_WORKGROUP, false);
    BAR();
    asm volatile("v_accvgpr_write_b32 a60, %0\n\tv_accvgpr_write_b32 a61, %1\n\tv_accvgpr_write_b32 a62, %2\n\tv_accvgpr_write_b32 a63, %3"
                 :: "v"(-LDF(maddr)), "v"(-LDF(maddr + 128)), "v"(-LDF(maddr + 256)), "v"(-LDF(maddr + 384)) : CLOB_A60_127);
  }
  STAMP(7);

  unsigned yv[8];
  if (kh == 0) {
    __builtin_amdgcn_s_setprio(2);
    {
      const unsigned c4 = (unsigned)(par * 64 + lane) * 4, tlo = sb + L_TLO + tch * TLO_CH + c4, thi = sb + L_THI + tch * THI_CH + c4;
#pragma unroll
      for (int i = 0; i < 32; ++i) LD32(tlo + i * 528) = T[i];
#pragma unroll
      for (int i = 0; i < 21; ++i) LD32(thi + i * 528) = T[32 + i];
    }
    unsigned ug[8];
    const unsigned go = (unsigned)(tid * 8192 + cp * 4);
#define GATHER4(k, base) asm volatile("global_load_dword %0, %4, %5\n\tglobal_load_dword %1, %4, %5 offset:1024\n\tglobal_load_dword %2, %4, %5 offset:2048\n\t" \
      "global_load_dword %3, %4, %5 offset:3072" : "=&v"(ug[k]), "=&v"(ug[k + 1]), "=&v"(ug[k + 2]), "=&v"(ug[k + 3]) : "v"(go), "s"(base) : "memory")
    post(sb + L_M + M_SW + w * 4, 1u);
    GATHER4(0, u);
    wait4(sb + L_M + M_SW, 1u);
    STAMP(8);
    const unsigned KT = sb + L_P;
    {
      const int it = w & 1;
      const unsigned aa = sb + L_TLO + tch * TLO_CH + (16 * it + l15) * 528 + g4 * 16;
      const unsigned b0 = sb + L_THI + tch * THI_CH + l15 * 528 + g4 * 16, b1 = sb + L_THI + tch * THI_CH + (l15 < 4 ? 16 + l15 : 20) * 528 + g4 * 16;
      const u32x2 sc = LD64(sb + L_M + M_SCALE + tch * 8);
      f32x4 k0 = {0, 0, 0, 0}, k1 = {0, 0, 0, 0};
      asm volatile("s_nop 1" : "+v"(k0), "+v"(k1));
#pragma unroll
      for (int ks = 0; ks < 8; ++ks) {
        const u32x4 fa = LD128(aa + ks * 64), fb = LD128(b0 + ks * 64), fc = LD128(b1 + ks * 64);
        asm volatile("v_mfma_f32_16x16x32_f16 %0, %2, %3, %0\n\tv_mfma_f32_16x16x32_f16 %1, %2, %4, %1" : "+v"(k0), "+v"(k1) : "v"(fa), "v"(fb), "v"(fc) : CLOB_A60_127);
      }
      asm volatile("s_nop 7" : "+v"(k0), "+v"(k1));
      const int lb = 128 * it + 32 * g4;
      const int l0 = lb + (l15 < 8 ? l15 : l15 < 12 ? 240 + 2 * l15 : 512 + 4 * (l15 & 1) + 128 * (l15 & 2)), l1 = lb + 1024 + 256 * (l15 < 4 ? l15 : 4);
      k0 *= __uint_as_float(sc[0]); k1 *= __uint_as_float(sc[0]);
      if (__builtin_amdgcn_readfirstlane(sc[1])) {
#pragma unroll
        for (int r = 0; r < 4; ++r) {
          k0[r] *= __builtin_amdgcn_exp2f(__uint_as_float(sc[1]) * (float)(l0 + 8 * r));
          k1[r] *= __builtin_amdgcn_exp2f(__uint_as_float(sc[1]) * (float)(l1 + 8 * r));
        }
      }
#define KWR(l, k) asm volatile("ds_write_b16 %0, %1\n\tds_write_b16_d16_hi %0, %1 offset:32\n\tds_write_b16 %0, %2 offset:64\n\tds_write_b16_d16_hi %0, %2 offset:96" \
                 :: "v"(KT + 16 + (l) * 4 + 2 * tch), "v"(pkbf(k[0], k[1])), "v"(pkbf(k[2], k[3])) : "memory")
      KWR(l0, k0); KWR(l1, k1);
      if (w == 0) LD128(KT) = (u32x4){0u, 0u, 0u, 0u};
      if (tid < 248) { const int c = tid >= 124, i = tid - 124 * c; LD128(sb + L_U + c * U_CH + (i >> 2) * 80 + (i & 3) * 16) = (u32x4){0u, 0u, 0u, 0u}; }
    }
    post(sb + L_M + M_SW + 16 + w * 4, 1u);
    GATHER4(4, u + 2048);
    wait4(sb + L_M + M_SW + 16, 1u);
    STAMP(9);
#pragma unroll
    for (int k = 0; k < 2; ++k) {
      const int id = tid + 256 * k;
      unsigned d[8];
      const float f0 = (float)(4 * id - 3);
#pragma unroll
      for (int i = 0; i < 4; ++i) {
        const unsigned r = pkbf(f0 + (float)(2 * i), f0 + (float)(2 * i + 1));
        d[2 * i] = LD32(KT + 16 + 4 * (int)bflo(r));
        if (i < 3) d[2 * i + 1] = LD32(KT + 16 + 4 * (int)bfhi(r));
      }
      const unsigned ka = sb + L_KC + 224 + id * 32;
#pragma unroll
      for (int c = 0; c < 2; ++c) {
        unsigned P[6];
#pragma unroll
        for (int i = 0; i < 6; ++i) P[i] = __builtin_amdgcn_perm(d[i], d[i + 1], c ? 0x07060302u : 0x05040100u);
        LD128(ka + c * KC_CH) = (u32x4){P[2], P[0], P[3], P[1]};
        LD128(ka + c * KC_CH + 16) = (u32x4){P[4], P[2], P[5], P[3]};
      }
    }
    if (tid < 28) LD128(sb + L_KC + (tid >= 14) * (KC_CH - 224) + tid * 16) = (u32x4){0u, 0u, 0u, 0u};
    STAMP(10);
    asm volatile("s_waitcnt vmcnt(0)" : "+v"(ug[0]), "+v"(ug[1]), "+v"(ug[2]), "+v"(ug[3]), "+v"(ug[4]), "+v"(ug[5]), "+v"(ug[6]), "+v"(ug[7]) :: "memory");
    {
      u32x4 c0, c1;
#pragma unroll
      for (int m = 0; m < 4; ++m) { c0[m] = __builtin_amdgcn_perm(ug[2 * m + 1], ug[2 * m], 0x05040100u); c1[m] = __builtin_amdgcn_perm(ug[2 * m + 1], ug[2 * m], 0x07060302u); }
      const unsigned ua = sb + L_U + (31 + (tid >> 2)) * 80 + (tid & 3) * 16;
      LD128(ua) = c0; LD128(ua + U_CH) = c1;
    }
    tl = kv_first(tl, ko, vo, rsKV);
    sync4(sb, w, 2);
    STAMP(11);
    const int ch = w >> 1, pi = w & 1;
    unsigned aA = sb + L_KC + ch * KC_CH + (unsigned)(8 * (16 * (32 * pi - 1) + l31 - 8 * h + 24));
    unsigned aF = sb + L_U + ch * U_CH + (unsigned)(80 * (l31 + 31 - 16 * pi) + 16 * h);
    f32x16 y0, y1;
    unsigned cl;
    const u32x2 zz = {0u, 0u};
#define CLD(R0, R1, R2, R3, R4, R5, OA, OF) \
      "ds_read_b64 v[" #R0 "], %3 offset:" #OA "+32\n\tds_read_b64 v[" #R1 "], %3 offset:" #OA "\n\tds_read_b128 v[" #R4 "], %4 offset:" #OF "\n\t" \
      "ds_read_b128 v[" #R5 "], %4 offset:" #OF "+2560\n\tds_read_b64 v[" #R2 "], %3 offset:" #OA "+8224\n\tds_read_b64 v[" #R3 "], %3 offset:" #OA "+8192\n\t"
#define CMM(A0, A1, F0, F1) "s_waitcnt lgkmcnt(6)\n\tv_mfma_f32_32x32x16_bf16 %0, v[" #A0 "], v[" #F0 "], %0\n\t" \
      "v_mfma_f32_32x32x16_bf16 %1, v[" #A0 "], v[" #F1 "], %1\n\tv_mfma_f32_32x32x16_bf16 %1, v[" #A1 "], v[" #F0 "], %1\n\t"
    asm volatile(
        "s_setprio " STR(CONV_PRIO) "\n\ts_nop 1\n\tv_mfma_f32_32x32x8_bf16 %0, %5, %5, 0\n\tv_mfma_f32_32x32x8_bf16 %1, %5, %5, 0\n\ts_mov_b32 %2, 16\n\t"
        CLD(96:97, 98:99, 100:101, 102:103, 104:107, 108:111, 0, 32)
        "1:\n\t"
        CLD(112:113, 114:115, 116:117, 118:119, 120:123, 124:127, 128, 0)
        CMM(96:99, 100:103, 104:107, 108:111)
        "v_add_u32 %3, 0x100, %3\n\tv_add_u32 %4, 0xffffffb0, %4\n\t"
        CLD(96:97, 98:99, 100:101, 102:103, 104:107, 108:111, 0, 32)
        CMM(112:115, 116:119, 120:123, 124:127)
        "s_sub_u32 %2, %2, 1\n\ts_cmp_lg_u32 %2, 0\n\ts_cbranch_scc1 1b\n\t"
        "s_setprio 2\n\ts_waitcnt lgkmcnt(0)\n\ts_nop 15"
        : "=&v"(y0), "=&v"(y1), "=&s"(cl), "+v"(aA), "+v"(aF) : "v"(zz)
        : "memory", "scc", CLOB_V96_127, CLOB_A60_127);
    const unsigned pa = opaque(sb + L_P + lane * 16), pw = opaque(pa + w * 8192);
#pragma unroll
    for (int q = 0; q < 4; ++q) {
      LDF4(pw + q * 1024) = (f32x4){y0[4 * q], y0[4 * q + 1], y0[4 * q + 2], y0[4 * q + 3]};
      LDF4(pw + 4096 + q * 1024) = (f32x4){y1[4 * q], y1[4 * q + 1], y1[4 * q + 2], y1[4 * q + 3]};
    }
    STAMP(12);
    sync4(sb, w, 3);
    const unsigned wo0 = (128 * w + (lane >> 3)) * 1024 + hg * 128 + (((lane & 7) ^ wswz(lane >> 3)) << 4);
    gdma_rows16(wo0, (wo0 ^ 64) + 8192, wo, sb + L_WO + w * 16384);
    const float d0 = bflo(d2), d1 = bfhi(d2);
    const unsigned pr = opaque(pa + ch * 4096 + pi * 2048), uo = opaque(sb + L_U + (31 + 32 * ch + l31) * 80 + 32 * pi + 8 * h);
#pragma unroll
    for (int qi = 0; qi < 2; ++qi) {
      const unsigned o = pr + qi * 1024;
      const f32x4 ya = LDF4(o) + LDF4(o + 8192), yb = LDF4(o + 16384) + LDF4(o + 24576);
      const u32x2 ua = LD64(uo + 16 * qi), ub = LD64(uo + 16 * qi + U_CH);
#pragma unroll
      for (int e = 0; e < 4; ++e) {
        const float u0 = (e & 1) ? bfhi(ua[e >> 1]) : bflo(ua[e >> 1]), u1 = (e & 1) ? bfhi(ub[e >> 1]) : bflo(ub[e >> 1]);
        const unsigned yy = pkbf(ya[e], yb[e]), ud = pkbf(u0 * d0, u1 * d1);
        yv[4 * qi + e] = pkbf(bflo(yy) + bflo(ud), bfhi(yy) + bfhi(ud));
      }
    }
    post(sb + L_M + M_SW + 64 + w * 4, 1u);
  } else {
    gdma1((64 * bi + 16 * qq + r8) * 1024 + xcd * 128 + c8 * 16, u, sb + L_DUMP);
    gdma1((64 * bi + 16 * qq + 8 + r8) * 1024 + xcd * 128 + c8 * 16, u, sb + L_DUMP);
#pragma unroll
    for (int i = 0; i < 8; ++i) asm volatile("" : "=a"(yv[i]));
  }
  STAMP(13);

  {
    f32x16 O[4];
    const i32x4 rsY = make_srd(out, 4194304);
    const unsigned yo = (1024 * (w >> 1) + 32 * l31 + 16 * (w & 1) + 4 * h) * 2048 + cp * 4;
    if (exact) attend<true>(O, tl, nt, kh ^ 1, qaddr, -bound, ko, vo, rsKV, yo, rsY, yv, sb + L_MX + w * 128 + 88);
    else attend<false>(O, tl, nt, kh ^ 1, qaddr, -bound, ko, vo, rsKV, yo, rsY, yv, sb + L_MX + w * 128 + 88);
    STAMP(21);
    if (kh == 0) wait4(sb + L_M + M_SW + 64, 1u);
    const unsigned oa = opaque(sb + L_OA + (w ^ 4) * 4 * OA_SLOT + lane * 16), ol = opaque(sb + L_OA + (w ^ 4) * 4 * OA_SLOT + 2048 + lane * 4);
#pragma unroll
    for (int t = 0; t < 4; ++t) {
      LDF4(oa + t * OA_SLOT) = (f32x4){O[t][0], O[t][1], O[t][2], O[t][3]};
      LDF4(oa + t * OA_SLOT + 1024) = (f32x4){O[t][4], O[t][5], O[t][6], O[t][7]};
      LDF(ol + t * OA_SLOT) = O[t][8];
    }
  }
  asm volatile("s_waitcnt vmcnt(0)" ::: "memory");
  BAR();
  STAMP(14);

  int lane2 = lane; asm volatile("" : "+v"(lane2));
  {
    const int lane = lane2, g4 = lane >> 4, l15 = lane & 15;
    const __amdgpu_buffer_rsrc_t rsO = __builtin_amdgcn_make_buffer_rsrc(ws + W_OX, 0, 2097152, 0x00020000);
    {
      const unsigned s0 = opaque(sb + L_OA + (8 * kh + qq) * OA_SLOT + lane * 16), s1 = opaque(sb + L_OA + (8 * kh + qq) * OA_SLOT + 2048 + lane * 4);
      constexpr unsigned W1 = 4 * OA_SLOT, W4 = 16 * OA_SLOT;
      f32x4 o0 = (LDF4(s0) + LDF4(s0 + W1)) + (LDF4(s0 + W4) + LDF4(s0 + W4 + W1));
      f32x4 o1 = (LDF4(s0 + 1024) + LDF4(s0 + W1 + 1024)) + (LDF4(s0 + W4 + 1024) + LDF4(s0 + W4 + W1 + 1024));
      const float inv = __builtin_amdgcn_rcpf(sum32((LDF(s1) + LDF(s1 + W1)) + (LDF(s1 + W4) + LDF(s1 + W4 + W1))));
      o0 *= inv; o1 *= inv;
      u32x4 pk = {pkbf(o0[0], o0[1]), pkbf(o0[2], o0[3]), pkbf(o1[0], o1[1]), pkbf(o1[2], o1[3])};
      __builtin_amdgcn_raw_buffer_store_b128(pk, rsO, lane * 16 + ((2 * grp + kh) * 32 + 4 * hg + qq) * 1024, 0, 16);
    }
    const unsigned r0 = 256 * kh + 16 * (g4 >> 1) + 4 * (g4 & 1) + (l15 >> 2);
    const unsigned A0 = sb + L_WO + r0 * 128 + (((4 * kh + ((lane & 3) >> 1)) ^ wswz(r0)) << 4) + (lane & 1) * 8;
#define A_WF1(s, e, A, B) "ds_read_b64_tr_b16 a[16*" #s "+4*" #e ":16*" #s "+4*" #e "+1], " A " offset:4096*" #s "\n\t" \
                          "ds_read_b64_tr_b16 a[16*" #s "+4*" #e "+2:16*" #s "+4*" #e "+3], " B " offset:4096*" #s "+1024\n\t"
#define A_WF(s) A_WF1(s, 0, "%0", "%2") A_WF1(s, 1, "%1", "%3") A_WF1(s, 2, "%2", "%0") A_WF1(s, 3, "%3", "%1")
    asm volatile(A_WF(0) A_WF(1) A_WF(2) A_WF(3) "s_waitcnt vmcnt(0)\n\ts_barrier"
                 :: "v"(A0), "v"(A0 ^ 32), "v"(A0 ^ 64), "v"(A0 ^ 96) : "memory", CLOB_A_ALL);
    if (tid == 0) __hip_atomic_store((gu32*)(ws + W_F2) + grp * 32 + hg, tag, RLX_AGENT);
    STAMP(15);
    asm volatile(A_WF(4) A_WF(5) A_WF(6) A_WF(7) "s_waitcnt lgkmcnt(0)"
                 :: "v"(A0), "v"(A0 ^ 32), "v"(A0 ^ 64), "v"(A0 ^ 96) : "memory", CLOB_A_ALL);
    _Pragma("unroll 1") for (unsigned spins = 0; spins < kSpinCap; ++spins) {
      unsigned f = __hip_atomic_load((gu32*)(ws + W_F2) + grp * 32 + 4 * kh + (lane & 3), RLX_AGENT);
      if (__all(f == tag)) break;
    }
    __builtin_amdgcn_fence(__ATOMIC_ACQUIRE, "wavefront");
    STAMP(16);
    const int oo = (g4 >> 1) * 1024 + ((g4 & 1) * 32 + 16 * (qq & 1) + l15) * 16, os = ((2 * grp + (qq >> 1)) * 32 + 16 * kh) * 1024;
    u32x4 of[8];
#pragma unroll
    for (int s = 0; s < 8; ++s) of[s] = __builtin_amdgcn_raw_buffer_load_b128(rsO, oo + (s & 1) * 2048, os + (s >> 1) * 4096, 16);
    __builtin_amdgcn_sched_barrier(0);
    f32x4 acc[4];
    asm volatile("v_mfma_f32_16x16x32_bf16 %0, a[0:3], %4, 0\n\tv_mfma_f32_16x16x32_bf16 %1, a[4:7], %4, 0\n\t"
                 "v_mfma_f32_16x16x32_bf16 %2, a[8:11], %4, 0\n\tv_mfma_f32_16x16x32_bf16 %3, a[12:15], %4, 0"
                 : "=&v"(acc[0]), "=&v"(acc[1]), "=&v"(acc[2]), "=&v"(acc[3]) : "v"(of[0]) : CLOB_A_ALL);
#define OPJ(s, T) asm volatile("v_mfma_f32_16x16x32_bf16 %0, " AR4(16 * s) ", %4, %0\n\tv_mfma_f32_16x16x32_bf16 %1, " AR4(16 * s + 4) ", %4, %1\n\t" \
                 "v_mfma_f32_16x16x32_bf16 %2, " AR4(16 * s + 8) ", %4, %2\n\tv_mfma_f32_16x16x32_bf16 %3, " AR4(16 * s + 12) ", %4, %3" T \
                 : "+v"(acc[0]), "+v"(acc[1]), "+v"(acc[2]), "+v"(acc[3]) : "v"(of[s]) : CLOB_A_ALL)
    OPJ(1, ""); OPJ(2, ""); OPJ(3, ""); OPJ(4, ""); OPJ(5, ""); OPJ(6, ""); OPJ(7, "\n\ts_nop 7");
    STAMP(17);
    const unsigned xa = opaque(sb + L_U + lane * 16 + w * 2048), xb = opaque(sb + L_U + lane * 16 + (w ^ 4) * 2048);
    LDF4(xa) = acc[2];
    LDF4(xa + 1024) = acc[3];
    BAR();
    const f32x4 q0 = acc[0] + LDF4(xb), q1 = acc[1] + LDF4(xb + 1024);
    auto sa = __builtin_amdgcn_permlane16_swap(pkbf(q0[0], q0[1]), pkbf(q1[0], q1[1]), false, false);
    auto sc = __builtin_amdgcn_permlane16_swap(pkbf(q0[2], q0[3]), pkbf(q1[2], q1[3]), false, false);
    u32x4 v = {sa[0], sc[0], sa[1], sc[1]};
    *(u32x4*)(out + (64 * grp + 16 * qq + l15) * 1024 + 512 + 64 * hg + 32 * kh + 16 * (g4 & 1) + 8 * (g4 >> 1)) = v;
    STAMP(18);
    STAMP_OUT();
  }
}

extern "C" void kernel_launch(void* const* d_in, const int* in_sizes, int n_in, void* d_out, int out_size, void* d_ws,
                              size_t ws_size, hipStream_t stream) {
  typedef const unsigned short* P;
  fused_kernel<<<256, 512, 0, stream>>>((P)d_in[0], (P)d_in[1], (P)d_in[2], (P)d_in[3], (P)d_in[4], (P)d_in[5], (P)d_in[6],
                                        (const unsigned*)d_in[7], (const unsigned*)d_in[8], (P)d_in[9], (P)d_in[10], (P)d_in[11],
                                        (P)d_in[12], (P)d_in[13], (unsigned short*)d_out, (unsigned char*)d_ws);
}
```

```cpp
#include <hip/hip_runtime.h>
#include <hip/hip_bf16.h>
#include <stdint.h>

typedef float f32x16 __attribute__((ext_vector_type(16)));
typedef float f32x8 __attribute__((ext_vector_type(8)));
typedef float f32x4 __attribute__((ext_vector_type(4)));
typedef float f32x2 __attribute__((ext_vector_type(2)));
typedef __bf16 bf16x8 __attribute__((ext_vector_type(8)));
typedef __bf16 bf16x2 __attribute__((ext_vector_type(2)));
typedef _Float16 f16x8 __attribute__((ext_vector_type(8)));
typedef _Float16 f16x2 __attribute__((ext_vector_type(2)));
typedef short s16x4 __attribute__((ext_vector_type(4)));
typedef unsigned u32x4 __attribute__((ext_vector_type(4)));
typedef unsigned u32x2 __attribute__((ext_vector_type(2)));
#define LAS __attribute__((address_space(3)))
#define GAS __attribute__((address_space(1)))
typedef GAS unsigned gu32;
typedef GAS unsigned long long gu64;
#define RLX_AGENT __ATOMIC_RELAXED, __HIP_MEMORY_SCOPE_AGENT

constexpr unsigned L_X = 0;
constexpr unsigned L_TLO = 0, TLO_CH = 16896, L_THI = 33792, THI_CH = 11088;
constexpr unsigned L_KC = 0, KC_CH = 16640;
constexpr unsigned L_WO = 0;
constexpr unsigned L_B = 65536;
constexpr unsigned L_WQ = L_B;
constexpr unsigned L_R2 = L_B + 65536;
constexpr unsigned L_OA = L_B, OA_SLOT = 2304;
constexpr unsigned L_P = L_B + 40960;
constexpr unsigned L_U = L_B + 73728, U_CH = 7600;
constexpr unsigned L_Q = L_B + 88928;
constexpr unsigned L_MX = L_Q + 8192;
constexpr unsigned L_M = L_MX + 1024;
constexpr unsigned LDS_BYTES = L_M + 128;
static_assert(LDS_BYTES <= 163840, "lds");
constexpr unsigned M_TAG = 0, M_RDY = 4, M_QMAX = 8, M_KMAX = 12, M_SCALE = 16  , M_SW = 32  , M_AB = 112  ;
#define CONV_PRIO 3
#define CONV_N 16
#define NYST 8
#define YSLEEP 12
#define GSLEEP 10
#define CNOP 7
#define CPAD "s_nop " STR(CNOP) "\n\t"
#define PSW 2
#define NREP 8
#define WSLEEP 16
#define SSM_PRIO 3

constexpr unsigned KV_TILE = 2304, KV_BYTES = 64 * KV_TILE;
constexpr unsigned W_E = 0, W_F2 = 4096, W_F1 = 8192  , W_KV = W_F1 + 65536, W_OX = W_KV + KV_BYTES, W_END = W_OX + 2097152;
constexpr unsigned W_DBG = W_END;

constexpr int CODE_LINES = 150;
constexpr unsigned kSpinCap = 1u << 20;

#define LD32(a) (*(LAS unsigned*)(size_t)(a))
#define LDF(a) (*(LAS float*)(size_t)(a))
#define LD64(a) (*(LAS u32x2*)(size_t)(a))
#define LD128(a) (*(LAS u32x4*)(size_t)(a))
#define LDF4(a) (*(LAS f32x4*)(size_t)(a))
#define BAR() asm volatile("s_waitcnt lgkmcnt(0)\n\ts_barrier" ::: "memory")

#define RSTAMP(i)
#define TS(k)
#define TSTAMP_OUT()
#define TSCLOB
#define STAMP(i)
#define STAMP_INIT()
#define STAMP_OUT()

__device__ __forceinline__ float bflo(unsigned v) { return __uint_as_float(v << 16); }
__device__ __forceinline__ float bfhi(unsigned v) { return __uint_as_float(v & 0xffff0000u); }
__device__ __forceinline__ unsigned pkbf(float a, float b) { f32x2 x = {a, b}; return __builtin_bit_cast(unsigned, __builtin_convertvector(x, bf16x2)); }
__device__ __forceinline__ unsigned pkh(float a, float b) { f32x2 x = {a, b}; return __builtin_bit_cast(unsigned, __builtin_convertvector(x, f16x2)); }
template <int CTRL> __device__ __forceinline__ unsigned dppu(unsigned v) { return __builtin_amdgcn_update_dpp(0u, v, CTRL, 0xf, 0xf, true); }
__device__ __forceinline__ unsigned umax(unsigned a, unsigned b) { return a > b ? a : b; }
__device__ __forceinline__ unsigned row_umax(unsigned v) {
  v = umax(v, dppu<0x128>(v)); v = umax(v, dppu<0x124>(v)); v = umax(v, dppu<0x122>(v)); return umax(v, dppu<0x121>(v));
}
__device__ __forceinline__ unsigned wave_umax(unsigned v) {
  v = row_umax(v);
  auto a = __builtin_amdgcn_permlane16_swap(v, v, false, false);
  v = umax(a[0], a[1]);
  auto c = __builtin_amdgcn_permlane32_swap(v, v, false, false);
  return umax(c[0], c[1]);
}
__device__ __forceinline__ float sum16(float a, float b) {
  auto r = __builtin_amdgcn_permlane16_swap(__float_as_uint(a), __float_as_uint(b), false, false);
  return __uint_as_float(r[0]) + __uint_as_float(r[1]);
}
__device__ __forceinline__ float sum32(float v) {
  auto c = __builtin_amdgcn_permlane32_swap(__float_as_uint(v), __float_as_uint(v), false, false);
  return __uint_as_float(c[0]) + __uint_as_float(c[1]);
}
__device__ __forceinline__ float sq2(unsigned v, float c) {
  return __builtin_amdgcn_fdot2_f32_bf16(__builtin_bit_cast(bf16x2, v), __builtin_bit_cast(bf16x2, v), c, false);
}
__device__ __forceinline__ unsigned opaque(unsigned v) { asm volatile("" : "+v"(v)); return v; }
__device__ __forceinline__ int lane_id() {
  int l;
  asm volatile("v_mbcnt_lo_u32_b32 %0, -1, 0\n\tv_mbcnt_hi_u32_b32 %0, -1, %0" : "=v"(l));
  __builtin_assume(l >= 0 && l < 64);
  return l;
}
struct cf { float x, y; };
__device__ __forceinline__ cf cmul(cf a, cf b) { return {a.x * b.x - a.y * b.y, a.x * b.y + a.y * b.x}; }
__device__ __forceinline__ cf cexp_(float re, float im) {
  float m = __builtin_amdgcn_exp2f(re * 1.44269504088896f);
  float f = __builtin_amdgcn_fractf(im * 0.159154943091895f);
  return {m * __builtin_amdgcn_cosf(f), m * __builtin_amdgcn_sinf(f)};
}
#define GLD1(dst, voff, base) asm volatile("global_load_dword %0, %1, %2" : "=v"(dst) : "v"(voff), "s"(base))
__device__ __forceinline__ void gdma4(unsigned voff, const void* base, unsigned lds_dst) {
  unsigned keep;
  asm volatile("s_mov_b32 %0, m0\n\ts_mov_b32 m0, %3\n\ts_nop 0\n\tglobal_load_lds_dwordx4 %1, %2\n\tglobal_load_lds_dwordx4 %1, %2 offset:1024\n\t"
               "global_load_lds_dwordx4 %1, %2 offset:2048\n\tglobal_load_lds_dwordx4 %1, %2 offset:3072\n\ts_mov_b32 m0, %0"
               : "=&s"(keep) : "v"(voff), "s"(base), "s"(lds_dst) : "memory");
}
#define DMA_PAIR "global_load_lds_dwordx4 %2, %1\n\ts_add_u32 m0, m0, 0x400\n\ts_nop 0\n\tglobal_load_lds_dwordx4 %3, %1\n\ts_add_u32 m0, m0, 0x400\n\tv_add_u32 %2, 0x4000, %2\n\tv_add_u32 %3, 0x4000, %3\n\t"
__device__ __forceinline__ void gdma_rows16(unsigned v0, unsigned v1, const void* base, unsigned lds_dst) {
  unsigned keep;
  asm volatile("s_mov_b32 %0, m0\n\ts_mov_b32 m0, %4\n\ts_nop 0\n\t" DMA_PAIR DMA_PAIR DMA_PAIR DMA_PAIR DMA_PAIR DMA_PAIR DMA_PAIR DMA_PAIR "s_mov_b32 m0, %0"
               : "=&s"(keep), "+s"(base), "+v"(v0), "+v"(v1) : "s"(lds_dst) : "memory", "scc");
}
#define DMA_X(v, o) "global_load_lds_dwordx4 " v ", %1 offset:" #o "\n\ts_add_u32 m0, m0, "
__device__ __forceinline__ void gdma_x(unsigned v0, unsigned v1, const void* base, unsigned lds_dst) {
  unsigned keep;
  asm volatile("s_mov_b32 %0, m0\n\ts_mov_b32 m0, %4\n\ts_nop 0\n\t"
               DMA_X("%2", 0) "896\n\ts_nop 0\n\t" DMA_X("%2", 128) "896\n\ts_nop 0\n\t" DMA_X("%2", 256) "896\n\ts_nop 0\n\t" DMA_X("%2", 384) "1408\n\ts_nop 0\n\t"
               DMA_X("%3", 0) "896\n\ts_nop 0\n\t" DMA_X("%3", 128) "896\n\ts_nop 0\n\t" DMA_X("%3", 256) "896\n\ts_nop 0\n\t" DMA_X("%3", 384) "0\n\t"
               "s_mov_b32 m0, %0"
               : "=&s"(keep) : "s"(base), "v"(v0), "v"(v1), "s"(lds_dst) : "memory", "scc");
}
__device__ __forceinline__ unsigned wswz(unsigned row) { return ((((row >> 1) & 3u) ^ (((row >> 3) & 1u) << 1)) << 1); }

__device__ __forceinline__ void wait4(unsigned addr, unsigned want) {
  _Pragma("unroll 1") for (unsigned spins = 0; spins < kSpinCap; ++spins) {
    u32x4 v;
    asm volatile("ds_read_b128 %0, %1\n\ts_waitcnt lgkmcnt(0)" : "=v"(v) : "v"(addr) : "memory");
    if (__builtin_amdgcn_readfirstlane(v[0] & v[1] & v[2] & v[3]) == want) break;
    __builtin_amdgcn_s_sleep(1);
  }
}
__device__ __forceinline__ void post(unsigned addr, unsigned val) {
  asm volatile("ds_write_b32 %0, %1" :: "v"(addr), "v"(val) : "memory");
}

__device__ __forceinline__ void sync4(unsigned sb, int w, int k) {
  post(sb + L_M + M_SW + k * 16 + w * 4, 1u);
  wait4(sb + L_M + M_SW + k * 16, 1u);
}

__device__ __forceinline__ void arrive8(unsigned addr) {
  asm volatile("s_mov_b64 exec, 1\n\tds_add_u32 %0, %1\n\ts_mov_b64 exec, -1" :: "v"(addr), "v"(1u) : "memory");
}
__device__ __forceinline__ void wait8(unsigned addr, unsigned count = 8u) {
  _Pragma("unroll 1") for (unsigned spins = 0; spins < kSpinCap; ++spins) {
    unsigned v;
    asm volatile("ds_read_b32 %0, %1\n\ts_waitcnt lgkmcnt(0)" : "=v"(v) : "v"(addr) : "memory");
    if (__builtin_amdgcn_readfirstlane(v) >= count) break;
  }
}

#define STR_(x) #x
#define STR(x) STR_(x)
#define VR(b, o) "v[" STR(b) "+" STR(o) "]"
#define VRR(b, o, n) "v[" STR(b) "+" STR(o) ":" STR(b) "+" STR(o) "+" STR(n) "-1]"
#define AR4(i) "a[" STR(i) ":" STR(i) "+3]"
#define C4(p, a, b, c, d) p #a, p #b, p #c, p #d
#define C8(p, a, b, c, d, e, f, g, h) p #a, p #b, p #c, p #d, p #e, p #f, p #g, p #h
#define CLOB_V36_83 C4("v", 36, 37, 38, 39), C8("v", 40, 41, 42, 43, 44, 45, 46, 47), C8("v", 48, 49, 50, 51, 52, 53, 54, 55), C8("v", 56, 57, 58, 59, 60, 61, 62, 63), \
  C8("v", 64, 65, 66, 67, 68, 69, 70, 71), C8("v", 72, 73, 74, 75, 76, 77, 78, 79), C4("v", 80, 81, 82, 83)
#define CLOB_V4_83 C4("v", 4, 5, 6, 7), C8("v", 8, 9, 10, 11, 12, 13, 14, 15), C8("v", 16, 17, 18, 19, 20, 21, 22, 23), C8("v", 24, 25, 26, 27, 28, 29, 30, 31), \
  C4("v", 32, 33, 34, 35), CLOB_V36_83
#define CLOB_A4_47 C4("a", 4, 5, 6, 7), C8("a", 8, 9, 10, 11, 12, 13, 14, 15), C8("a", 16, 17, 18, 19, 20, 21, 22, 23), C8("a", 24, 25, 26, 27, 28, 29, 30, 31), \
  C8("a", 32, 33, 34, 35, 36, 37, 38, 39), C8("a", 40, 41, 42, 43, 44, 45, 46, 47)
typedef int i32x4 __attribute__((ext_vector_type(4)));
__device__ __forceinline__ i32x4 make_srd(const void* p, unsigned bytes) {
  const unsigned long long a = (unsigned long long)p;
  return (i32x4){(int)(unsigned)a, (int)((unsigned)(a >> 32) & 0xffffu), (int)bytes, 0x00020000};
}

#define A_EX(S, i) "v_exp_f32 " VR(S, i) ", " VR(S, i) "\n\t"
#define A_QK(S, p, q) "v_mfma_f32_32x32x16_bf16 " VRR(S, 0, 16) ", " AR4(4 + 4 * p) ", " AR4(28 + 4 * q) ", v[68:83]\n\t"
#define A_PV0(o, p, S) "v_smfmac_f32_32x32x32_bf16 " VRR(4, 16 * o, 16) ", " AR4(12 + 4 * p) ", " VRR(S, 0, 8) ", v116\n\t"
#define A_PV1(o, p, S) "v_smfmac_f32_32x32x32_bf16 " VRR(4, 16 * o, 16) ", " AR4(20 + 4 * p) ", " VRR(S, 8, 8) ", v116\n\t"
#define A_RD4(o, q) "v_accvgpr_read_b32 " VR(68, o) ", a[44+" #q "]\n\tv_accvgpr_read_b32 " VR(69, o) ", a[44+" #q "]\n\t" \
                    "v_accvgpr_read_b32 " VR(70, o) ", a[44+" #q "]\n\tv_accvgpr_read_b32 " VR(71, o) ", a[44+" #q "]\n\t"
#define A_SHIFT_EXACT(q) A_RD4(0, q) A_RD4(4, q) A_RD4(8, q) A_RD4(12, q) "s_nop 1\n\t"
#define A_SHIFT_NONE(q) ""
#define A_BLOCK(Sc, Sp, op, pp, pn, qn, oc, pc, SH, XM, X3, X5, X7, X10, X12) \
  A_EX(Sc, 0) A_EX(Sc, 1) A_PV1(op, pp, Sp) XM A_EX(Sc, 2) A_EX(Sc, 3) X3 A_EX(Sc, 4) A_EX(Sc, 5) X5 SH(qn) A_QK(Sp, pn, qn) \
  A_EX(Sc, 6) A_EX(Sc, 7) X7 A_EX(Sc, 8) A_EX(Sc, 9) A_EX(Sc, 10) X10 A_PV0(oc, pc, Sc) \
  A_EX(Sc, 11) A_EX(Sc, 12) X12 A_EX(Sc, 13) A_EX(Sc, 14) A_EX(Sc, 15)
#define A_LK(p, so) "buffer_load_dwordx4 " AR4(4 + 4 * p) ", %[ko], %[rk], " so " offen sc1\n\t"
#define A_LV0(p, so) "buffer_load_dwordx4 " AR4(12 + 4 * p) ", %[vo], %[rk], " so " offen sc1\n\t"
#define A_LV1(p, so) "buffer_load_dwordx4 " AR4(20 + 4 * p) ", %[vo], %[rk], " so " offen offset:640 sc1\n\t"
#define A_NEXT "s_add_u32 %[tl], %[tl], 1\n\ts_and_b32 %[tl], %[tl], 63\n\t"
#define A_W3 "s_waitcnt vmcnt(3)\n\t"
#define A_STEP(p, pq, SH, XM, E) \
  A_BLOCK(84, 100, 3, pq, p, 1, 0, p, SH, XM, A_LV1(pq, "%[sa]"), "", "", A_W3, "") \
  A_BLOCK(100, 84, 0, p, p, 2, 1, p, SH, "", "s_mov_b32 %[sa], %[sb]\n\t", "", A_NEXT, "", "") \
  A_BLOCK(84, 100, 1, p, p, 3, 2, p, SH, "", "", "", A_LK(p, "%[sb]"), "", "") \
  A_BLOCK(100, 84, 2, p, pq, 0, 3, p, SH, "", "", A_W3, E, "", A_LV0(p, "%[sb]") "s_mul_i32 %[sb], %[tl], 2304\n\t")
#define A_MOV4(o) "v_mov_b32 " VR(68, o) ", %[nb]\n\tv_mov_b32 " VR(69, o) ", %[nb]\n\tv_mov_b32 " VR(70, o) ", %[nb]\n\tv_mov_b32 " VR(71, o) ", %[nb]\n\t"
#define A_ZERO(o) "v_mfma_f32_32x32x8_bf16 " VRR(4, 16 * o, 16) ", v[116:117], v[116:117], 0\n\t"
#define A_FLUSH(o) "ds_write_b128 v118, " VRR(4, 16 * o, 4) " offset:2304*" #o "\n\tds_write_b128 v118, " VRR(8, 16 * o, 4) " offset:2304*" #o "+1024\n\t" \
                   "ds_write_b32 v119, " VR(12, 16 * o) " offset:2304*" #o "+2048\n\t"
#define A_FIRST \
  "s_mul_i32 %[sb], %[tl], 2304\n\t" A_NEXT "s_mul_i32 %[sa], %[tl], 2304\n\t" A_LK(0, "%[sb]") A_LV0(0, "%[sb]") A_LV1(0, "%[sb]") A_LK(1, "%[sa]") A_LV0(1, "%[sa]")
#define A_ATT(SH) \
  A_NEXT "s_mul_i32 %[sa], %[tl], 2304\n\t" A_NEXT "s_mul_i32 %[sb], %[tl], 2304\n\t" \
  "v_add_u32 v118, %[qb], %[ko]\n\tv_mov_b32 v116, 0\n\tv_mov_b32 v117, 0\n\t" \
  "ds_read_b128 a[28:31], v118\n\tds_read_b128 a[32:35], v118 offset:1024\n\tds_read_b128 a[36:39], v118 offset:2048\n\tds_read_b128 a[40:43], v118 offset:3072\n\t" \
  A_MOV4(0) A_ZERO(0) A_MOV4(4) A_ZERO(1) A_MOV4(8) A_ZERO(2) A_MOV4(12) A_ZERO(3) \
  "v_lshrrev_b32 v119, 2, %[ko]\n\tv_add_u32 v118, %[ob], %[ko]\n\tv_add_u32 v119, %[ob], v119\n\t" \
  "s_waitcnt vmcnt(4) lgkmcnt(0)\n\t" SH(0) A_QK(84, 0, 0) "v_mov_b32 v116, 0xdddd\n\ts_setprio 1\n\ts_cmp_eq_u32 %[sw], 0\n\ts_cbranch_scc1 14f\n\ts_setprio 2\n\t14:\n\ts_nop 13\n\t" \
  A_EX(84, 0) A_EX(84, 1) "s_branch 11f\n\t" \
  "10:\n\t" \
  A_STEP(0, 1, SH, "11:\n\t", "") \
  A_STEP(1, 0, SH, "", "s_sub_u32 %[n], %[n], 1\n\t") "s_cmp_lg_u32 %[n], %[sw]\n\ts_cbranch_scc1 10b\n\ts_setprio 0\n\ts_mov_b32 %[sw], 0\n\ts_cmp_lg_u32 %[n], 0\n\ts_cbranch_scc1 10b\n\t" \
  A_PV1(3, 1, 100) "s_setprio 0\n\tv_mov_b32 v117, %[pf]\n\ts_mov_b32 %[n], 0x100000\n\t" \
  "12:\n\tds_read_b128 v[84:87], v117\n\ts_waitcnt lgkmcnt(0)\n\tv_and_b32 v84, v84, v85\n\tv_and_b32 v86, v86, v87\n\tv_and_b32 v84, v84, v86\n\t" \
  "s_sub_u32 %[n], %[n], 1\n\ts_cbranch_scc1 13f\n\tv_cmp_eq_u32 vcc, 0, v84\n\ts_cbranch_vccnz 12b\n\t13:\n\t" \
  A_FLUSH(0) A_FLUSH(1) A_FLUSH(2) "s_nop 3\n\t" A_FLUSH(3)

#pragma clang diagnostic ignored "-Winline-asm"
__global__ __launch_bounds__(768, 3) void fused_kernel(
    const unsigned short* __restrict__ u, const unsigned short* __restrict__ Are, const unsigned short* __restrict__ Aim,
    const unsigned short* __restrict__ Bre, const unsigned short* __restrict__ Bim, const unsigned short* __restrict__ Cre,
    const unsigned short* __restrict__ Cim, const unsigned* __restrict__ logdt2, const unsigned* __restrict__ dskip2,
    const unsigned short* __restrict__ x, const unsigned short* __restrict__ wq, const unsigned short* __restrict__ wk,
    const unsigned short* __restrict__ wv, const unsigned short* __restrict__ wo, unsigned short* __restrict__ out,
    unsigned char* __restrict__ ws) {
  __shared__ __attribute__((aligned(128))) unsigned char smem[LDS_BYTES];
  unsigned long long pc;
  asm volatile("s_getpc_b64 %0" : "=s"(pc) :: "v119", "a47");
  const unsigned sb = (unsigned)(size_t)(LAS unsigned char*)smem;
  const int w = __builtin_amdgcn_readfirstlane(threadIdx.x >> 6);
  const int b = blockIdx.x, xcd = b & 7, bi = b >> 3;
  const int grp = xcd * 4 + (bi >> 3), hg = bi & 7, cp = xcd * 32 + bi;
#define LANE_VARS() const int lane = lane_id(), h = lane >> 5, g4 = lane >> 4, l15 = lane & 15, l31 = lane & 31; \
  const unsigned r8 = lane >> 3, c8 = lane & 7; (void)h; (void)g4; (void)l15; (void)l31; (void)r8; (void)c8
  STAMP_INIT();
  asm volatile("" :: "s"(u), "s"(Are), "s"(Aim), "s"(Bre), "s"(Bim), "s"(Cre), "s"(Cim), "s"(logdt2), "s"(dskip2), "s"(x), "s"(wq),
               "s"(wk), "s"(wv), "s"(wo), "s"(out), "s"(ws));

  if (w >= 8) {
    LANE_VARS();
    const int j = w - 8, tid = j * 64 + lane, tch = j >> 1, par = j & 1;
    unsigned pAr, pAi, pBr, pBi, pCr, pCi, ld2, d2, cpf = 0;
    const unsigned po = (unsigned)((2 * cp + tch) * 256 + lane * 4);
    GLD1(pAr, po, Are); GLD1(pAi, po, Aim); GLD1(pBr, po, Bre); GLD1(pBi, po, Bim); GLD1(pCr, po, Cre); GLD1(pCi, po, Cim);
    LD32(sb + L_M + l31 * 4) = 0;
    asm volatile("s_load_dword %0, %2, %4\n\ts_load_dword %1, %3, %4\n\ts_waitcnt lgkmcnt(0)\n\ts_barrier"
                 : "=&s"(ld2), "=&s"(d2) : "s"(logdt2), "s"(dskip2), "s"(cp * 4) : "memory");
    const float ldt = tch ? bfhi(ld2) : bflo(ld2);
    const float eh = ldt * 1.44269502162933349609f, el = fmaf(ldt, 1.44269502162933349609f, -eh) + ldt * 1.925963033500011e-8f;
    const float e0 = __builtin_amdgcn_exp2f(eh);
    const float dt = bflo(pkbf(fmaf(e0, el * 0.693147180559945f, e0), 0.f));
    asm volatile("s_waitcnt vmcnt(0)" : "+v"(pAr), "+v"(pAi), "+v"(pBr), "+v"(pBi), "+v"(pCr), "+v"(pCi), "+v"(cpf) :: "memory");
    STAMP(1);
    const float ar0 = dt * bflo(pAr), ar1 = dt * bfhi(pAr);
    float lam = 0.f;
    if (__any(fmaxf(ar0, ar1) > 0.f)) lam = __uint_as_float(wave_umax(__float_as_uint(fmaxf(fmaxf(ar0, ar1), 0.f))));
    const float ar = (par ? ar1 : ar0) - lam, ai = dt * (par ? bfhi(pAi) : bflo(pAi));
    unsigned T[21];
    {
      cf c0 = cmul({bflo(pCr), bflo(pCi)}, {bflo(pBr), bflo(pBi)});
      cf c1 = cmul({bfhi(pCr), bfhi(pCi)}, {bfhi(pBr), bfhi(pBi)});
      int ex = (int)(wave_umax(__float_as_uint(fmaxf(fmaxf(fabsf(c0.x), fabsf(c0.y)), fmaxf(fabsf(c1.x), fabsf(c1.y))))) >> 23);
      ex = ex < 8 ? 8 : (ex > 245 ? 245 : ex);
      const float sdn = __uint_as_float((unsigned)(252 - ex) << 23), sup = __uint_as_float((unsigned)(ex + 2) << 23) * dt;
      LD64(sb + L_M + M_SCALE + tch * 8) = (u32x2){__float_as_uint(sup), __float_as_uint(lam * 1.44269504088896f)};
      cf c = par ? c1 : c0; c.x *= sdn; c.y *= sdn;
      const cf w1 = cexp_(ar, ai), W = cexp_(256.f * ar, 256.f * ai);
      const cf w2 = cmul(w1, w1), w4 = cmul(w2, w2), w6 = cmul(w4, w2);
      cf v = c;
#pragma unroll
      for (int k = 0; k < 8; ++k) { T[k] = pkh(v.x, v.y); if (k < 7) v = cmul(v, w1); }
      v = cmul(c, W); T[8] = pkh(v.x, v.y);
      cf e = cmul(v, w2); T[9] = pkh(e.x, e.y);
      e = cmul(v, w4); T[10] = pkh(e.x, e.y);
      e = cmul(v, w6); T[11] = pkh(e.x, e.y);
      v = cmul(v, W); T[12] = pkh(v.x, v.y);
      e = cmul(v, w4); T[13] = pkh(e.x, e.y);
      v = cmul(v, W); T[14] = pkh(v.x, v.y);
      e = cmul(v, w4); T[15] = pkh(e.x, e.y);
#pragma unroll
      for (int k = 16; k < 21; ++k) { v = cmul(v, W); T[k] = pkh(v.x, v.y); }
    }
    {
      cf p[16];
      cf g = cexp_(8.f * ar, -8.f * ai);
      p[0] = {1.f, 0.f}; p[1] = g;
      g = cmul(g, g); p[2] = g; p[3] = cmul(p[1], g);
      g = cmul(g, g);
      p[4] = g;
#pragma unroll
      for (int i = 1; i < 4; ++i) p[4 + i] = cmul(p[i], g);
      g = cmul(g, g);
      p[8] = g;
#pragma unroll
      for (int i = 1; i < 8; ++i) p[8 + i] = cmul(p[i], g);
      g = cmul(g, g);
#pragma unroll
      for (int i = 0; i < 16; ++i) {
        const cf q = i ? cmul(p[i], g) : g;
        asm volatile("v_cvt_pk_f16_f32 v[84+%c4], %0, %1\n\tv_cvt_pk_f16_f32 v[100+%c4], %2, %3" :: "v"(p[i].x), "v"(p[i].y), "v"(q.x), "v"(q.y), "i"(i));
      }
    }
#pragma unroll
    for (int k = 0; k < 21; ++k) asm volatile("" : "+v"(T[k]));
    STAMP(2);
    BAR();
    const unsigned go = (unsigned)(tid * 8192 + cp * 4);
#define GATHER(i, R) asm volatile("global_load_dword v" #R ", %0, %1 offset:1024*(" #i "&3)" :: "v"(go), "s"(u + (i >> 2) * 2048) : "memory")
    const unsigned tag = __builtin_amdgcn_readfirstlane(LD32(sb + L_M + M_TAG));
    if (j == 0) {
      u32x4 f0, f1, g0, g1;
      unsigned fa, km, spins = kSpinCap;
      const unsigned fo = W_F1 + (b & (NREP - 1)) * 2048 + lane * 16;
      __builtin_amdgcn_s_sleep(WSLEEP);
#define F_LD(A, B) "global_load_dwordx4 " A ", %[fo], %[ws] sc1\n\tglobal_load_dwordx4 " B ", %[fo], %[ws] offset:1024 sc1\n\t"
      asm volatile(F_LD("%0", "%1") : "=&v"(f0), "=&v"(f1) : [fo] "v"(fo), [ws] "s"(ws) : "memory");
      _Pragma("unroll 1") for (;;) {
        asm volatile(F_LD("%0", "%1") "s_waitcnt vmcnt(2)" : "=&v"(g0), "=&v"(g1), "+v"(f0), "+v"(f1) : [fo] "v"(fo), [ws] "s"(ws) : "memory");
        fa = (f0[1] ^ tag) | (f0[3] ^ tag) | (f1[1] ^ tag) | (f1[3] ^ tag); km = umax(f0[0], f0[2]);
        if (__all(fa == 0u)) break;
        asm volatile(F_LD("%2", "%3") "s_waitcnt vmcnt(2)" : "+v"(g0), "+v"(g1), "=&v"(f0), "=&v"(f1) : [fo] "v"(fo), [ws] "s"(ws) : "memory");
        fa = (g0[1] ^ tag) | (g0[3] ^ tag) | (g1[1] ^ tag) | (g1[3] ^ tag); km = umax(g0[0], g0[2]);
        if (__all(fa == 0u) || --spins == 0) break;
      }
      LD32(sb + L_M + M_KMAX) = wave_umax(km);
      LD32(sb + L_M + M_RDY) = 1u;
      arrive8(sb + L_M + M_AB);
      asm volatile("s_waitcnt vmcnt(0)" : "+v"(f0), "+v"(f1), "+v"(g0), "+v"(g1) :: "memory");
      if (b == 0 && lane == 0) __hip_atomic_store((gu32*)(ws + W_E), tag, RLX_AGENT);
    }
    STAMP(4);
    wait8(sb + L_M + M_AB, 9u);
    GATHER(0, 116);
    __builtin_amdgcn_s_setprio(SSM_PRIO);
    {
      const unsigned c4 = (unsigned)(par * 64 + lane) * 4, tlo = sb + L_TLO + tch * TLO_CH + c4, thi = sb + L_THI + tch * THI_CH + c4;
#pragma unroll
      for (int i = 0; i < 32; ++i) asm volatile("ds_write_b32 %0, v[84+%c1] offset:%c2" :: "v"(tlo), "i"(i), "i"(i * 528) : "memory");
      GATHER(1, 117);
#pragma unroll
      for (int i = 0; i < 21; ++i) LD32(thi + i * 528) = T[i];
    }
    if (tid < 248) { const int c = tid >= 124, i = tid - 124 * c; LD128(sb + L_U + c * U_CH + (i >> 2) * 80 + (i & 3) * 16) = (u32x4){0u, 0u, 0u, 0u}; }
    GATHER(2, 118);
    sync4(sb, j, 0);
    STAMP(5);
    const unsigned KT = sb + L_P;
    {
      const unsigned aa = sb + L_TLO + tch * TLO_CH + (16 * par + l15) * 528 + g4 * 16;
      const unsigned b0 = sb + L_THI + tch * THI_CH + l15 * 528 + g4 * 16, b1 = sb + L_THI + tch * THI_CH + (l15 < 4 ? 16 + l15 : 20) * 528 + g4 * 16;
      const u32x2 sc = LD64(sb + L_M + M_SCALE + tch * 8);
      f32x4 k0, k1;
#pragma unroll
      for (int ks = 0; ks < 8; ++ks) {
        const u32x4 fa = LD128(aa + ks * 64), fb = LD128(b0 + ks * 64), fc = LD128(b1 + ks * 64);
        if (ks == 0) asm volatile("v_mfma_f32_16x16x32_f16 %0, %3, %2, 0\n\tv_mfma_f32_16x16x32_f16 %1, %4, %2, 0" : "=&v"(k0), "=&v"(k1) : "v"(fa), "v"(fb), "v"(fc));
        else asm volatile("v_mfma_f32_16x16x32_f16 %0, %3, %2, %0\n\tv_mfma_f32_16x16x32_f16 %1, %4, %2, %1" : "+v"(k0), "+v"(k1) : "v"(fa), "v"(fb), "v"(fc));
        if (ks == 3) GATHER(3, 119);
      }
      asm volatile("s_nop 7" : "+v"(k0), "+v"(k1));
      const int lb = 128 * par + 8 * l15;
      const int l0 = lb + (g4 < 2 ? 4 * g4 : 256 * (g4 - 1)), s1 = g4 < 2 ? 1 : 2 * (g4 - 1), s2 = g4 < 3 ? 2 * s1 : 256, l1 = lb + 1024 + 1024 * g4;
      k0 *= __uint_as_float(sc[0]); k1 *= __uint_as_float(sc[0]);
      if (__builtin_amdgcn_readfirstlane(sc[1])) {
#pragma unroll
        for (int r = 0; r < 4; ++r) {
          k0[r] *= __builtin_amdgcn_exp2f(__uint_as_float(sc[1]) * (float)(l0 + (r & 1) * s1 + (r >> 1) * s2));
          k1[r] *= __builtin_amdgcn_exp2f(__uint_as_float(sc[1]) * (float)(l1 + 256 * r));
        }
      }
      if (l0 == 0) k0[0] += tch ? bfhi(d2) : bflo(d2);
      const unsigned long long g23 = 0xffffffff00000000ull, g3 = 0xffff000000000000ull;
      const unsigned long long g23d = par ? g23 : g23 & ~(1ull << 32), g3d = par ? g3 : g3 & ~(1ull << 48);
      const unsigned long long a0 = par ? 0xffffull : 0x1ffffull, a0d = par ? 0xffffull : 0x1fffeull, e0 = par ? 0x5555ull : 0x15555ull, e0d = par ? 0x5555ull : 0x15554ull;
      const unsigned ka0 = KT + l0 * 4 + 2 * tch, ka2 = ka0 + s2 * 4;
#define KW(R, o) "ds_write_b16" R " offset:" STR(o) "\n\t"
#define KR0 " %0, %5"
#define KR1 "_d16_hi %1, %5"
#define KR2 " %2, %6"
#define KR3 "_d16_hi %3, %6"
#define KS0 " %4, %7"
#define KS1 "_d16_hi %4, %7"
#define KS2 " %4, %8"
#define KS3 "_d16_hi %4, %8"
#define KW6(R, o) KW(R, o - 8) KW(R, o - 4) KW(R, o) KW(R, o + 4) KW(R, o + 8) KW(R, o + 12)
      asm volatile(KW(KR0, 16) KW(KR1, 16) KW(KR2, 16) KW(KR3, 16)
                   "s_mov_b64 exec, %9\n\t" KW(KR0, 20) KW(KR2, 20) KW(KR2, 12)
                   "s_mov_b64 exec, %10\n\t" KW(KR0, 12)
                   "s_mov_b64 exec, %11\n\t" KW(KR1, 20) KW(KR1, 12) KW(KR3, 20) KW(KR3, 12) KW(KR0, 24) KW(KR2, 24) KW(KR2, 8)
                   "s_mov_b64 exec, %12\n\t" KW(KR0, 8)
                   "s_mov_b64 exec, %13\n\t" KW6(KS0, 16)
                   "s_mov_b64 exec, %14\n\t" KW(KS0, 4)
                   "s_mov_b64 exec, %15\n\t" KW(KS0, 32)
                   "s_mov_b64 exec, %16\n\t" KW(KS0, 0)
                   "s_mov_b64 exec, 0xffff\n\t" KW6(KS1, 1040) KW(KS1, 1028) KW6(KS2, 2064) KW(KS2, 2052) KW6(KS3, 3088) KW(KS3, 3076)
                   "s_mov_b64 exec, 0x5555\n\t" KW(KS1, 1056) KW(KS1, 1024) KW(KS2, 2080) KW(KS2, 2048) KW(KS3, 3104) KW(KS3, 3072)
                   "s_mov_b64 exec, -1"
                   :: "v"(ka0), "v"(ka0 + s1 * 4), "v"(ka2), "v"(ka2 + s1 * 4), "v"(KT + l1 * 4 + 2 * tch), "v"(pkbf(k0[0], k0[1])), "v"(pkbf(k0[2], k0[3])),
                      "v"(pkbf(k1[0], k1[1])), "v"(pkbf(k1[2], k1[3])), "s"(g23), "s"(g23d), "s"(g3), "s"(g3d), "s"(a0), "s"(a0d), "s"(e0), "s"(e0d) : "memory");
      if (j == 0) LD128(KT) = (u32x4){0u, 0u, 0u, 0u};
    }
    post(sb + L_M + M_SW + 16 + j * 4, 1u);
    GATHER(4, 84);
    wait4(sb + L_M + M_SW + 16, 1u);
    STAMP(6);
#pragma unroll
    for (int k = 0; k < 2; ++k) {
      if (k == 0) GATHER(5, 85); else GATHER(6, 86);
      const u32x4 da = LD128(KT + tid * 16 + k * 4096), db = LD128(KT + tid * 16 + k * 4096 + 16);
      const unsigned d[7] = {da[1], da[2], da[3], db[0], db[1], db[2], db[3]};
      const unsigned ka = sb + L_KC + 224 + tid * 32 + k * 8192;
#pragma unroll
      for (int c = 0; c < 2; ++c) {
        unsigned P[6];
#pragma unroll
        for (int i = 0; i < 6; ++i) P[i] = __builtin_amdgcn_perm(d[i], d[i + 1], c ? 0x07060302u : 0x05040100u);
        LD128(ka + c * KC_CH) = (u32x4){P[2], P[0], P[3], P[1]};
        LD128(ka + c * KC_CH + 16) = (u32x4){P[4], P[2], P[5], P[3]};
      }
    }
    if (tid < 28) LD128(sb + L_KC + (tid >= 14) * (KC_CH - 224) + tid * 16) = (u32x4){0u, 0u, 0u, 0u};
    STAMP(7);
    __builtin_amdgcn_s_sleep(GSLEEP);
    GATHER(7, 87);
    asm volatile("s_waitcnt vmcnt(0)\n\t"
                 "v_perm_b32 v88, v117, v116, %1\n\tv_perm_b32 v89, v119, v118, %1\n\tv_perm_b32 v90, v85, v84, %1\n\tv_perm_b32 v91, v87, v86, %1\n\t"
                 "v_perm_b32 v92, v117, v116, %2\n\tv_perm_b32 v93, v119, v118, %2\n\tv_perm_b32 v94, v85, v84, %2\n\tv_perm_b32 v95, v87, v86, %2\n\t"
                 "ds_write_b128 %0, v[88:91]\n\tds_write_b128 %0, v[92:95] offset:7600"
                 :: "v"(sb + L_U + (31 + (tid >> 2)) * 80 + (tid & 3) * 16), "s"(0x05040100u), "s"(0x07060302u) : "memory");
    sync4(sb, j, 2);
    STAMP(8);
    unsigned aA = sb + L_KC + tch * KC_CH + (unsigned)(8 * (16 * (32 * par - 1) + l31 - 8 * h + 24));
    unsigned aF = sb + L_U + tch * U_CH + (unsigned)(80 * (l31 + 31 - 16 * par) + 16 * h);
    f32x16 y0, y1;
    unsigned cl;
    const u32x2 zz = {0u, 0u};
#define CLD(R0, R1, R2, R3, R4, R5, OA, OF) \
      "ds_read_b64 v[" #R0 "], %3 offset:" #OA "+32\n\tds_read_b64 v[" #R1 "], %3 offset:" #OA "\n\tds_read_b128 v[" #R4 "], %4 offset:" #OF "\n\t" \
      "ds_read_b128 v[" #R5 "], %4 offset:" #OF "+2560\n\tds_read_b64 v[" #R2 "], %3 offset:" #OA "+8224\n\tds_read_b64 v[" #R3 "], %3 offset:" #OA "+8192\n\t"
#define CMM(A0, A1, F0, F1) "s_waitcnt lgkmcnt(6)\n\tv_mfma_f32_32x32x16_bf16 %0, v[" #A0 "], v[" #F0 "], %0\n\t" \
      CPAD "v_mfma_f32_32x32x16_bf16 %1, v[" #A0 "], v[" #F1 "], %1\n\t" CPAD "v_mfma_f32_32x32x16_bf16 %1, v[" #A1 "], v[" #F0 "], %1\n\t" CPAD
    asm volatile(
        "s_setprio " STR(CONV_PRIO) "\n\ts_nop 1\n\tv_mfma_f32_32x32x8_bf16 %0, %5, %5, 0\n\tv_mfma_f32_32x32x8_bf16 %1, %5, %5, 0\n\ts_mov_b32 %2, " STR(CONV_N) "\n\t"
        CLD(84:85, 86:87, 88:89, 90:91, 92:95, 96:99, 0, 32)
        "1:\n\t"
        CLD(100:101, 102:103, 104:105, 106:107, 108:111, 112:115, 128, 0)
        CMM(84:87, 88:91, 92:95, 96:99)
        "v_add_u32 %3, 0x100, %3\n\tv_add_u32 %4, 0xffffffb0, %4\n\t"
        CLD(84:85, 86:87, 88:89, 90:91, 92:95, 96:99, 0, 32)
        CMM(100:103, 104:107, 108:111, 112:115)
        "s_sub_u32 %2, %2, 1\n\ts_cmp_lg_u32 %2, 0\n\ts_cbranch_scc1 1b\n\t"
        "s_setprio " STR(SSM_PRIO) "\n\ts_waitcnt lgkmcnt(0)\n\ts_nop 15"
        : "=&v"(y0), "=&v"(y1), "=&s"(cl), "+v"(aA), "+v"(aF) : "v"(zz) : "memory", "scc");
    const unsigned pa = opaque(sb + L_P + lane * 16), pw = opaque(pa + j * 8192);
#pragma unroll
    for (int q = 0; q < 4; ++q) {
      LDF4(pw + q * 1024) = (f32x4){y0[4 * q], y0[4 * q + 1], y0[4 * q + 2], y0[4 * q + 3]};
      LDF4(pw + 4096 + q * 1024) = (f32x4){y1[4 * q], y1[4 * q + 1], y1[4 * q + 2], y1[4 * q + 3]};
    }
    STAMP(9);
    sync4(sb, j, 3);
    const unsigned wo0 = (128 * j + r8) * 1024 + hg * 128 + ((c8 ^ wswz(r8)) << 4);
    gdma_rows16(wo0, (wo0 ^ 64) + 8192, wo, sb + L_WO + j * 16384);
    const unsigned pr = opaque(pa + tch * 4096 + par * 2048);
    unsigned yv[8];
#pragma unroll
    for (int qi = 0; qi < 2; ++qi) {
      const unsigned o = pr + qi * 1024;
      const f32x4 ya = LDF4(o) + LDF4(o + 8192), yb = LDF4(o + 16384) + LDF4(o + 24576);
#pragma unroll
      for (int e = 0; e < 4; ++e) yv[4 * qi + e] = pkbf(ya[e], yb[e]);
    }
    post(sb + L_M + M_SW + 64 + j * 4, 1u);
    const __amdgpu_buffer_rsrc_t rsY = __builtin_amdgcn_make_buffer_rsrc(out, 0, 4194304, 0x00020000);
    const unsigned yo = (1024 * tch + 32 * l31 + 16 * par + 4 * h) * 2048 + cp * 4;
    STAMP(10);
#pragma unroll
    for (int i = 0; i < NYST; ++i) {
      __builtin_amdgcn_raw_buffer_store_b32(yv[i], rsY, yo + (i & 1) * 2048, (i >> 2) * 16384 + ((i >> 1) & 1) * 4096, 0);
      if (i < 7) asm volatile("s_sleep " STR(YSLEEP) ::: "memory");
    }
    asm volatile("s_waitcnt vmcnt(0)" ::: "memory");
    STAMP(11);
    STAMP_OUT();
    return;
  }

  LANE_VARS();
  const int qq = w & 3, kh = w >> 2, chain = hg >> 2;
  const bool crit = w == (hg & 3);
  if (!crit) asm volatile("s_barrier" ::: "memory");
  else __builtin_amdgcn_s_setprio(3);
  if (kh == 0) {
    const unsigned xo = (64 * grp + 16 * qq + r8) * 1024 + ((c8 ^ (r8 >> 1)) << 4);
    gdma_x(xo, (xo ^ 64) + 8192, x, sb + L_X + qq * 16384);
    gdma_x(xo + 512, (xo ^ 64) + 8704, x, sb + L_X + qq * 16384 + 8192);
  } else {
    const unsigned wq0 = (128 * qq + r8) * 1024 + hg * 128 + ((c8 ^ ((r8 & 2) << 1)) << 4);
    gdma_rows16(wq0, wq0 + 8192, wq, sb + L_WQ + qq * 16384);
  }
  if (crit) {
    const unsigned short* wkv = chain ? wv : wk;
#pragma unroll
    for (int i = 0; i < 4; ++i) gdma4((unsigned)(i * 4096 + lane * 16), wkv, sb + L_R2 + i * 4096);
    asm volatile("s_barrier" ::: "memory");
    unsigned E;
    asm volatile("s_load_dword %0, %1, 0x0\n\ts_waitcnt lgkmcnt(0)" : "=s"(E) : "s"(ws) : "memory");
    const unsigned tg = E + (((E * 0x9E3779B1u ^ (E >> 15) ^ 0x5bd1e995u) & 0x7fffffffu) | 0x101u);
    LD32(sb + L_M + M_TAG) = tg;
    const unsigned wa = sb + L_R2 + (8 * g4 + (l15 >> 2)) * 32 + (lane & 3) * 8;
    const unsigned xe = sb + L_X + qq * 16384 + (l15 >> 3) * 4096 + (l15 & 7) * 128 + ((g4 ^ (l15 >> 1)) << 4);
    const unsigned off = (2 * grp + (qq >> 1)) * KV_TILE + l15 * 16 +
                         (chain ? 1024 + (qq & 1) * 640 + (g4 >> 1) * 320 + (g4 & 1) * 8 : (qq & 1) * 256 + (g4 & 1) * 512 + (g4 >> 1) * 8);
    f32x4 acc, acd;
#define KR(s, o) "v[36+8*(" #s "%%6)+" #o ":36+8*(" #s "%%6)+" #o "+3]"
#define K_LD(s, X) "ds_read_b128 " KR(s, 0) ", " X " offset:8192*(" #s "/8)+1024*((" #s "&7)/2)\n\t" \
      "ds_read_b64_tr_b16 v[36+8*(" #s "%%6)+4:36+8*(" #s "%%6)+5], %4 offset:1024*" #s "\n\tds_read_b64_tr_b16 v[36+8*(" #s "%%6)+6:36+8*(" #s "%%6)+7], %4 offset:1024*" #s "+128\n\t"
#define K_LD2(s, t) K_LD(s, "%2") K_LD(t, "%3")
#define K_MM(N, s, t, A, B, C0, C1) "s_waitcnt lgkmcnt(" #N ")\n\tv_mfma_f32_16x16x32_bf16 %0, " KR(s, A) ", " KR(s, B) ", " C0 "\n\t" \
      "v_mfma_f32_16x16x32_bf16 %1, " KR(t, A) ", " KR(t, B) ", " C1 "\n\t"
#define K_ALL(A, B) asm volatile("s_waitcnt vmcnt(0) lgkmcnt(0)\n\t" K_LD2(0, 1) K_LD2(2, 3) K_LD2(4, 5) K_MM(12, 0, 1, A, B, "0", "0") K_LD2(6, 7) K_MM(12, 2, 3, A, B, "%0", "%1") \
        K_LD2(8, 9) K_MM(12, 4, 5, A, B, "%0", "%1") K_LD2(10, 11) K_MM(12, 6, 7, A, B, "%0", "%1") K_LD2(12, 13) K_MM(12, 8, 9, A, B, "%0", "%1") \
        K_LD2(14, 15) K_MM(12, 10, 11, A, B, "%0", "%1") K_MM(6, 12, 13, A, B, "%0", "%1") K_MM(0, 14, 15, A, B, "%0", "%1") "s_nop 7" \
        : "=&v"(acc), "=&v"(acd) : "v"(xe), "v"(xe ^ 64), "v"(wa) : "memory", CLOB_V36_83)
    if (chain) K_ALL(0, 4); else K_ALL(4, 0);
    STAMP(1);
    acc = (acc + acd) * (chain ? 1.0f : 0.25f * 1.44269504088896f);
    const u32x2 pk = {pkbf(acc[0], acc[1]), pkbf(acc[2], acc[3])};
    const __amdgpu_buffer_rsrc_t rs = __builtin_amdgcn_make_buffer_rsrc(ws + W_KV, 0, KV_BYTES, 0x00020000);
    __builtin_amdgcn_raw_buffer_store_b64(pk, rs, off, 0, 16);
    __builtin_amdgcn_raw_buffer_store_b64((u32x2){0x3f803f80u, 0x3f803f80u}, rs, (chain && l15 == 0) ? off + 256 : 0x80000000u, 0, 16);
    const float n2 = sq2(pk[0], sq2(pk[1], 0.f));
    const unsigned kn2 = row_umax(__float_as_uint(sum32(sum16(n2, n2))));
    asm volatile("s_waitcnt vmcnt(0)" ::: "memory");
    if (lane < NREP) __hip_atomic_store((gu64*)(ws + W_F1) + lane * 256 + chain * 128 + 4 * grp + qq, ((unsigned long long)tg << 32) | kn2, RLX_AGENT);
    __builtin_amdgcn_s_setprio(0);
    STAMP(2);
  } else {
    asm volatile("s_waitcnt vmcnt(0)" ::: "memory");
  }
  STAMP(3);
  BAR();
  STAMP(4);

  const unsigned tag = __builtin_amdgcn_readfirstlane(LD32(sb + L_M + M_TAG));
  f32x16 qa16;
  {
    const unsigned A0 = sb + L_WQ + (256 * kh + 8 * h + (l15 >> 2)) * 128 + (((4 * (qq & 1) + 2 * ((g4 & 1) ^ kh) + ((lane & 3) >> 1)) ^ ((l15 >> 3) << 2)) << 4) + (lane & 1) * 8;
    const unsigned X0 = sb + L_X + ((qq & 2) + (g4 & 1)) * 16384 + kh * 8192 + (l15 >> 3) * 4096 + (l15 & 7) * 128 + ((h ^ (l15 >> 1)) << 4);
#define Q_LD(s, X) "ds_read_b128 " KR(s, 0) ", " X " offset:1024*(" #s "/4)\n\t" \
      "ds_read_b64_tr_b16 v[36+8*(" #s "%%6)+4:36+8*(" #s "%%6)+5], %1 offset:2048*" #s "\n\tds_read_b64_tr_b16 v[36+8*(" #s "%%6)+6:36+8*(" #s "%%6)+7], %1 offset:2048*" #s "+512\n\t"
#define Q_MM(N, s, C) "s_waitcnt lgkmcnt(" #N ")\n\tv_mfma_f32_32x32x16_bf16 %0, " KR(s, 4) ", " KR(s, 0) ", " C "\n\t"
    asm volatile(Q_LD(0, "%2") Q_LD(1, "%3") Q_LD(2, "%4") Q_LD(3, "%5") Q_LD(4, "%2") Q_MM(12, 0, "0") Q_LD(5, "%3") Q_MM(12, 1, "%0") Q_LD(6, "%4") Q_MM(12, 2, "%0")
                 Q_LD(7, "%5") Q_MM(12, 3, "%0") Q_LD(8, "%2") Q_MM(12, 4, "%0") Q_LD(9, "%3") Q_MM(12, 5, "%0") Q_LD(10, "%4") Q_MM(12, 6, "%0") Q_LD(11, "%5") Q_MM(12, 7, "%0")
                 Q_LD(12, "%2") Q_MM(12, 8, "%0") Q_LD(13, "%3") Q_MM(12, 9, "%0") Q_LD(14, "%4") Q_MM(12, 10, "%0") Q_LD(15, "%5") Q_MM(12, 11, "%0")
                 Q_MM(9, 12, "%0") Q_MM(6, 13, "%0") Q_MM(3, 14, "%0") Q_MM(0, 15, "%0") "s_nop 11"
                 : "=&v"(qa16) : "v"(A0), "v"(X0), "v"(X0 ^ 32), "v"(X0 ^ 64), "v"(X0 ^ 96) : "memory", CLOB_V36_83);
  }
  STAMP(5);
  const unsigned xa = opaque(sb + L_R2 + lane * 16 + w * 2048);
  LDF4(xa) = __builtin_shufflevector(qa16, qa16, 8, 9, 10, 11);
  LDF4(xa + 1024) = __builtin_shufflevector(qa16, qa16, 12, 13, 14, 15);
  arrive8(sb + L_M + M_AB + 12);
  wait8(sb + L_M + M_AB + 12);
  STAMP(6);
  const i32x4 rsKV = make_srd(ws + W_KV, KV_BYTES);
  const unsigned ko = lane * 16, vo = l31 <= 16 ? 1024 + h * 320 + l31 * 16 : 0x80000000u;
  const unsigned t0 = (16 * qq + 2 * bi) & 63;
  const bool ready = __builtin_amdgcn_readfirstlane(LD32(sb + L_M + M_RDY));
#define KV_FIRST() { unsigned tl_ = t0, sa_, sb_; asm volatile(A_FIRST : [tl] "+s"(tl_), [sa] "=&s"(sa_), [sb] "=&s"(sb_) \
    : [ko] "v"(ko), [vo] "v"(vo), [rk] "s"(rsKV) : "memory", "scc", CLOB_A4_47); }
  if (ready) KV_FIRST();
  {
    const f32x4 q0 = __builtin_shufflevector(qa16, qa16, 0, 1, 2, 3) + LDF4(xa ^ 8192), q1 = __builtin_shufflevector(qa16, qa16, 4, 5, 6, 7) + LDF4((xa ^ 8192) + 1024);
    const u32x4 o = {pkbf(q0[0], q0[1]), pkbf(q0[2], q0[3]), pkbf(q1[0], q1[1]), pkbf(q1[2], q1[3])};
    LD128(sb + L_Q + (2 * qq + kh) * 1024 + lane * 16) = o;
    const unsigned n2 = row_umax(__float_as_uint(sum32(sq2(o[0], sq2(o[1], sq2(o[2], sq2(o[3], 0.f)))))));
    asm volatile("s_mov_b64 exec, 0x10001\n\tds_max_u32 %0, %1\n\ts_mov_b64 exec, -1" :: "v"(sb + L_M + M_QMAX), "v"(n2) : "memory");
  }
  STAMP(7);
  arrive8(sb + L_M + M_AB);
  wait8(sb + L_M + M_AB, 9u);
  STAMP(8);
  if (!ready) KV_FIRST();

  const u32x2 qk2 = LD64(sb + L_M + M_QMAX);
  const float bound = fmaf(__builtin_amdgcn_sqrtf(__uint_as_float(qk2[0]) * __uint_as_float(qk2[1])), 1.002f, 1e-3f);
  const unsigned exact = __builtin_amdgcn_readfirstlane(opaque(bound <= 32.0f ? 0u : 1u));
  {
    unsigned tl = t0, n = 8, sa, sbo, sw = kh * PSW;
    const unsigned qb = sb + L_Q + kh * 4096, ob = sb + L_OA + w * 4 * OA_SLOT;
    if (exact) {
      const unsigned maddr = sb + L_MX + kh * 512 + l31 * 4;
      if (w < 4) LDF(sb + L_MX + (w * 64 + lane) * 4) = -3.0e38f;
      arrive8(sb + L_M + M_AB + 4);
      wait8(sb + L_M + M_AB + 4);
      float m[4] = {-3.0e38f, -3.0e38f, -3.0e38f, -3.0e38f};
      const __amdgpu_buffer_rsrc_t rs = __builtin_amdgcn_make_buffer_rsrc(ws + W_KV, 0, KV_BYTES, 0x00020000);
#pragma unroll 1
      for (unsigned i = 0; i < 16; ++i) {
        u32x4 kf = __builtin_amdgcn_raw_buffer_load_b128(rs, ko, ((tl + i) & 63) * KV_TILE, 16);
#pragma unroll
        for (int t = 0; t < 4; ++t) {
          const u32x4 q = LD128(qb + ko + t * 1024);
          f32x16 s_;
          asm volatile("s_nop 1\n\tv_mfma_f32_32x32x16_bf16 %0, %1, %2, 0\n\ts_nop 15" : "=&v"(s_) : "v"(kf), "v"(q) : CLOB_A4_47);
#pragma unroll
          for (int r = 0; r < 16; ++r) m[t] = fmaxf(m[t], s_[r]);
        }
      }
#pragma unroll
      for (int t = 0; t < 4; ++t) __builtin_amdgcn_ds_fmaxf((LAS float*)(size_t)(maddr + t * 128), m[t], __ATOMIC_RELAXED, __HIP_MEMORY_SCOPE_WORKGROUP, false);
      arrive8(sb + L_M + M_AB + 8);
      wait8(sb + L_M + M_AB + 8);
      asm volatile("v_accvgpr_write_b32 a44, %0\n\tv_accvgpr_write_b32 a45, %1\n\tv_accvgpr_write_b32 a46, %2\n\tv_accvgpr_write_b32 a47, %3"
                   :: "v"(-LDF(maddr)), "v"(-LDF(maddr + 128)), "v"(-LDF(maddr + 256)), "v"(-LDF(maddr + 384)) : CLOB_A4_47);
    }
    asm volatile("s_cmp_lg_u32 %[ex], 0\n\ts_cbranch_scc1 90f\n\t" A_ATT(A_SHIFT_NONE) "s_branch 91f\n\t90:\n\t" A_ATT(A_SHIFT_EXACT) "91:"
                 : [tl] "+s"(tl), [n] "+s"(n), [sa] "=&s"(sa), [sb] "=&s"(sbo), [sw] "+s"(sw)
                 : [ko] "v"(ko), [vo] "v"(vo), [nb] "v"(-bound), [rk] "s"(rsKV), [qb] "s"(qb), [ob] "s"(ob), [pf] "s"(sb + L_M + M_SW + 64), [ex] "s"(exact)
                 : "memory", "scc", "vcc", CLOB_V4_83, CLOB_A4_47);
  }
  STAMP(9);
  RSTAMP(17);
  BAR();
  STAMP(10);

  {
    LANE_VARS();
    {
      const unsigned s0 = opaque(sb + L_OA + (16 * kh + qq) * OA_SLOT + lane * 16), s1 = opaque(sb + L_OA + (16 * kh + qq) * OA_SLOT + 2048 + lane * 4);
      constexpr unsigned W1 = 4 * OA_SLOT;
      f32x4 o0 = (LDF4(s0) + LDF4(s0 + W1)) + (LDF4(s0 + 2 * W1) + LDF4(s0 + 3 * W1));
      f32x4 o1 = (LDF4(s0 + 1024) + LDF4(s0 + W1 + 1024)) + (LDF4(s0 + 2 * W1 + 1024) + LDF4(s0 + 3 * W1 + 1024));
      const float inv = __builtin_amdgcn_rcpf(sum32((LDF(s1) + LDF(s1 + W1)) + (LDF(s1 + 2 * W1) + LDF(s1 + 3 * W1))));
      o0 *= inv; o1 *= inv;
      u32x4 pk = {pkbf(o0[0], o0[1]), pkbf(o0[2], o0[3]), pkbf(o1[0], o1[1]), pkbf(o1[2], o1[3])};
      const __amdgpu_buffer_rsrc_t rsO = __builtin_amdgcn_make_buffer_rsrc(ws + W_OX, 0, 2097152, 0x00020000);
      __builtin_amdgcn_raw_buffer_store_b128(pk, rsO, lane * 16 + ((2 * grp + kh) * 32 + 4 * hg + qq) * 1024, 0, 16);
    }
    STAMP(11);
    RSTAMP(13);
    const unsigned r0 = 256 * kh + 16 * (g4 >> 1) + 4 * (g4 & 1) + (l15 >> 2);
    const unsigned A0 = sb + L_WO + r0 * 128 + (((4 * kh + ((lane & 3) >> 1)) ^ wswz(r0)) << 4) + (lane & 1) * 8;
    const unsigned oo = (g4 >> 1) * 1024 + ((g4 & 1) * 32 + 16 * (qq & 1) + l15) * 16 + ((2 * grp + (qq >> 1)) * 32 + 16 * kh) * 1024;
    const unsigned xa = sb + L_U + lane * 16;
    const unsigned go = (64 * grp + 16 * qq + l15) * 2048 + 1024 + 128 * hg + 64 * kh + 32 * (g4 & 1) + 16 * (g4 >> 1);
    const unsigned char* f2 = ws + W_F2 + grp * 128;
    unsigned t0, t1;
#define T_WF1(F, B, s, e, P, Q) "ds_read_b64_tr_b16 " F "[" #B "+4*" #e ":" #B "+4*" #e "+1], " P " offset:4096*" #s "\n\t" \
                                "ds_read_b64_tr_b16 " F "[" #B "+4*" #e "+2:" #B "+4*" #e "+3], " Q " offset:4096*" #s "+1024\n\t"
#define T_WF(F, B, s) T_WF1(F, B, s, 0, "%[a0]", "v6") T_WF1(F, B, s, 1, "v5", "v7") T_WF1(F, B, s, 2, "v6", "%[a0]") T_WF1(F, B, s, 3, "v7", "v5")
#define T_M1(F, B, s, e, C) "v_mfma_f32_16x16x32_bf16 v[104+4*" #e ":107+4*" #e "], " F "[" #B "+4*" #e ":" #B "+4*" #e "+3], v[72+4*" #s ":75+4*" #s "], " C "\n\t"
#define T_MA(e) "v[104+4*" #e ":107+4*" #e "]"
#define T_MM(F, B, s, N) "s_waitcnt vmcnt(" #N ")\n\t" T_M1(F, B, s, 0, T_MA(0)) T_M1(F, B, s, 1, T_MA(1)) T_M1(F, B, s, 2, T_MA(2)) T_M1(F, B, s, 3, T_MA(3))
#define T_OL(s, so, imm) "buffer_load_dwordx4 v[72+4*" #s ":75+4*" #s "], %[oo], %[ro], " so " offen offset:" #imm " sc1\n\t"
    asm volatile(
        "v_xor_b32 v5, 32, %[a0]\n\tv_xor_b32 v6, 64, %[a0]\n\tv_xor_b32 v7, 0x60, %[a0]\n\t"
        T_WF("v", 8, 0) T_WF("v", 24, 1) T_WF("v", 40, 2)
        "s_waitcnt vmcnt(0)\n\ts_barrier\n\t"
        TS(80) "s_cmp_lg_u32 %[w], 0\n\ts_cbranch_scc1 1f\n\t"
        "v_mov_b32 v72, %[fo]\n\tv_mov_b32 v73, %[tg]\n\ts_mov_b64 exec, 1\n\tglobal_store_dword v72, v73, %[f2] sc1\n\ts_mov_b64 exec, -1\n\t"
        "1:\n\t"
        "v_and_b32 v4, 48, %[xa]\n\tv_lshrrev_b32 v4, 2, v4\n\tv_or_b32 v4, %[k4], v4\n\ts_mov_b32 %[t0], 0x100000\n\t"
        T_WF("v", 56, 3) "global_load_dword v104, v4, %[f2] sc1\n\t" T_WF("a", 8, 4) T_WF("a", 24, 5)
        "2:\n\tglobal_load_dword v105, v4, %[f2] sc1\n\ts_waitcnt vmcnt(1)\n\tv_cmp_ne_u32 vcc, %[tg], v104\n\ts_cbranch_vccz 3f\n\t"
        "global_load_dword v104, v4, %[f2] sc1\n\ts_waitcnt vmcnt(1)\n\tv_cmp_ne_u32 vcc, %[tg], v105\n\ts_cbranch_vccz 3f\n\t"
        "s_sub_u32 %[t0], %[t0], 1\n\ts_cbranch_scc0 2b\n\t"
        "3:\n\t" TS(82) "s_movk_i32 %[t0], 0x1000\n\ts_movk_i32 %[t1], 0x2000\n\t"
        T_OL(0, "0", 0) T_OL(1, "0", 2048) T_OL(2, "%[t0]", 0) T_OL(3, "%[t0]", 2048) "s_movk_i32 %[t0], 0x3000\n\t"
        T_OL(4, "%[t1]", 0) T_OL(5, "%[t1]", 2048) T_OL(6, "%[t0]", 0) T_OL(7, "%[t0]", 2048)
        "s_lshl_b32 %[t0], %[w], 11\n\ts_xor_b32 %[t1], %[t0], 0x2000\n\t"
        "s_waitcnt lgkmcnt(0) vmcnt(7)\n\t" T_M1("v", 8, 0, 0, "0") T_M1("v", 8, 0, 1, "0") T_M1("v", 8, 0, 2, "0") T_M1("v", 8, 0, 3, "0")
        T_WF("v", 8, 6) T_MM("v", 24, 1, 6) T_WF("v", 24, 7) T_MM("v", 40, 2, 5) T_MM("v", 56, 3, 4) T_MM("a", 8, 4, 3) T_MM("a", 24, 5, 2)
        "s_waitcnt lgkmcnt(0)\n\t" T_MM("v", 8, 6, 1) T_MM("v", 24, 7, 0)
        TS(84) "v_add_u32 v4, %[t0], %[xa]\n\tv_add_u32 v5, %[t1], %[xa]\n\ts_nop 6\n\t"
        "ds_write_b128 v4, v[112:115]\n\tds_write_b128 v4, v[116:119] offset:1024\n\ts_waitcnt lgkmcnt(0)\n\ts_barrier\n\t"
        "ds_read_b128 v[72:75], v5\n\tds_read_b128 v[76:79], v5 offset:1024\n\ts_waitcnt lgkmcnt(1)\n\t"
        "v_add_f32 v72, v72, v104\n\tv_add_f32 v73, v73, v105\n\tv_add_f32 v74, v74, v106\n\tv_add_f32 v75, v75, v107\n\ts_waitcnt lgkmcnt(0)\n\t"
        "v_add_f32 v76, v76, v108\n\tv_add_f32 v77, v77, v109\n\tv_add_f32 v78, v78, v110\n\tv_add_f32 v79, v79, v111\n\t"
        "v_cvt_pk_bf16_f32 v80, v72, v73\n\tv_cvt_pk_bf16_f32 v81, v74, v75\n\tv_cvt_pk_bf16_f32 v82, v76, v77\n\tv_cvt_pk_bf16_f32 v83, v78, v79\n\ts_nop 1\n\t"
        "v_permlane16_swap_b32 v80, v82\n\tv_permlane16_swap_b32 v81, v83\n\ts_nop 1\n\t"
        "global_store_dwordx4 %[go], v[80:83], %[out]\n\ts_nop 1"
        : [t0] "=&s"(t0), [t1] "=&s"(t1)
        : [a0] "v"(A0), [oo] "v"(oo), [xa] "v"(xa), [go] "v"(go), [w] "s"(w), [fo] "s"(hg * 4), [tg] "s"(tag), [f2] "s"(f2), [k4] "s"(16 * kh),
          [ro] "s"(make_srd(ws + W_OX, 2097152)), [out] "s"(out)
        : "memory", "scc", "vcc", CLOB_V4_83, CLOB_A4_47 TSCLOB);
    TSTAMP_OUT();
    STAMP(12);
    STAMP_OUT();
  }
}

extern "C" void kernel_launch(void* const* d_in, const int* in_sizes, int n_in, void* d_out, int out_size, void* d_ws,
                              size_t ws_size, hipStream_t stream) {
  typedef const unsigned short* P;
  fused_kernel<<<256, 768, 0, stream>>>((P)d_in[0], (P)d_in[1], (P)d_in[2], (P)d_in[3], (P)d_in[4], (P)d_in[5], (P)d_in[6],
                                        (const unsigned*)d_in[7], (const unsigned*)d_in[8], (P)d_in[9], (P)d_in[10], (P)d_in[11],
                                        (P)d_in[12], (P)d_in[13], (unsigned short*)d_out, (unsigned char*)d_ws);
}
```

```cpp
#define DIAG 8
#include <hip/hip_runtime.h>
#include <hip/hip_bf16.h>
#include <stdint.h>

typedef float f32x16 __attribute__((ext_vector_type(16)));
typedef float f32x8 __attribute__((ext_vector_type(8)));
typedef float f32x4 __attribute__((ext_vector_type(4)));
typedef float f32x2 __attribute__((ext_vector_type(2)));
typedef __bf16 bf16x8 __attribute__((ext_vector_type(8)));
typedef __bf16 bf16x2 __attribute__((ext_vector_type(2)));
typedef _Float16 f16x8 __attribute__((ext_vector_type(8)));
typedef _Float16 f16x2 __attribute__((ext_vector_type(2)));
typedef short s16x4 __attribute__((ext_vector_type(4)));
typedef unsigned u32x4 __attribute__((ext_vector_type(4)));
typedef unsigned u32x2 __attribute__((ext_vector_type(2)));
#define LAS __attribute__((address_space(3)))
#define GAS __attribute__((address_space(1)))
typedef GAS unsigned gu32;
typedef GAS unsigned long long gu64;
#define RLX_AGENT __ATOMIC_RELAXED, __HIP_MEMORY_SCOPE_AGENT

constexpr unsigned L_X = 0;
constexpr unsigned L_TLO = 0, TLO_CH = 16896, L_THI = 33792, THI_CH = 11088;
constexpr unsigned L_KC = 0, KC_CH = 16640;
constexpr unsigned L_WO = 0;
constexpr unsigned L_B = 65536;
constexpr unsigned L_WQ = L_B;
constexpr unsigned L_R2 = L_B + 65536;
constexpr unsigned L_OA = L_B, OA_SLOT = 2304;
constexpr unsigned L_P = L_B + 40960;
constexpr unsigned L_U = L_B + 73728, U_CH = 7600;
constexpr unsigned L_Q = L_B + 88928;
constexpr unsigned L_MX = L_Q + 8192;
constexpr unsigned L_M = L_MX + 1024;
constexpr unsigned LDS_BYTES = L_M + 128;
static_assert(LDS_BYTES <= 163840, "lds");
constexpr unsigned M_TAG = 0, M_RDY = 4, M_QMAX = 8, M_KMAX = 12, M_SCALE = 16  , M_SW = 32  , M_AB = 112  ;
#define CONV_PRIO 3
#define CONV_N 16
#define NYST 8
#define YSLEEP 12
#define GSLEEP 10
#define CNOP 7
#define CPAD "s_nop " STR(CNOP) "\n\t"
#define PSW 2
#define NREP 8
#define WSLEEP 16
#define SSM_PRIO 3

constexpr unsigned KV_TILE = 2304, KV_BYTES = 64 * KV_TILE;
constexpr unsigned W_E = 0, W_F2 = 4096, W_F1 = 8192  , W_KV = W_F1 + 65536, W_OX = W_KV + KV_BYTES, W_END = W_OX + 2097152;
constexpr unsigned W_DBG = W_END;

constexpr int CODE_LINES = 150;
constexpr unsigned kSpinCap = 1u << 20;

#define LD32(a) (*(LAS unsigned*)(size_t)(a))
#define LDF(a) (*(LAS float*)(size_t)(a))
#define LD64(a) (*(LAS u32x2*)(size_t)(a))
#define LD128(a) (*(LAS u32x4*)(size_t)(a))
#define LDF4(a) (*(LAS f32x4*)(size_t)(a))
#define BAR() asm volatile("s_waitcnt lgkmcnt(0)\n\ts_barrier" ::: "memory")

#define RT_(v) asm volatile("s_memrealtime %0\n\ts_waitcnt lgkmcnt(0)" : "=s"(v) :: "memory")
#define DIAGW 4
#define STAMP(i) do { if ((i) == DIAG && w == DIAGW) { unsigned long long ti_; RT_(ti_); LD32(sb + L_MX) = (unsigned)(ti_ - t0_); } } while (0)
#define STAMP_INIT() unsigned long long t0_; RT_(t0_)
#define STAMP_OUT() do { if (w == 4) { unsigned long long te_, t_; RT_(te_); te_ += __builtin_amdgcn_readfirstlane(LD32(sb + L_MX)); \
  _Pragma("unroll 1") for (int k_ = 0; k_ < 100000; ++k_) { RT_(t_); if (t_ >= te_) break; } } } while (0)
#define RSTAMP(i)
#define TS(k)
#define TSTAMP_OUT()
#define TSCLOB

__device__ __forceinline__ float bflo(unsigned v) { return __uint_as_float(v << 16); }
__device__ __forceinline__ float bfhi(unsigned v) { return __uint_as_float(v & 0xffff0000u); }
__device__ __forceinline__ unsigned pkbf(float a, float b) { f32x2 x = {a, b}; return __builtin_bit_cast(unsigned, __builtin_convertvector(x, bf16x2)); }
__device__ __forceinline__ unsigned pkh(float a, float b) { f32x2 x = {a, b}; return __builtin_bit_cast(unsigned, __builtin_convertvector(x, f16x2)); }
template <int CTRL> __device__ __forceinline__ unsigned dppu(unsigned v) { return __builtin_amdgcn_update_dpp(0u, v, CTRL, 0xf, 0xf, true); }
__device__ __forceinline__ unsigned umax(unsigned a, unsigned b) { return a > b ? a : b; }
__device__ __forceinline__ unsigned row_umax(unsigned v) {
  v = umax(v, dppu<0x128>(v)); v = umax(v, dppu<0x124>(v)); v = umax(v, dppu<0x122>(v)); return umax(v, dppu<0x121>(v));
}
__device__ __forceinline__ unsigned wave_umax(unsigned v) {
  v = row_umax(v);
  auto a = __builtin_amdgcn_permlane16_swap(v, v, false, false);
  v = umax(a[0], a[1]);
  auto c = __builtin_amdgcn_permlane32_swap(v, v, false, false);
  return umax(c[0], c[1]);
}
__device__ __forceinline__ float sum16(float a, float b) {
  auto r = __builtin_amdgcn_permlane16_swap(__float_as_uint(a), __float_as_uint(b), false, false);
  return __uint_as_float(r[0]) + __uint_as_float(r[1]);
}
__device__ __forceinline__ float sum32(float v) {
  auto c = __builtin_amdgcn_permlane32_swap(__float_as_uint(v), __float_as_uint(v), false, false);
  return __uint_as_float(c[0]) + __uint_as_float(c[1]);
}
__device__ __forceinline__ float sq2(unsigned v, float c) {
  return __builtin_amdgcn_fdot2_f32_bf16(__builtin_bit_cast(bf16x2, v), __builtin_bit_cast(bf16x2, v), c, false);
}
__device__ __forceinline__ unsigned opaque(unsigned v) { asm volatile("" : "+v"(v)); return v; }
__device__ __forceinline__ int lane_id() {
  int l;
  asm volatile("v_mbcnt_lo_u32_b32 %0, -1, 0\n\tv_mbcnt_hi_u32_b32 %0, -1, %0" : "=v"(l));
  __builtin_assume(l >= 0 && l < 64);
  return l;
}
struct cf { float x, y; };
__device__ __forceinline__ cf cmul(cf a, cf b) { return {a.x * b.x - a.y * b.y, a.x * b.y + a.y * b.x}; }
__device__ __forceinline__ cf cexp_(float re, float im) {
  float m = __builtin_amdgcn_exp2f(re * 1.44269504088896f);
  float f = __builtin_amdgcn_fractf(im * 0.159154943091895f);
  return {m * __builtin_amdgcn_cosf(f), m * __builtin_amdgcn_sinf(f)};
}
#define GLD1(dst, voff, base) asm volatile("global_load_dword %0, %1, %2" : "=v"(dst) : "v"(voff), "s"(base))
__device__ __forceinline__ void gdma4(unsigned voff, const void* base, unsigned lds_dst) {
  unsigned keep;
  asm volatile("s_mov_b32 %0, m0\n\ts_mov_b32 m0, %3\n\ts_nop 0\n\tglobal_load_lds_dwordx4 %1, %2\n\tglobal_load_lds_dwordx4 %1, %2 offset:1024\n\t"
               "global_load_lds_dwordx4 %1, %2 offset:2048\n\tglobal_load_lds_dwordx4 %1, %2 offset:3072\n\ts_mov_b32 m0, %0"
               : "=&s"(keep) : "v"(voff), "s"(base), "s"(lds_dst) : "memory");
}
#define DMA_PAIR "global_load_lds_dwordx4 %2, %1\n\ts_add_u32 m0, m0, 0x400\n\ts_nop 0\n\tglobal_load_lds_dwordx4 %3, %1\n\ts_add_u32 m0, m0, 0x400\n\tv_add_u32 %2, 0x4000, %2\n\tv_add_u32 %3, 0x4000, %3\n\t"
__device__ __forceinline__ void gdma_rows16(unsigned v0, unsigned v1, const void* base, unsigned lds_dst) {
  unsigned keep;
  asm volatile("s_mov_b32 %0, m0\n\ts_mov_b32 m0, %4\n\ts_nop 0\n\t" DMA_PAIR DMA_PAIR DMA_PAIR DMA_PAIR DMA_PAIR DMA_PAIR DMA_PAIR DMA_PAIR "s_mov_b32 m0, %0"
               : "=&s"(keep), "+s"(base), "+v"(v0), "+v"(v1) : "s"(lds_dst) : "memory", "scc");
}
#define DMA_X(v, o) "global_load_lds_dwordx4 " v ", %1 offset:" #o "\n\ts_add_u32 m0, m0, "
__device__ __forceinline__ void gdma_x(unsigned v0, unsigned v1, const void* base, unsigned lds_dst) {
  unsigned keep;
  asm volatile("s_mov_b32 %0, m0\n\ts_mov_b32 m0, %4\n\ts_nop 0\n\t"
               DMA_X("%2", 0) "896\n\ts_nop 0\n\t" DMA_X("%2", 128) "896\n\ts_nop 0\n\t" DMA_X("%2", 256) "896\n\ts_nop 0\n\t" DMA_X("%2", 384) "1408\n\ts_nop 0\n\t"
               DMA_X("%3", 0) "896\n\ts_nop 0\n\t" DMA_X("%3", 128) "896\n\ts_nop 0\n\t" DMA_X("%3", 256) "896\n\ts_nop 0\n\t" DMA_X("%3", 384) "0\n\t"
               "s_mov_b32 m0, %0"
               : "=&s"(keep) : "s"(base), "v"(v0), "v"(v1), "s"(lds_dst) : "memory", "scc");
}
__device__ __forceinline__ unsigned wswz(unsigned row) { return ((((row >> 1) & 3u) ^ (((row >> 3) & 1u) << 1)) << 1); }

__device__ __forceinline__ void wait4(unsigned addr, unsigned want) {
  _Pragma("unroll 1") for (unsigned spins = 0; spins < kSpinCap; ++spins) {
    u32x4 v;
    asm volatile("ds_read_b128 %0, %1\n\ts_waitcnt lgkmcnt(0)" : "=v"(v) : "v"(addr) : "memory");
    if (__builtin_amdgcn_readfirstlane(v[0] & v[1] & v[2] & v[3]) == want) break;
    __builtin_amdgcn_s_sleep(1);
  }
}
__device__ __forceinline__ void post(unsigned addr, unsigned val) {
  asm volatile("ds_write_b32 %0, %1" :: "v"(addr), "v"(val) : "memory");
}

__device__ __forceinline__ void sync4(unsigned sb, int w, int k) {
  post(sb + L_M + M_SW + k * 16 + w * 4, 1u);
  wait4(sb + L_M + M_SW + k * 16, 1u);
}

__device__ __forceinline__ void arrive8(unsigned addr) {
  asm volatile("s_mov_b64 exec, 1\n\tds_add_u32 %0, %1\n\ts_mov_b64 exec, -1" :: "v"(addr), "v"(1u) : "memory");
}
__device__ __forceinline__ void wait8(unsigned addr, unsigned count = 8u) {
  _Pragma("unroll 1") for (unsigned spins = 0; spins < kSpinCap; ++spins) {
    unsigned v;
    asm volatile("ds_read_b32 %0, %1\n\ts_waitcnt lgkmcnt(0)" : "=v"(v) : "v"(addr) : "memory");
    if (__builtin_amdgcn_readfirstlane(v) >= count) break;
  }
}

#define STR_(x) #x
#define STR(x) STR_(x)
#define VR(b, o) "v[" STR(b) "+" STR(o) "]"
#define VRR(b, o, n) "v[" STR(b) "+" STR(o) ":" STR(b) "+" STR(o) "+" STR(n) "-1]"
#define AR4(i) "a[" STR(i) ":" STR(i) "+3]"
#define C4(p, a, b, c, d) p #a, p #b, p #c, p #d
#define C8(p, a, b, c, d, e, f, g, h) p #a, p #b, p #c, p #d, p #e, p #f, p #g, p #h
#define CLOB_V36_83 C4("v", 36, 37, 38, 39), C8("v", 40, 41, 42, 43, 44, 45, 46, 47), C8("v", 48, 49, 50, 51, 52, 53, 54, 55), C8("v", 56, 57, 58, 59, 60, 61, 62, 63), \
  C8("v", 64, 65, 66, 67, 68, 69, 70, 71), C8("v", 72, 73, 74, 75, 76, 77, 78, 79), C4("v", 80, 81, 82, 83)
#define CLOB_V4_83 C4("v", 4, 5, 6, 7), C8("v", 8, 9, 10, 11, 12, 13, 14, 15), C8("v", 16, 17, 18, 19, 20, 21, 22, 23), C8("v", 24, 25, 26, 27, 28, 29, 30, 31), \
  C4("v", 32, 33, 34, 35), CLOB_V36_83
#define CLOB_A4_47 C4("a", 4, 5, 6, 7), C8("a", 8, 9, 10, 11, 12, 13, 14, 15), C8("a", 16, 17, 18, 19, 20, 21, 22, 23), C8("a", 24, 25, 26, 27, 28, 29, 30, 31), \
  C8("a", 32, 33, 34, 35, 36, 37, 38, 39), C8("a", 40, 41, 42, 43, 44, 45, 46, 47)
typedef int i32x4 __attribute__((ext_vector_type(4)));
__device__ __forceinline__ i32x4 make_srd(const void* p, unsigned bytes) {
  const unsigned long long a = (unsigned long long)p;
  return (i32x4){(int)(unsigned)a, (int)((unsigned)(a >> 32) & 0xffffu), (int)bytes, 0x00020000};
}

#define A_EX(S, i) "v_exp_f32 " VR(S, i) ", " VR(S, i) "\n\t"
#define A_QK(S, p, q) "v_mfma_f32_32x32x16_bf16 " VRR(S, 0, 16) ", " AR4(4 + 4 * p) ", " AR4(28 + 4 * q) ", v[68:83]\n\t"
#define A_PV0(o, p, S) "v_smfmac_f32_32x32x32_bf16 " VRR(4, 16 * o, 16) ", " AR4(12 + 4 * p) ", " VRR(S, 0, 8) ", v116\n\t"
#define A_PV1(o, p, S) "v_smfmac_f32_32x32x32_bf16 " VRR(4, 16 * o, 16) ", " AR4(20 + 4 * p) ", " VRR(S, 8, 8) ", v116\n\t"
#define A_RD4(o, q) "v_accvgpr_read_b32 " VR(68, o) ", a[44+" #q "]\n\tv_accvgpr_read_b32 " VR(69, o) ", a[44+" #q "]\n\t" \
                    "v_accvgpr_read_b32 " VR(70, o) ", a[44+" #q "]\n\tv_accvgpr_read_b32 " VR(71, o) ", a[44+" #q "]\n\t"
#define A_SHIFT_EXACT(q) A_RD4(0, q) A_RD4(4, q) A_RD4(8, q) A_RD4(12, q) "s_nop 1\n\t"
#define A_SHIFT_NONE(q) ""
#define A_BLOCK(Sc, Sp, op, pp, pn, qn, oc, pc, SH, XM, X3, X5, X7, X10, X12) \
  A_EX(Sc, 0) A_EX(Sc, 1) A_PV1(op, pp, Sp) XM A_EX(Sc, 2) A_EX(Sc, 3) X3 A_EX(Sc, 4) A_EX(Sc, 5) X5 SH(qn) A_QK(Sp, pn, qn) \
  A_EX(Sc, 6) A_EX(Sc, 7) X7 A_EX(Sc, 8) A_EX(Sc, 9) A_EX(Sc, 10) X10 A_PV0(oc, pc, Sc) \
  A_EX(Sc, 11) A_EX(Sc, 12) X12 A_EX(Sc, 13) A_EX(Sc, 14) A_EX(Sc, 15)
#define A_LK(p, so) "buffer_load_dwordx4 " AR4(4 + 4 * p) ", %[ko], %[rk], " so " offen sc1\n\t"
#define A_LV0(p, so) "buffer_load_dwordx4 " AR4(12 + 4 * p) ", %[vo], %[rk], " so " offen sc1\n\t"
#define A_LV1(p, so) "buffer_load_dwordx4 " AR4(20 + 4 * p) ", %[vo], %[rk], " so " offen offset:640 sc1\n\t"
#define A_NEXT "s_add_u32 %[tl], %[tl], 1\n\ts_and_b32 %[tl], %[tl], 63\n\t"
#define A_W3 "s_waitcnt vmcnt(3)\n\t"
#define A_STEP(p, pq, SH, XM, E) \
  A_BLOCK(84, 100, 3, pq, p, 1, 0, p, SH, XM, A_LV1(pq, "%[sa]"), "", "", A_W3, "") \
  A_BLOCK(100, 84, 0, p, p, 2, 1, p, SH, "", "s_mov_b32 %[sa], %[sb]\n\t", "", A_NEXT, "", "") \
  A_BLOCK(84, 100, 1, p, p, 3, 2, p, SH, "", "", "", A_LK(p, "%[sb]"), "", "") \
  A_BLOCK(100, 84, 2, p, pq, 0, 3, p, SH, "", "", A_W3, E, "", A_LV0(p, "%[sb]") "s_mul_i32 %[sb], %[tl], 2304\n\t")
#define A_MOV4(o) "v_mov_b32 " VR(68, o) ", %[nb]\n\tv_mov_b32 " VR(69, o) ", %[nb]\n\tv_mov_b32 " VR(70, o) ", %[nb]\n\tv_mov_b32 " VR(71, o) ", %[nb]\n\t"
#define A_ZERO(o) "v_mfma_f32_32x32x8_bf16 " VRR(4, 16 * o, 16) ", v[116:117], v[116:117], 0\n\t"
#define A_FLUSH(o) "ds_write_b128 v118, " VRR(4, 16 * o, 4) " offset:2304*" #o "\n\tds_write_b128 v118, " VRR(8, 16 * o, 4) " offset:2304*" #o "+1024\n\t" \
                   "ds_write_b32 v119, " VR(12, 16 * o) " offset:2304*" #o "+2048\n\t"
#define A_FIRST \
  "s_mul_i32 %[sb], %[tl], 2304\n\t" A_NEXT "s_mul_i32 %[sa], %[tl], 2304\n\t" A_LK(0, "%[sb]") A_LV0(0, "%[sb]") A_LV1(0, "%[sb]") A_LK(1, "%[sa]") A_LV0(1, "%[sa]")
#define A_ATT(SH) \
  A_NEXT "s_mul_i32 %[sa], %[tl], 2304\n\t" A_NEXT "s_mul_i32 %[sb], %[tl], 2304\n\t" \
  "v_add_u32 v118, %[qb], %[ko]\n\tv_mov_b32 v116, 0\n\tv_mov_b32 v117, 0\n\t" \
  "ds_read_b128 a[28:31], v118\n\tds_read_b128 a[32:35], v118 offset:1024\n\tds_read_b128 a[36:39], v118 offset:2048\n\tds_read_b128 a[40:43], v118 offset:3072\n\t" \
  A_MOV4(0) A_ZERO(0) A_MOV4(4) A_ZERO(1) A_MOV4(8) A_ZERO(2) A_MOV4(12) A_ZERO(3) \
  "v_lshrrev_b32 v119, 2, %[ko]\n\tv_add_u32 v118, %[ob], %[ko]\n\tv_add_u32 v119, %[ob], v119\n\t" \
  "s_waitcnt vmcnt(4) lgkmcnt(0)\n\t" SH(0) A_QK(84, 0, 0) "v_mov_b32 v116, 0xdddd\n\ts_setprio 1\n\ts_cmp_eq_u32 %[sw], 0\n\ts_cbranch_scc1 14f\n\ts_setprio 2\n\t14:\n\ts_nop 13\n\t" \
  A_EX(84, 0) A_EX(84, 1) "s_branch 11f\n\t" \
  "10:\n\t" \
  A_STEP(0, 1, SH, "11:\n\t", "") \
  A_STEP(1, 0, SH, "", "s_sub_u32 %[n], %[n], 1\n\t") "s_cmp_lg_u32 %[n], %[sw]\n\ts_cbranch_scc1 10b\n\ts_setprio 0\n\ts_mov_b32 %[sw], 0\n\ts_cmp_lg_u32 %[n], 0\n\ts_cbranch_scc1 10b\n\t" \
  A_PV1(3, 1, 100) "s_setprio 0\n\tv_mov_b32 v117, %[pf]\n\ts_mov_b32 %[n], 0x100000\n\t" \
  "12:\n\tds_read_b128 v[84:87], v117\n\ts_waitcnt lgkmcnt(0)\n\tv_and_b32 v84, v84, v85\n\tv_and_b32 v86, v86, v87\n\tv_and_b32 v84, v84, v86\n\t" \
  "s_sub_u32 %[n], %[n], 1\n\ts_cbranch_scc1 13f\n\tv_cmp_eq_u32 vcc, 0, v84\n\ts_cbranch_vccnz 12b\n\t13:\n\t" \
  A_FLUSH(0) A_FLUSH(1) A_FLUSH(2) "s_nop 3\n\t" A_FLUSH(3)

#pragma clang diagnostic ignored "-Winline-asm"
__global__ __launch_bounds__(768, 3) void fused_kernel(
    const unsigned short* __restrict__ u, const unsigned short* __restrict__ Are, const unsigned short* __restrict__ Aim,
    const unsigned short* __restrict__ Bre, const unsigned short* __restrict__ Bim, const unsigned short* __restrict__ Cre,
    const unsigned short* __restrict__ Cim, const unsigned* __restrict__ logdt2, const unsigned* __restrict__ dskip2,
    const unsigned short* __restrict__ x, const unsigned short* __restrict__ wq, const unsigned short* __restrict__ wk,
    const unsigned short* __restrict__ wv, const unsigned short* __restrict__ wo, unsigned short* __restrict__ out,
    unsigned char* __restrict__ ws) {
  __shared__ __attribute__((aligned(128))) unsigned char smem[LDS_BYTES];
  unsigned long long pc;
  asm volatile("s_getpc_b64 %0" : "=s"(pc) :: "v119", "a47");
  const unsigned sb = (unsigned)(size_t)(LAS unsigned char*)smem;
  const int w = __builtin_amdgcn_readfirstlane(threadIdx.x >> 6);
  const int b = blockIdx.x, xcd = b & 7, bi = b >> 3;
  const int grp = xcd * 4 + (bi >> 3), hg = bi & 7, cp = xcd * 32 + bi;
#define LANE_VARS() const int lane = lane_id(), h = lane >> 5, g4 = lane >> 4, l15 = lane & 15, l31 = lane & 31; \
  const unsigned r8 = lane >> 3, c8 = lane & 7; (void)h; (void)g4; (void)l15; (void)l31; (void)r8; (void)c8
  STAMP_INIT();
  asm volatile("" :: "s"(u), "s"(Are), "s"(Aim), "s"(Bre), "s"(Bim), "s"(Cre), "s"(Cim), "s"(logdt2), "s"(dskip2), "s"(x), "s"(wq),
               "s"(wk), "s"(wv), "s"(wo), "s"(out), "s"(ws));

  if (w >= 8) {
    LANE_VARS();
    const int j = w - 8, tid = j * 64 + lane, tch = j >> 1, par = j & 1;
    unsigned pAr, pAi, pBr, pBi, pCr, pCi, ld2, d2, cpf = 0;
    const unsigned po = (unsigned)((2 * cp + tch) * 256 + lane * 4);
    GLD1(pAr, po, Are); GLD1(pAi, po, Aim); GLD1(pBr, po, Bre); GLD1(pBi, po, Bim); GLD1(pCr, po, Cre); GLD1(pCi, po, Cim);
    LD32(sb + L_M + l31 * 4) = 0;
    asm volatile("s_load_dword %0, %2, %4\n\ts_load_dword %1, %3, %4\n\ts_waitcnt lgkmcnt(0)\n\ts_barrier"
                 : "=&s"(ld2), "=&s"(d2) : "s"(logdt2), "s"(dskip2), "s"(cp * 4) : "memory");
    const float ldt = tch ? bfhi(ld2) : bflo(ld2);
    const float eh = ldt * 1.44269502162933349609f, el = fmaf(ldt, 1.44269502162933349609f, -eh) + ldt * 1.925963033500011e-8f;
    const float e0 = __builtin_amdgcn_exp2f(eh);
    const float dt = bflo(pkbf(fmaf(e0, el * 0.693147180559945f, e0), 0.f));
    asm volatile("s_waitcnt vmcnt(0)" : "+v"(pAr), "+v"(pAi), "+v"(pBr), "+v"(pBi), "+v"(pCr), "+v"(pCi), "+v"(cpf) :: "memory");
    STAMP(1);
    const float ar0 = dt * bflo(pAr), ar1 = dt * bfhi(pAr);
    float lam = 0.f;
    if (__any(fmaxf(ar0, ar1) > 0.f)) lam = __uint_as_float(wave_umax(__float_as_uint(fmaxf(fmaxf(ar0, ar1), 0.f))));
    const float ar = (par ? ar1 : ar0) - lam, ai = dt * (par ? bfhi(pAi) : bflo(pAi));
    unsigned T[21];
    {
      cf c0 = cmul({bflo(pCr), bflo(pCi)}, {bflo(pBr), bflo(pBi)});
      cf c1 = cmul({bfhi(pCr), bfhi(pCi)}, {bfhi(pBr), bfhi(pBi)});
      int ex = (int)(wave_umax(__float_as_uint(fmaxf(fmaxf(fabsf(c0.x), fabsf(c0.y)), fmaxf(fabsf(c1.x), fabsf(c1.y))))) >> 23);
      ex = ex < 8 ? 8 : (ex > 245 ? 245 : ex);
      const float sdn = __uint_as_float((unsigned)(252 - ex) << 23), sup = __uint_as_float((unsigned)(ex + 2) << 23) * dt;
      LD64(sb + L_M + M_SCALE + tch * 8) = (u32x2){__float_as_uint(sup), __float_as_uint(lam * 1.44269504088896f)};
      cf c = par ? c1 : c0; c.x *= sdn; c.y *= sdn;
      const cf w1 = cexp_(ar, ai), W = cexp_(256.f * ar, 256.f * ai);
      const cf w2 = cmul(w1, w1), w4 = cmul(w2, w2), w6 = cmul(w4, w2);
      cf v = c;
#pragma unroll
      for (int k = 0; k < 8; ++k) { T[k] = pkh(v.x, v.y); if (k < 7) v = cmul(v, w1); }
      v = cmul(c, W); T[8] = pkh(v.x, v.y);
      cf e = cmul(v, w2); T[9] = pkh(e.x, e.y);
      e = cmul(v, w4); T[10] = pkh(e.x, e.y);
      e = cmul(v, w6); T[11] = pkh(e.x, e.y);
      v = cmul(v, W); T[12] = pkh(v.x, v.y);
      e = cmul(v, w4); T[13] = pkh(e.x, e.y);
      v = cmul(v, W); T[14] = pkh(v.x, v.y);
      e = cmul(v, w4); T[15] = pkh(e.x, e.y);
#pragma unroll
      for (int k = 16; k < 21; ++k) { v = cmul(v, W); T[k] = pkh(v.x, v.y); }
    }
    {
      cf p[16];
      cf g = cexp_(8.f * ar, -8.f * ai);
      p[0] = {1.f, 0.f}; p[1] = g;
      g = cmul(g, g); p[2] = g; p[3] = cmul(p[1], g);
      g = cmul(g, g);
      p[4] = g;
#pragma unroll
      for (int i = 1; i < 4; ++i) p[4 + i] = cmul(p[i], g);
      g = cmul(g, g);
      p[8] = g;
#pragma unroll
      for (int i = 1; i < 8; ++i) p[8 + i] = cmul(p[i], g);
      g = cmul(g, g);
#pragma unroll
      for (int i = 0; i < 16; ++i) {
        const cf q = i ? cmul(p[i], g) : g;
        asm volatile("v_cvt_pk_f16_f32 v[84+%c4], %0, %1\n\tv_cvt_pk_f16_f32 v[100+%c4], %2, %3" :: "v"(p[i].x), "v"(p[i].y), "v"(q.x), "v"(q.y), "i"(i));
      }
    }
#pragma unroll
    for (int k = 0; k < 21; ++k) asm volatile("" : "+v"(T[k]));
    STAMP(2);
    BAR();
    const unsigned go = (unsigned)(tid * 8192 + cp * 4);
#define GATHER(i, R) asm volatile("global_load_dword v" #R ", %0, %1 offset:1024*(" #i "&3)" :: "v"(go), "s"(u + (i >> 2) * 2048) : "memory")
    const unsigned tag = __builtin_amdgcn_readfirstlane(LD32(sb + L_M + M_TAG));
    if (j == 0) {
      u32x4 f0, f1, g0, g1;
      unsigned fa, km, spins = kSpinCap;
      const unsigned fo = W_F1 + (b & (NREP - 1)) * 2048 + lane * 16;
      __builtin_amdgcn_s_sleep(WSLEEP);
#define F_LD(A, B) "global_load_dwordx4 " A ", %[fo], %[ws] sc1\n\tglobal_load_dwordx4 " B ", %[fo], %[ws] offset:1024 sc1\n\t"
      asm volatile(F_LD("%0", "%1") : "=&v"(f0), "=&v"(f1) : [fo] "v"(fo), [ws] "s"(ws) : "memory");
      _Pragma("unroll 1") for (;;) {
        asm volatile(F_LD("%0", "%1") "s_waitcnt vmcnt(2)" : "=&v"(g0), "=&v"(g1), "+v"(f0), "+v"(f1) : [fo] "v"(fo), [ws] "s"(ws) : "memory");
        fa = (f0[1] ^ tag) | (f0[3] ^ tag) | (f1[1] ^ tag) | (f1[3] ^ tag); km = umax(f0[0], f0[2]);
        if (__all(fa == 0u)) break;
        asm volatile(F_LD("%2", "%3") "s_waitcnt vmcnt(2)" : "+v"(g0), "+v"(g1), "=&v"(f0), "=&v"(f1) : [fo] "v"(fo), [ws] "s"(ws) : "memory");
        fa = (g0[1] ^ tag) | (g0[3] ^ tag) | (g1[1] ^ tag) | (g1[3] ^ tag); km = umax(g0[0], g0[2]);
        if (__all(fa == 0u) || --spins == 0) break;
      }
      LD32(sb + L_M + M_KMAX) = wave_umax(km);
      LD32(sb + L_M + M_RDY) = 1u;
      arrive8(sb + L_M + M_AB);
      asm volatile("s_waitcnt vmcnt(0)" : "+v"(f0), "+v"(f1), "+v"(g0), "+v"(g1) :: "memory");
      if (b == 0 && lane == 0) __hip_atomic_store((gu32*)(ws + W_E), tag, RLX_AGENT);
    }
    STAMP(4);
    wait8(sb + L_M + M_AB, 9u);
    GATHER(0, 116);
    __builtin_amdgcn_s_setprio(SSM_PRIO);
    {
      const unsigned c4 = (unsigned)(par * 64 + lane) * 4, tlo = sb + L_TLO + tch * TLO_CH + c4, thi = sb + L_THI + tch * THI_CH + c4;
#pragma unroll
      for (int i = 0; i < 32; ++i) asm volatile("ds_write_b32 %0, v[84+%c1] offset:%c2" :: "v"(tlo), "i"(i), "i"(i * 528) : "memory");
      GATHER(1, 117);
#pragma unroll
      for (int i = 0; i < 21; ++i) LD32(thi + i * 528) = T[i];
    }
    if (tid < 248) { const int c = tid >= 124, i = tid - 124 * c; LD128(sb + L_U + c * U_CH + (i >> 2) * 80 + (i & 3) * 16) = (u32x4){0u, 0u, 0u, 0u}; }
    GATHER(2, 118);
    sync4(sb, j, 0);
    STAMP(5);
    const unsigned KT = sb + L_P;
    {
      const unsigned aa = sb + L_TLO + tch * TLO_CH + (16 * par + l15) * 528 + g4 * 16;
      const unsigned b0 = sb + L_THI + tch * THI_CH + l15 * 528 + g4 * 16, b1 = sb + L_THI + tch * THI_CH + (l15 < 4 ? 16 + l15 : 20) * 528 + g4 * 16;
      const u32x2 sc = LD64(sb + L_M + M_SCALE + tch * 8);
      f32x4 k0, k1;
#pragma unroll
      for (int ks = 0; ks < 8; ++ks) {
        const u32x4 fa = LD128(aa + ks * 64), fb = LD128(b0 + ks * 64), fc = LD128(b1 + ks * 64);
        if (ks == 0) asm volatile("v_mfma_f32_16x16x32_f16 %0, %3, %2, 0\n\tv_mfma_f32_16x16x32_f16 %1, %4, %2, 0" : "=&v"(k0), "=&v"(k1) : "v"(fa), "v"(fb), "v"(fc));
        else asm volatile("v_mfma_f32_16x16x32_f16 %0, %3, %2, %0\n\tv_mfma_f32_16x16x32_f16 %1, %4, %2, %1" : "+v"(k0), "+v"(k1) : "v"(fa), "v"(fb), "v"(fc));
        if (ks == 3) GATHER(3, 119);
      }
      asm volatile("s_nop 7" : "+v"(k0), "+v"(k1));
      const int lb = 128 * par + 8 * l15;
      const int l0 = lb + (g4 < 2 ? 4 * g4 : 256 * (g4 - 1)), s1 = g4 < 2 ? 1 : 2 * (g4 - 1), s2 = g4 < 3 ? 2 * s1 : 256, l1 = lb + 1024 + 1024 * g4;
      k0 *= __uint_as_float(sc[0]); k1 *= __uint_as_float(sc[0]);
      if (__builtin_amdgcn_readfirstlane(sc[1])) {
#pragma unroll
        for (int r = 0; r < 4; ++r) {
          k0[r] *= __builtin_amdgcn_exp2f(__uint_as_float(sc[1]) * (float)(l0 + (r & 1) * s1 + (r >> 1) * s2));
          k1[r] *= __builtin_amdgcn_exp2f(__uint_as_float(sc[1]) * (float)(l1 + 256 * r));
        }
      }
      if (l0 == 0) k0[0] += tch ? bfhi(d2) : bflo(d2);
      const unsigned long long g23 = 0xffffffff00000000ull, g3 = 0xffff000000000000ull;
      const unsigned long long g23d = par ? g23 : g23 & ~(1ull << 32), g3d = par ? g3 : g3 & ~(1ull << 48);
      const unsigned long long a0 = par ? 0xffffull : 0x1ffffull, a0d = par ? 0xffffull : 0x1fffeull, e0 = par ? 0x5555ull : 0x15555ull, e0d = par ? 0x5555ull : 0x15554ull;
      const unsigned ka0 = KT + l0 * 4 + 2 * tch, ka2 = ka0 + s2 * 4;
#define KW(R, o) "ds_write_b16" R " offset:" STR(o) "\n\t"
#define KR0 " %0, %5"
#define KR1 "_d16_hi %1, %5"
#define KR2 " %2, %6"
#define KR3 "_d16_hi %3, %6"
#define KS0 " %4, %7"
#define KS1 "_d16_hi %4, %7"
#define KS2 " %4, %8"
#define KS3 "_d16_hi %4, %8"
#define KW6(R, o) KW(R, o - 8) KW(R, o - 4) KW(R, o) KW(R, o + 4) KW(R, o + 8) KW(R, o + 12)
      asm volatile(KW(KR0, 16) KW(KR1, 16) KW(KR2, 16) KW(KR3, 16)
                   "s_mov_b64 exec, %9\n\t" KW(KR0, 20) KW(KR2, 20) KW(KR2, 12)
                   "s_mov_b64 exec, %10\n\t" KW(KR0, 12)
                   "s_mov_b64 exec, %11\n\t" KW(KR1, 20) KW(KR1, 12) KW(KR3, 20) KW(KR3, 12) KW(KR0, 24) KW(KR2, 24) KW(KR2, 8)
                   "s_mov_b64 exec, %12\n\t" KW(KR0, 8)
                   "s_mov_b64 exec, %13\n\t" KW6(KS0, 16)
                   "s_mov_b64 exec, %14\n\t" KW(KS0, 4)
                   "s_mov_b64 exec, %15\n\t" KW(KS0, 32)
                   "s_mov_b64 exec, %16\n\t" KW(KS0, 0)
                   "s_mov_b64 exec, 0xffff\n\t" KW6(KS1, 1040) KW(KS1, 1028) KW6(KS2, 2064) KW(KS2, 2052) KW6(KS3, 3088) KW(KS3, 3076)
                   "s_mov_b64 exec, 0x5555\n\t" KW(KS1, 1056) KW(KS1, 1024) KW(KS2, 2080) KW(KS2, 2048) KW(KS3, 3104) KW(KS3, 3072)
                   "s_mov_b64 exec, -1"
                   :: "v"(ka0), "v"(ka0 + s1 * 4), "v"(ka2), "v"(ka2 + s1 * 4), "v"(KT + l1 * 4 + 2 * tch), "v"(pkbf(k0[0], k0[1])), "v"(pkbf(k0[2], k0[3])),
                      "v"(pkbf(k1[0], k1[1])), "v"(pkbf(k1[2], k1[3])), "s"(g23), "s"(g23d), "s"(g3), "s"(g3d), "s"(a0), "s"(a0d), "s"(e0), "s"(e0d) : "memory");
      if (j == 0) LD128(KT) = (u32x4){0u, 0u, 0u, 0u};
    }
    post(sb + L_M + M_SW + 16 + j * 4, 1u);
    GATHER(4, 84);
    wait4(sb + L_M + M_SW + 16, 1u);
    STAMP(6);
#pragma unroll
    for (int k = 0; k < 2; ++k) {
      if (k == 0) GATHER(5, 85); else GATHER(6, 86);
      const u32x4 da = LD128(KT + tid * 16 + k * 4096), db = LD128(KT + tid * 16 + k * 4096 + 16);
      const unsigned d[7] = {da[1], da[2], da[3], db[0], db[1], db[2], db[3]};
      const unsigned ka = sb + L_KC + 224 + tid * 32 + k * 8192;
#pragma unroll
      for (int c = 0; c < 2; ++c) {
        unsigned P[6];
#pragma unroll
        for (int i = 0; i < 6; ++i) P[i] = __builtin_amdgcn_perm(d[i], d[i + 1], c ? 0x07060302u : 0x05040100u);
        LD128(ka + c * KC_CH) = (u32x4){P[2], P[0], P[3], P[1]};
        LD128(ka + c * KC_CH + 16) = (u32x4){P[4], P[2], P[5], P[3]};
      }
    }
    if (tid < 28) LD128(sb + L_KC + (tid >= 14) * (KC_CH - 224) + tid * 16) = (u32x4){0u, 0u, 0u, 0u};
    STAMP(7);
    __builtin_amdgcn_s_sleep(GSLEEP);
    GATHER(7, 87);
    asm volatile("s_waitcnt vmcnt(0)\n\t"
                 "v_perm_b32 v88, v117, v116, %1\n\tv_perm_b32 v89, v119, v118, %1\n\tv_perm_b32 v90, v85, v84, %1\n\tv_perm_b32 v91, v87, v86, %1\n\t"
                 "v_perm_b32 v92, v117, v116, %2\n\tv_perm_b32 v93, v119, v118, %2\n\tv_perm_b32 v94, v85, v84, %2\n\tv_perm_b32 v95, v87, v86, %2\n\t"
                 "ds_write_b128 %0, v[88:91]\n\tds_write_b128 %0, v[92:95] offset:7600"
                 :: "v"(sb + L_U + (31 + (tid >> 2)) * 80 + (tid & 3) * 16), "s"(0x05040100u), "s"(0x07060302u) : "memory");
    sync4(sb, j, 2);
    STAMP(8);
    unsigned aA = sb + L_KC + tch * KC_CH + (unsigned)(8 * (16 * (32 * par - 1) + l31 - 8 * h + 24));
    unsigned aF = sb + L_U + tch * U_CH + (unsigned)(80 * (l31 + 31 - 16 * par) + 16 * h);
    f32x16 y0, y1;
    unsigned cl;
    const u32x2 zz = {0u, 0u};
#define CLD(R0, R1, R2, R3, R4, R5, OA, OF) \
      "ds_read_b64 v[" #R0 "], %3 offset:" #OA "+32\n\tds_read_b64 v[" #R1 "], %3 offset:" #OA "\n\tds_read_b128 v[" #R4 "], %4 offset:" #OF "\n\t" \
      "ds_read_b128 v[" #R5 "], %4 offset:" #OF "+2560\n\tds_read_b64 v[" #R2 "], %3 offset:" #OA "+8224\n\tds_read_b64 v[" #R3 "], %3 offset:" #OA "+8192\n\t"
#define CMM(A0, A1, F0, F1) "s_waitcnt lgkmcnt(6)\n\tv_mfma_f32_32x32x16_bf16 %0, v[" #A0 "], v[" #F0 "], %0\n\t" \
      CPAD "v_mfma_f32_32x32x16_bf16 %1, v[" #A0 "], v[" #F1 "], %1\n\t" CPAD "v_mfma_f32_32x32x16_bf16 %1, v[" #A1 "], v[" #F0 "], %1\n\t" CPAD
    asm volatile(
        "s_setprio " STR(CONV_PRIO) "\n\ts_nop 1\n\tv_mfma_f32_32x32x8_bf16 %0, %5, %5, 0\n\tv_mfma_f32_32x32x8_bf16 %1, %5, %5, 0\n\ts_mov_b32 %2, " STR(CONV_N) "\n\t"
        CLD(84:85, 86:87, 88:89, 90:91, 92:95, 96:99, 0, 32)
        "1:\n\t"
        CLD(100:101, 102:103, 104:105, 106:107, 108:111, 112:115, 128, 0)
        CMM(84:87, 88:91, 92:95, 96:99)
        "v_add_u32 %3, 0x100, %3\n\tv_add_u32 %4, 0xffffffb0, %4\n\t"
        CLD(84:85, 86:87, 88:89, 90:91, 92:95, 96:99, 0, 32)
        CMM(100:103, 104:107, 108:111, 112:115)
        "s_sub_u32 %2, %2, 1\n\ts_cmp_lg_u32 %2, 0\n\ts_cbranch_scc1 1b\n\t"
        "s_setprio " STR(SSM_PRIO) "\n\ts_waitcnt lgkmcnt(0)\n\ts_nop 15"
        : "=&v"(y0), "=&v"(y1), "=&s"(cl), "+v"(aA), "+v"(aF) : "v"(zz) : "memory", "scc");
    const unsigned pa = opaque(sb + L_P + lane * 16), pw = opaque(pa + j * 8192);
#pragma unroll
    for (int q = 0; q < 4; ++q) {
      LDF4(pw + q * 1024) = (f32x4){y0[4 * q], y0[4 * q + 1], y0[4 * q + 2], y0[4 * q + 3]};
      LDF4(pw + 4096 + q * 1024) = (f32x4){y1[4 * q], y1[4 * q + 1], y1[4 * q + 2], y1[4 * q + 3]};
    }
    STAMP(9);
    sync4(sb, j, 3);
    const unsigned wo0 = (128 * j + r8) * 1024 + hg * 128 + ((c8 ^ wswz(r8)) << 4);
    gdma_rows16(wo0, (wo0 ^ 64) + 8192, wo, sb + L_WO + j * 16384);
    const unsigned pr = opaque(pa + tch * 4096 + par * 2048);
    unsigned yv[8];
#pragma unroll
    for (int qi = 0; qi < 2; ++qi) {
      const unsigned o = pr + qi * 1024;
      const f32x4 ya = LDF4(o) + LDF4(o + 8192), yb = LDF4(o + 16384) + LDF4(o + 24576);
#pragma unroll
      for (int e = 0; e < 4; ++e) yv[4 * qi + e] = pkbf(ya[e], yb[e]);
    }
    post(sb + L_M + M_SW + 64 + j * 4, 1u);
    const __amdgpu_buffer_rsrc_t rsY = __builtin_amdgcn_make_buffer_rsrc(out, 0, 4194304, 0x00020000);
    const unsigned yo = (1024 * tch + 32 * l31 + 16 * par + 4 * h) * 2048 + cp * 4;
    STAMP(10);
#pragma unroll
    for (int i = 0; i < NYST; ++i) {
      __builtin_amdgcn_raw_buffer_store_b32(yv[i], rsY, yo + (i & 1) * 2048, (i >> 2) * 16384 + ((i >> 1) & 1) * 4096, 0);
      if (i < 7) asm volatile("s_sleep " STR(YSLEEP) ::: "memory");
    }
    asm volatile("s_waitcnt vmcnt(0)" ::: "memory");
    STAMP(11);
    STAMP_OUT();
    return;
  }

  LANE_VARS();
  const int qq = w & 3, kh = w >> 2, chain = hg >> 2;
  const bool crit = w == (hg & 3);
  if (!crit) asm volatile("s_barrier" ::: "memory");
  else __builtin_amdgcn_s_setprio(3);
  if (kh == 0) {
    const unsigned xo = (64 * grp + 16 * qq + r8) * 1024 + ((c8 ^ (r8 >> 1)) << 4);
    gdma_x(xo, (xo ^ 64) + 8192, x, sb + L_X + qq * 16384);
    gdma_x(xo + 512, (xo ^ 64) + 8704, x, sb + L_X + qq * 16384 + 8192);
  } else {
    const unsigned wq0 = (128 * qq + r8) * 1024 + hg * 128 + ((c8 ^ ((r8 & 2) << 1)) << 4);
    gdma_rows16(wq0, wq0 + 8192, wq, sb + L_WQ + qq * 16384);
  }
  if (crit) {
    const unsigned short* wkv = chain ? wv : wk;
#pragma unroll
    for (int i = 0; i < 4; ++i) gdma4((unsigned)(i * 4096 + lane * 16), wkv, sb + L_R2 + i * 4096);
    asm volatile("s_barrier" ::: "memory");
    unsigned E;
    asm volatile("s_load_dword %0, %1, 0x0\n\ts_waitcnt lgkmcnt(0)" : "=s"(E) : "s"(ws) : "memory");
    const unsigned tg = E + (((E * 0x9E3779B1u ^ (E >> 15) ^ 0x5bd1e995u) & 0x7fffffffu) | 0x101u);
    LD32(sb + L_M + M_TAG) = tg;
    const unsigned wa = sb + L_R2 + (8 * g4 + (l15 >> 2)) * 32 + (lane & 3) * 8;
    const unsigned xe = sb + L_X + qq * 16384 + (l15 >> 3) * 4096 + (l15 & 7) * 128 + ((g4 ^ (l15 >> 1)) << 4);
    const unsigned off = (2 * grp + (qq >> 1)) * KV_TILE + l15 * 16 +
                         (chain ? 1024 + (qq & 1) * 640 + (g4 >> 1) * 320 + (g4 & 1) * 8 : (qq & 1) * 256 + (g4 & 1) * 512 + (g4 >> 1) * 8);
    f32x4 acc, acd;
#define KR(s, o) "v[36+8*(" #s "%%6)+" #o ":36+8*(" #s "%%6)+" #o "+3]"
#define K_LD(s, X) "ds_read_b128 " KR(s, 0) ", " X " offset:8192*(" #s "/8)+1024*((" #s "&7)/2)\n\t" \
      "ds_read_b64_tr_b16 v[36+8*(" #s "%%6)+4:36+8*(" #s "%%6)+5], %4 offset:1024*" #s "\n\tds_read_b64_tr_b16 v[36+8*(" #s "%%6)+6:36+8*(" #s "%%6)+7], %4 offset:1024*" #s "+128\n\t"
#define K_LD2(s, t) K_LD(s, "%2") K_LD(t, "%3")
#define K_MM(N, s, t, A, B, C0, C1) "s_waitcnt lgkmcnt(" #N ")\n\tv_mfma_f32_16x16x32_bf16 %0, " KR(s, A) ", " KR(s, B) ", " C0 "\n\t" \
      "v_mfma_f32_16x16x32_bf16 %1, " KR(t, A) ", " KR(t, B) ", " C1 "\n\t"
#define K_ALL(A, B) asm volatile("s_waitcnt vmcnt(0) lgkmcnt(0)\n\t" K_LD2(0, 1) K_LD2(2, 3) K_LD2(4, 5) K_MM(12, 0, 1, A, B, "0", "0") K_LD2(6, 7) K_MM(12, 2, 3, A, B, "%0", "%1") \
        K_LD2(8, 9) K_MM(12, 4, 5, A, B, "%0", "%1") K_LD2(10, 11) K_MM(12, 6, 7, A, B, "%0", "%1") K_LD2(12, 13) K_MM(12, 8, 9, A, B, "%0", "%1") \
        K_LD2(14, 15) K_MM(12, 10, 11, A, B, "%0", "%1") K_MM(6, 12, 13, A, B, "%0", "%1") K_MM(0, 14, 15, A, B, "%0", "%1") "s_nop 7" \
        : "=&v"(acc), "=&v"(acd) : "v"(xe), "v"(xe ^ 64), "v"(wa) : "memory", CLOB_V36_83)
    if (chain) K_ALL(0, 4); else K_ALL(4, 0);
    STAMP(1);
    acc = (acc + acd) * (chain ? 1.0f : 0.25f * 1.44269504088896f);
    const u32x2 pk = {pkbf(acc[0], acc[1]), pkbf(acc[2], acc[3])};
    const __amdgpu_buffer_rsrc_t rs = __builtin_amdgcn_make_buffer_rsrc(ws + W_KV, 0, KV_BYTES, 0x00020000);
    __builtin_amdgcn_raw_buffer_store_b64(pk, rs, off, 0, 16);
    __builtin_amdgcn_raw_buffer_store_b64((u32x2){0x3f803f80u, 0x3f803f80u}, rs, (chain && l15 == 0) ? off + 256 : 0x80000000u, 0, 16);
    const float n2 = sq2(pk[0], sq2(pk[1], 0.f));
    const unsigned kn2 = row_umax(__float_as_uint(sum32(sum16(n2, n2))));
    asm volatile("s_waitcnt vmcnt(0)" ::: "memory");
    if (lane < NREP) __hip_atomic_store((gu64*)(ws + W_F1) + lane * 256 + chain * 128 + 4 * grp + qq, ((unsigned long long)tg << 32) | kn2, RLX_AGENT);
    __builtin_amdgcn_s_setprio(0);
    STAMP(2);
  } else {
    asm volatile("s_waitcnt vmcnt(0)" ::: "memory");
  }
  STAMP(3);
  BAR();
  STAMP(4);

  const unsigned tag = __builtin_amdgcn_readfirstlane(LD32(sb + L_M + M_TAG));
  f32x16 qa16;
  {
    const unsigned A0 = sb + L_WQ + (256 * kh + 8 * h + (l15 >> 2)) * 128 + (((4 * (qq & 1) + 2 * ((g4 & 1) ^ kh) + ((lane & 3) >> 1)) ^ ((l15 >> 3) << 2)) << 4) + (lane & 1) * 8;
    const unsigned X0 = sb + L_X + ((qq & 2) + (g4 & 1)) * 16384 + kh * 8192 + (l15 >> 3) * 4096 + (l15 & 7) * 128 + ((h ^ (l15 >> 1)) << 4);
#define Q_LD(s, X) "ds_read_b128 " KR(s, 0) ", " X " offset:1024*(" #s "/4)\n\t" \
      "ds_read_b64_tr_b16 v[36+8*(" #s "%%6)+4:36+8*(" #s "%%6)+5], %1 offset:2048*" #s "\n\tds_read_b64_tr_b16 v[36+8*(" #s "%%6)+6:36+8*(" #s "%%6)+7], %1 offset:2048*" #s "+512\n\t"
#define Q_MM(N, s, C) "s_waitcnt lgkmcnt(" #N ")\n\tv_mfma_f32_32x32x16_bf16 %0, " KR(s, 4) ", " KR(s, 0) ", " C "\n\t"
    asm volatile(Q_LD(0, "%2") Q_LD(1, "%3") Q_LD(2, "%4") Q_LD(3, "%5") Q_LD(4, "%2") Q_MM(12, 0, "0") Q_LD(5, "%3") Q_MM(12, 1, "%0") Q_LD(6, "%4") Q_MM(12, 2, "%0")
                 Q_LD(7, "%5") Q_MM(12, 3, "%0") Q_LD(8, "%2") Q_MM(12, 4, "%0") Q_LD(9, "%3") Q_MM(12, 5, "%0") Q_LD(10, "%4") Q_MM(12, 6, "%0") Q_LD(11, "%5") Q_MM(12, 7, "%0")
                 Q_LD(12, "%2") Q_MM(12, 8, "%0") Q_LD(13, "%3") Q_MM(12, 9, "%0") Q_LD(14, "%4") Q_MM(12, 10, "%0") Q_LD(15, "%5") Q_MM(12, 11, "%0")
                 Q_MM(9, 12, "%0") Q_MM(6, 13, "%0") Q_MM(3, 14, "%0") Q_MM(0, 15, "%0") "s_nop 11"
                 : "=&v"(qa16) : "v"(A0), "v"(X0), "v"(X0 ^ 32), "v"(X0 ^ 64), "v"(X0 ^ 96) : "memory", CLOB_V36_83);
  }
  STAMP(5);
  const unsigned xa = opaque(sb + L_R2 + lane * 16 + w * 2048);
  LDF4(xa) = __builtin_shufflevector(qa16, qa16, 8, 9, 10, 11);
  LDF4(xa + 1024) = __builtin_shufflevector(qa16, qa16, 12, 13, 14, 15);
  arrive8(sb + L_M + M_AB + 12);
  wait8(sb + L_M + M_AB + 12);
  STAMP(6);
  const i32x4 rsKV = make_srd(ws + W_KV, KV_BYTES);
  const unsigned ko = lane * 16, vo = l31 <= 16 ? 1024 + h * 320 + l31 * 16 : 0x80000000u;
  const unsigned t0 = (16 * qq + 2 * bi) & 63;
  const bool ready = __builtin_amdgcn_readfirstlane(LD32(sb + L_M + M_RDY));
#define KV_FIRST() { unsigned tl_ = t0, sa_, sb_; asm volatile(A_FIRST : [tl] "+s"(tl_), [sa] "=&s"(sa_), [sb] "=&s"(sb_) \
    : [ko] "v"(ko), [vo] "v"(vo), [rk] "s"(rsKV) : "memory", "scc", CLOB_A4_47); }
  if (ready) KV_FIRST();
  {
    const f32x4 q0 = __builtin_shufflevector(qa16, qa16, 0, 1, 2, 3) + LDF4(xa ^ 8192), q1 = __builtin_shufflevector(qa16, qa16, 4, 5, 6, 7) + LDF4((xa ^ 8192) + 1024);
    const u32x4 o = {pkbf(q0[0], q0[1]), pkbf(q0[2], q0[3]), pkbf(q1[0], q1[1]), pkbf(q1[2], q1[3])};
    LD128(sb + L_Q + (2 * qq + kh) * 1024 + lane * 16) = o;
    const unsigned n2 = row_umax(__float_as_uint(sum32(sq2(o[0], sq2(o[1], sq2(o[2], sq2(o[3], 0.f)))))));
    asm volatile("s_mov_b64 exec, 0x10001\n\tds_max_u32 %0, %1\n\ts_mov_b64 exec, -1" :: "v"(sb + L_M + M_QMAX), "v"(n2) : "memory");
  }
  STAMP(7);
  arrive8(sb + L_M + M_AB);
  wait8(sb + L_M + M_AB, 9u);
  STAMP(8);
  if (!ready) KV_FIRST();

  const u32x2 qk2 = LD64(sb + L_M + M_QMAX);
  const float bound = fmaf(__builtin_amdgcn_sqrtf(__uint_as_float(qk2[0]) * __uint_as_float(qk2[1])), 1.002f, 1e-3f);
  const unsigned exact = __builtin_amdgcn_readfirstlane(opaque(bound <= 32.0f ? 0u : 1u));
  {
    unsigned tl = t0, n = 8, sa, sbo, sw = kh * PSW;
    const unsigned qb = sb + L_Q + kh * 4096, ob = sb + L_OA + w * 4 * OA_SLOT;
    if (exact) {
      const unsigned maddr = sb + L_MX + kh * 512 + l31 * 4;
      if (w < 4) LDF(sb + L_MX + (w * 64 + lane) * 4) = -3.0e38f;
      arrive8(sb + L_M + M_AB + 4);
      wait8(sb + L_M + M_AB + 4);
      float m[4] = {-3.0e38f, -3.0e38f, -3.0e38f, -3.0e38f};
      const __amdgpu_buffer_rsrc_t rs = __builtin_amdgcn_make_buffer_rsrc(ws + W_KV, 0, KV_BYTES, 0x00020000);
#pragma unroll 1
      for (unsigned i = 0; i < 16; ++i) {
        u32x4 kf = __builtin_amdgcn_raw_buffer_load_b128(rs, ko, ((tl + i) & 63) * KV_TILE, 16);
#pragma unroll
        for (int t = 0; t < 4; ++t) {
          const u32x4 q = LD128(qb + ko + t * 1024);
          f32x16 s_;
          asm volatile("s_nop 1\n\tv_mfma_f32_32x32x16_bf16 %0, %1, %2, 0\n\ts_nop 15" : "=&v"(s_) : "v"(kf), "v"(q) : CLOB_A4_47);
#pragma unroll
          for (int r = 0; r < 16; ++r) m[t] = fmaxf(m[t], s_[r]);
        }
      }
#pragma unroll
      for (int t = 0; t < 4; ++t) __builtin_amdgcn_ds_fmaxf((LAS float*)(size_t)(maddr + t * 128), m[t], __ATOMIC_RELAXED, __HIP_MEMORY_SCOPE_WORKGROUP, false);
      arrive8(sb + L_M + M_AB + 8);
      wait8(sb + L_M + M_AB + 8);
      asm volatile("v_accvgpr_write_b32 a44, %0\n\tv_accvgpr_write_b32 a45, %1\n\tv_accvgpr_write_b32 a46, %2\n\tv_accvgpr_write_b32 a47, %3"
                   :: "v"(-LDF(maddr)), "v"(-LDF(maddr + 128)), "v"(-LDF(maddr + 256)), "v"(-LDF(maddr + 384)) : CLOB_A4_47);
    }
    asm volatile("s_cmp_lg_u32 %[ex], 0\n\ts_cbranch_scc1 90f\n\t" A_ATT(A_SHIFT_NONE) "s_branch 91f\n\t90:\n\t" A_ATT(A_SHIFT_EXACT) "91:"
                 : [tl] "+s"(tl), [n] "+s"(n), [sa] "=&s"(sa), [sb] "=&s"(sbo), [sw] "+s"(sw)
                 : [ko] "v"(ko), [vo] "v"(vo), [nb] "v"(-bound), [rk] "s"(rsKV), [qb] "s"(qb), [ob] "s"(ob), [pf] "s"(sb + L_M + M_SW + 64), [ex] "s"(exact)
                 : "memory", "scc", "vcc", CLOB_V4_83, CLOB_A4_47);
  }
  STAMP(9);
  RSTAMP(17);
  BAR();
  STAMP(10);

  {
    LANE_VARS();
    {
      const unsigned s0 = opaque(sb + L_OA + (16 * kh + qq) * OA_SLOT + lane * 16), s1 = opaque(sb + L_OA + (16 * kh + qq) * OA_SLOT + 2048 + lane * 4);
      constexpr unsigned W1 = 4 * OA_SLOT;
      f32x4 o0 = (LDF4(s0) + LDF4(s0 + W1)) + (LDF4(s0 + 2 * W1) + LDF4(s0 + 3 * W1));
      f32x4 o1 = (LDF4(s0 + 1024) + LDF4(s0 + W1 + 1024)) + (LDF4(s0 + 2 * W1 + 1024) + LDF4(s0 + 3 * W1 + 1024));
      const float inv = __builtin_amdgcn_rcpf(sum32((LDF(s1) + LDF(s1 + W1)) + (LDF(s1 + 2 * W1) + LDF(s1 + 3 * W1))));
      o0 *= inv; o1 *= inv;
      u32x4 pk = {pkbf(o0[0], o0[1]), pkbf(o0[2], o0[3]), pkbf(o1[0], o1[1]), pkbf(o1[2], o1[3])};
      const __amdgpu_buffer_rsrc_t rsO = __builtin_amdgcn_make_buffer_rsrc(ws + W_OX, 0, 2097152, 0x00020000);
      __builtin_amdgcn_raw_buffer_store_b128(pk, rsO, lane * 16 + ((2 * grp + kh) * 32 + 4 * hg + qq) * 1024, 0, 16);
    }
    STAMP(11);
    RSTAMP(13);
    const unsigned r0 = 256 * kh + 16 * (g4 >> 1) + 4 * (g4 & 1) + (l15 >> 2);
    const unsigned A0 = sb + L_WO + r0 * 128 + (((4 * kh + ((lane & 3) >> 1)) ^ wswz(r0)) << 4) + (lane & 1) * 8;
    const unsigned oo = (g4 >> 1) * 1024 + ((g4 & 1) * 32 + 16 * (qq & 1) + l15) * 16 + ((2 * grp + (qq >> 1)) * 32 + 16 * kh) * 1024;
    const unsigned xa = sb + L_U + lane * 16;
    const unsigned go = (64 * grp + 16 * qq + l15) * 2048 + 1024 + 128 * hg + 64 * kh + 32 * (g4 & 1) + 16 * (g4 >> 1);
    const unsigned char* f2 = ws + W_F2 + grp * 128;
    unsigned t0, t1;
#define T_WF1(F, B, s, e, P, Q) "ds_read_b64_tr_b16 " F "[" #B "+4*" #e ":" #B "+4*" #e "+1], " P " offset:4096*" #s "\n\t" \
                                "ds_read_b64_tr_b16 " F "[" #B "+4*" #e "+2:" #B "+4*" #e "+3], " Q " offset:4096*" #s "+1024\n\t"
#define T_WF(F, B, s) T_WF1(F, B, s, 0, "%[a0]", "v6") T_WF1(F, B, s, 1, "v5", "v7") T_WF1(F, B, s, 2, "v6", "%[a0]") T_WF1(F, B, s, 3, "v7", "v5")
#define T_M1(F, B, s, e, C) "v_mfma_f32_16x16x32_bf16 v[104+4*" #e ":107+4*" #e "], " F "[" #B "+4*" #e ":" #B "+4*" #e "+3], v[72+4*" #s ":75+4*" #s "], " C "\n\t"
#define T_MA(e) "v[104+4*" #e ":107+4*" #e "]"
#define T_MM(F, B, s, N) "s_waitcnt vmcnt(" #N ")\n\t" T_M1(F, B, s, 0, T_MA(0)) T_M1(F, B, s, 1, T_MA(1)) T_M1(F, B, s, 2, T_MA(2)) T_M1(F, B, s, 3, T_MA(3))
#define T_OL(s, so, imm) "buffer_load_dwordx4 v[72+4*" #s ":75+4*" #s "], %[oo], %[ro], " so " offen offset:" #imm " sc1\n\t"
    asm volatile(
        "v_xor_b32 v5, 32, %[a0]\n\tv_xor_b32 v6, 64, %[a0]\n\tv_xor_b32 v7, 0x60, %[a0]\n\t"
        T_WF("v", 8, 0) T_WF("v", 24, 1) T_WF("v", 40, 2)
        "s_waitcnt vmcnt(0)\n\ts_barrier\n\t"
        TS(80) "s_cmp_lg_u32 %[w], 0\n\ts_cbranch_scc1 1f\n\t"
        "v_mov_b32 v72, %[fo]\n\tv_mov_b32 v73, %[tg]\n\ts_mov_b64 exec, 1\n\tglobal_store_dword v72, v73, %[f2] sc1\n\ts_mov_b64 exec, -1\n\t"
        "1:\n\t"
        "v_and_b32 v4, 48, %[xa]\n\tv_lshrrev_b32 v4, 2, v4\n\tv_or_b32 v4, %[k4], v4\n\ts_mov_b32 %[t0], 0x100000\n\t"
        T_WF("v", 56, 3) "global_load_dword v104, v4, %[f2] sc1\n\t" T_WF("a", 8, 4) T_WF("a", 24, 5)
        "2:\n\tglobal_load_dword v105, v4, %[f2] sc1\n\ts_waitcnt vmcnt(1)\n\tv_cmp_ne_u32 vcc, %[tg], v104\n\ts_cbranch_vccz 3f\n\t"
        "global_load_dword v104, v4, %[f2] sc1\n\ts_waitcnt vmcnt(1)\n\tv_cmp_ne_u32 vcc, %[tg], v105\n\ts_cbranch_vccz 3f\n\t"
        "s_sub_u32 %[t0], %[t0], 1\n\ts_cbranch_scc0 2b\n\t"
        "3:\n\t" TS(82) "s_movk_i32 %[t0], 0x1000\n\ts_movk_i32 %[t1], 0x2000\n\t"
        T_OL(0, "0", 0) T_OL(1, "0", 2048) T_OL(2, "%[t0]", 0) T_OL(3, "%[t0]", 2048) "s_movk_i32 %[t0], 0x3000\n\t"
        T_OL(4, "%[t1]", 0) T_OL(5, "%[t1]", 2048) T_OL(6, "%[t0]", 0) T_OL(7, "%[t0]", 2048)
        "s_lshl_b32 %[t0], %[w], 11\n\ts_xor_b32 %[t1], %[t0], 0x2000\n\t"
        "s_waitcnt lgkmcnt(0) vmcnt(7)\n\t" T_M1("v", 8, 0, 0, "0") T_M1("v", 8, 0, 1, "0") T_M1("v", 8, 0, 2, "0") T_M1("v", 8, 0, 3, "0")
        T_WF("v", 8, 6) T_MM("v", 24, 1, 6) T_WF("v", 24, 7) T_MM("v", 40, 2, 5) T_MM("v", 56, 3, 4) T_MM("a", 8, 4, 3) T_MM("a", 24, 5, 2)
        "s_waitcnt lgkmcnt(0)\n\t" T_MM("v", 8, 6, 1) T_MM("v", 24, 7, 0)
        TS(84) "v_add_u32 v4, %[t0], %[xa]\n\tv_add_u32 v5, %[t1], %[xa]\n\ts_nop 6\n\t"
        "ds_write_b128 v4, v[112:115]\n\tds_write_b128 v4, v[116:119] offset:1024\n\ts_waitcnt lgkmcnt(0)\n\ts_barrier\n\t"
        "ds_read_b128 v[72:75], v5\n\tds_read_b128 v[76:79], v5 offset:1024\n\ts_waitcnt lgkmcnt(1)\n\t"
        "v_add_f32 v72, v72, v104\n\tv_add_f32 v73, v73, v105\n\tv_add_f32 v74, v74, v106\n\tv_add_f32 v75, v75, v107\n\ts_waitcnt lgkmcnt(0)\n\t"
        "v_add_f32 v76, v76, v108\n\tv_add_f32 v77, v77, v109\n\tv_add_f32 v78, v78, v110\n\tv_add_f32 v79, v79, v111\n\t"
        "v_cvt_pk_bf16_f32 v80, v72, v73\n\tv_cvt_pk_bf16_f32 v81, v74, v75\n\tv_cvt_pk_bf16_f32 v82, v76, v77\n\tv_cvt_pk_bf16_f32 v83, v78, v79\n\ts_nop 1\n\t"
        "v_permlane16_swap_b32 v80, v82\n\tv_permlane16_swap_b32 v81, v83\n\ts_nop 1\n\t"
        "global_store_dwordx4 %[go], v[80:83], %[out]\n\ts_nop 1"
        : [t0] "=&s"(t0), [t1] "=&s"(t1)
        : [a0] "v"(A0), [oo] "v"(oo), [xa] "v"(xa), [go] "v"(go), [w] "s"(w), [fo] "s"(hg * 4), [tg] "s"(tag), [f2] "s"(f2), [k4] "s"(16 * kh),
          [ro] "s"(make_srd(ws + W_OX, 2097152)), [out] "s"(out)
        : "memory", "scc", "vcc", CLOB_V4_83, CLOB_A4_47 TSCLOB);
    TSTAMP_OUT();
    STAMP(12);
    STAMP_OUT();
  }
}

extern "C" void kernel_launch(void* const* d_in, const int* in_sizes, int n_in, void* d_out, int out_size, void* d_ws,
                              size_t ws_size, hipStream_t stream) {
  typedef const unsigned short* P;
  fused_kernel<<<256, 768, 0, stream>>>((P)d_in[0], (P)d_in[1], (P)d_in[2], (P)d_in[3], (P)d_in[4], (P)d_in[5], (P)d_in[6],
                                        (const unsigned*)d_in[7], (const unsigned*)d_in[8], (P)d_in[9], (P)d_in[10], (P)d_in[11],
                                        (P)d_in[12], (P)d_in[13], (unsigned short*)d_out, (unsigned char*)d_ws);
}
```

```cpp
#define YAUX 17
#define OAUX "sc0 sc1"
#include <hip/hip_runtime.h>
#include <hip/hip_bf16.h>
#include <stdint.h>

typedef float f32x16 __attribute__((ext_vector_type(16)));
typedef float f32x8 __attribute__((ext_vector_type(8)));
typedef float f32x4 __attribute__((ext_vector_type(4)));
typedef float f32x2 __attribute__((ext_vector_type(2)));
typedef __bf16 bf16x8 __attribute__((ext_vector_type(8)));
typedef __bf16 bf16x2 __attribute__((ext_vector_type(2)));
typedef _Float16 f16x8 __attribute__((ext_vector_type(8)));
typedef _Float16 f16x2 __attribute__((ext_vector_type(2)));
typedef short s16x4 __attribute__((ext_vector_type(4)));
typedef unsigned u32x4 __attribute__((ext_vector_type(4)));
typedef unsigned u32x2 __attribute__((ext_vector_type(2)));
#define LAS __attribute__((address_space(3)))
#define GAS __attribute__((address_space(1)))
typedef GAS unsigned gu32;
typedef GAS unsigned long long gu64;
#define RLX_AGENT __ATOMIC_RELAXED, __HIP_MEMORY_SCOPE_AGENT

constexpr unsigned L_X = 0;
constexpr unsigned L_TLO = 0, TLO_CH = 16896, L_THI = 33792, THI_CH = 11088;
constexpr unsigned L_WO = 0;
constexpr unsigned L_B = 65536;
constexpr unsigned L_WQ = L_B;
constexpr unsigned L_R2 = L_B + 65536;
constexpr unsigned L_TA0 = L_B + 81920, L_TB0 = L_TA0 + 8192;
constexpr unsigned L_KC = L_B, KC_CH = 16640;
constexpr unsigned L_OA = L_B, OA_SLOT = 2304;
constexpr unsigned L_P = L_B + 40960;
constexpr unsigned L_U = L_B + 73728, U_CH = 7600;
constexpr unsigned L_Q = L_B + 88928;
constexpr unsigned L_MX = L_Q + 8192;
constexpr unsigned L_M = L_MX + 1024;
constexpr unsigned LDS_BYTES = L_M + 128;
static_assert(LDS_BYTES <= 163840, "lds");
constexpr unsigned M_TAG = 0, M_RDY = 4, M_QMAX = 8, M_KMAX = 12, M_SCALE = 16  , M_SW = 32  , M_HA = 96  , M_AB = 112  ;
#define CONV_PRIO 3
#define CONV_N 16
#define CNOP 7
#define CPAD "s_nop " STR(CNOP) "\n\t"
#define PSW 2
#define NREP 8
#define WSLEEP 6
#define FSLEEP 6
#define YPRIO 3
#define TSLEEP 8
#define U_MODE 1
#define F3POS 2
#define SSM_PRIO 3

constexpr unsigned KV_TILE = 2304, KV_BYTES = 64 * KV_TILE;
static_assert(NREP <= 8, "flag copies");
#define TROW_ 4224
constexpr unsigned TROW = TROW_;
constexpr unsigned W_E = 0, W_F1 = 8192  , W_F2 = W_F1 + 16384  , W_F3 = W_F2 + 32768  ,
                   W_F4 = W_F3 + 8192, W_KV = W_F1 + 65536, W_OX = W_KV + KV_BYTES,
                   W_UT = W_OX + 2097152, W_YT = W_UT + 512 * TROW, W_END = W_YT + 512 * TROW;
constexpr unsigned W_DBG = W_END;

constexpr int CODE_LINES = 150;
constexpr unsigned kSpinCap = 1u << 20;

#define LD32(a) (*(LAS unsigned*)(size_t)(a))
#define LDF(a) (*(LAS float*)(size_t)(a))
#define LD64(a) (*(LAS u32x2*)(size_t)(a))
#define LD128(a) (*(LAS u32x4*)(size_t)(a))
#define LDF4(a) (*(LAS f32x4*)(size_t)(a))
#define BAR() asm volatile("s_waitcnt lgkmcnt(0)\n\ts_barrier" ::: "memory")

#define RSTAMP(i)
#define TS(k)
#define TSTAMP_OUT()
#define TSCLOB
#define STAMP(i)
#define STAMP_INIT()
#define STAMP_OUT()

__device__ __forceinline__ float bflo(unsigned v) { return __uint_as_float(v << 16); }
__device__ __forceinline__ float bfhi(unsigned v) { return __uint_as_float(v & 0xffff0000u); }
__device__ __forceinline__ unsigned pkbf(float a, float b) { f32x2 x = {a, b}; return __builtin_bit_cast(unsigned, __builtin_convertvector(x, bf16x2)); }
__device__ __forceinline__ unsigned pkh(float a, float b) { f32x2 x = {a, b}; return __builtin_bit_cast(unsigned, __builtin_convertvector(x, f16x2)); }
template <int CTRL> __device__ __forceinline__ unsigned dppu(unsigned v) { return __builtin_amdgcn_update_dpp(0u, v, CTRL, 0xf, 0xf, true); }
__device__ __forceinline__ unsigned umax(unsigned a, unsigned b) { return a > b ? a : b; }
__device__ __forceinline__ unsigned row_umax(unsigned v) {
  v = umax(v, dppu<0x128>(v)); v = umax(v, dppu<0x124>(v)); v = umax(v, dppu<0x122>(v)); return umax(v, dppu<0x121>(v));
}
__device__ __forceinline__ unsigned wave_umax(unsigned v) {
  v = row_umax(v);
  auto a = __builtin_amdgcn_permlane16_swap(v, v, false, false);
  v = umax(a[0], a[1]);
  auto c = __builtin_amdgcn_permlane32_swap(v, v, false, false);
  return umax(c[0], c[1]);
}
__device__ __forceinline__ float sum16(float a, float b) {
  auto r = __builtin_amdgcn_permlane16_swap(__float_as_uint(a), __float_as_uint(b), false, false);
  return __uint_as_float(r[0]) + __uint_as_float(r[1]);
}
__device__ __forceinline__ float sum32(float v) {
  auto c = __builtin_amdgcn_permlane32_swap(__float_as_uint(v), __float_as_uint(v), false, false);
  return __uint_as_float(c[0]) + __uint_as_float(c[1]);
}
__device__ __forceinline__ float sq2(unsigned v, float c) {
  return __builtin_amdgcn_fdot2_f32_bf16(__builtin_bit_cast(bf16x2, v), __builtin_bit_cast(bf16x2, v), c, false);
}
__device__ __forceinline__ unsigned opaque(unsigned v) { asm volatile("" : "+v"(v)); return v; }
__device__ __forceinline__ int lane_id() {
  int l;
  asm volatile("v_mbcnt_lo_u32_b32 %0, -1, 0\n\tv_mbcnt_hi_u32_b32 %0, -1, %0" : "=v"(l));
  __builtin_assume(l >= 0 && l < 64);
  return l;
}
struct cf { float x, y; };
__device__ __forceinline__ cf cmul(cf a, cf b) { return {a.x * b.x - a.y * b.y, a.x * b.y + a.y * b.x}; }
__device__ __forceinline__ cf cexp_(float re, float im) {
  float m = __builtin_amdgcn_exp2f(re * 1.44269504088896f);
  float f = __builtin_amdgcn_fractf(im * 0.159154943091895f);
  return {m * __builtin_amdgcn_cosf(f), m * __builtin_amdgcn_sinf(f)};
}
#define GLD1(dst, voff, base) asm volatile("global_load_dword %0, %1, %2" : "=v"(dst) : "v"(voff), "s"(base))
__device__ __forceinline__ void gdma4(unsigned voff, const void* base, unsigned lds_dst) {
  unsigned keep;
  asm volatile("s_mov_b32 %0, m0\n\ts_mov_b32 m0, %3\n\ts_nop 0\n\tglobal_load_lds_dwordx4 %1, %2\n\tglobal_load_lds_dwordx4 %1, %2 offset:1024\n\t"
               "global_load_lds_dwordx4 %1, %2 offset:2048\n\tglobal_load_lds_dwordx4 %1, %2 offset:3072\n\ts_mov_b32 m0, %0"
               : "=&s"(keep) : "v"(voff), "s"(base), "s"(lds_dst) : "memory");
}
#define DMA_PAIR "global_load_lds_dwordx4 %2, %1\n\ts_add_u32 m0, m0, 0x400\n\ts_nop 0\n\tglobal_load_lds_dwordx4 %3, %1\n\ts_add_u32 m0, m0, 0x400\n\tv_add_u32 %2, 0x4000, %2\n\tv_add_u32 %3, 0x4000, %3\n\t"
__device__ __forceinline__ void gdma_rows16(unsigned v0, unsigned v1, const void* base, unsigned lds_dst) {
  unsigned keep;
  asm volatile("s_mov_b32 %0, m0\n\ts_mov_b32 m0, %4\n\ts_nop 0\n\t" DMA_PAIR DMA_PAIR DMA_PAIR DMA_PAIR DMA_PAIR DMA_PAIR DMA_PAIR DMA_PAIR "s_mov_b32 m0, %0"
               : "=&s"(keep), "+s"(base), "+v"(v0), "+v"(v1) : "s"(lds_dst) : "memory", "scc");
}
#define DMA_X(v, o) "global_load_lds_dwordx4 " v ", %1 offset:" #o "\n\ts_add_u32 m0, m0, "
__device__ __forceinline__ void gdma_x(unsigned v0, unsigned v1, const void* base, unsigned lds_dst) {
  unsigned keep;
  asm volatile("s_mov_b32 %0, m0\n\ts_mov_b32 m0, %4\n\ts_nop 0\n\t"
               DMA_X("%2", 0) "896\n\ts_nop 0\n\t" DMA_X("%2", 128) "896\n\ts_nop 0\n\t" DMA_X("%2", 256) "896\n\ts_nop 0\n\t" DMA_X("%2", 384) "1408\n\ts_nop 0\n\t"
               DMA_X("%3", 0) "896\n\ts_nop 0\n\t" DMA_X("%3", 128) "896\n\ts_nop 0\n\t" DMA_X("%3", 256) "896\n\ts_nop 0\n\t" DMA_X("%3", 384) "0\n\t"
               "s_mov_b32 m0, %0"
               : "=&s"(keep) : "s"(base), "v"(v0), "v"(v1), "s"(lds_dst) : "memory", "scc");
}
__device__ __forceinline__ void gdma_rows8(unsigned v0, unsigned v1, const void* base, unsigned lds_dst) {
  unsigned keep;
  asm volatile("s_mov_b32 %0, m0\n\ts_mov_b32 m0, %4\n\ts_nop 0\n\t" DMA_PAIR DMA_PAIR DMA_PAIR DMA_PAIR "s_mov_b32 m0, %0"
               : "=&s"(keep), "+s"(base), "+v"(v0), "+v"(v1) : "s"(lds_dst) : "memory", "scc");
}
__device__ __forceinline__ void gdma2(unsigned v0, unsigned v1, const void* base, unsigned lds_dst) {
  unsigned keep;
  asm volatile("s_mov_b32 %0, m0\n\ts_mov_b32 m0, %4\n\ts_nop 0\n\tglobal_load_lds_dwordx4 %2, %1\n\ts_add_u32 m0, m0, 0x400\n\ts_nop 0\n\t"
               "global_load_lds_dwordx4 %3, %1\n\ts_mov_b32 m0, %0" : "=&s"(keep) : "s"(base), "v"(v0), "v"(v1), "s"(lds_dst) : "memory", "scc");
}
__device__ __forceinline__ unsigned wswz(unsigned row) { return ((((row >> 1) & 3u) ^ (((row >> 3) & 1u) << 1)) << 1); }

__device__ __forceinline__ void arrive(unsigned addr) {
  asm volatile("s_mov_b64 exec, 1\n\tds_add_u32 %0, %1\n\ts_mov_b64 exec, -1" :: "v"(addr), "v"(1u) : "memory");
}
template <int SLEEP>
__device__ __forceinline__ void await(unsigned addr, unsigned count) {
  _Pragma("unroll 1") for (unsigned spins = 0; spins < kSpinCap; ++spins) {
    unsigned v;
    asm volatile("ds_read_b32 %0, %1\n\ts_waitcnt lgkmcnt(0)" : "=v"(v) : "v"(addr) : "memory");
    if (__builtin_amdgcn_readfirstlane(v) >= count) break;
    if (SLEEP) __builtin_amdgcn_s_sleep(SLEEP);
  }
}
__device__ __forceinline__ void sync4(unsigned sb, int k) {
  arrive(sb + L_M + M_SW + k * 4);
  await<2>(sb + L_M + M_SW + k * 4, 4u);
}
template <int SLEEP>
__device__ __forceinline__ void wait_flags32(const unsigned char* base, unsigned tag, int lane) {
  _Pragma("unroll 1") for (unsigned spins = 0; spins < kSpinCap; ++spins) {
    const unsigned f = __hip_atomic_load((gu32*)base + (lane & 31) * 8, RLX_AGENT);
    if (__all(f == tag)) break;
    __builtin_amdgcn_s_sleep(SLEEP);
  }
  __builtin_amdgcn_fence(__ATOMIC_ACQUIRE, "wavefront");
}
__device__ __forceinline__ void transpose_out(unsigned img, unsigned scr, int j, int lane, u32x4& o0, u32x4& o1) {
  const unsigned g = lane >> 4, i = lane & 15, row = 8 * g + (i >> 2);
  const unsigned ra = img + row * 128 + (((2 * j + ((lane & 3) >> 1)) ^ wswz(row)) << 4) + (lane & 1) * 8;
  const unsigned wa = scr + (i & 7) * 128 + ((g ^ (i & 7)) << 4), rb = scr + (lane >> 3) * 128 + (((lane & 7) ^ (lane >> 3)) << 4);
  u32x2 t0, t1, t2, t3;
  asm volatile("ds_read_b64_tr_b16 %0, %4\n\tds_read_b64_tr_b16 %1, %5 offset:512\n\tds_read_b64_tr_b16 %2, %4 offset:4096\n\t"
               "ds_read_b64_tr_b16 %3, %5 offset:4608\n\ts_waitcnt lgkmcnt(0)"
               : "=&v"(t0), "=&v"(t1), "=&v"(t2), "=&v"(t3) : "v"(ra), "v"(ra ^ 64) : "memory");
  const u32x4 c0 = {t0[0], t0[1], t1[0], t1[1]}, c1 = {t2[0], t2[1], t3[0], t3[1]};
  asm volatile("s_mov_b64 exec, %6\n\tds_write_b128 %2, %4\n\tds_write_b128 %3, %5\n\ts_mov_b64 exec, -1\n\tds_read_b128 %0, %7\n\t"
               "s_not_b64 exec, %6\n\tds_write_b128 %2, %4\n\tds_write_b128 %3, %5\n\ts_mov_b64 exec, -1\n\tds_read_b128 %1, %7\n\ts_waitcnt lgkmcnt(0)"
               : "=&v"(o0), "=&v"(o1) : "v"(wa), "v"(wa ^ 64), "v"(c0), "v"(c1), "s"(0x00ff00ff00ff00ffull), "v"(rb) : "memory", "scc");
}

#define STR_(x) #x
#define STR(x) STR_(x)
#define VR(b, o) "v[" STR(b) "+" STR(o) "]"
#define VRR(b, o, n) "v[" STR(b) "+" STR(o) ":" STR(b) "+" STR(o) "+" STR(n) "-1]"
#define AR4(i) "a[" STR(i) ":" STR(i) "+3]"
#define C4(p, a, b, c, d) p #a, p #b, p #c, p #d
#define C8(p, a, b, c, d, e, f, g, h) p #a, p #b, p #c, p #d, p #e, p #f, p #g, p #h
#define CLOB_V36_83 C4("v", 36, 37, 38, 39), C8("v", 40, 41, 42, 43, 44, 45, 46, 47), C8("v", 48, 49, 50, 51, 52, 53, 54, 55), C8("v", 56, 57, 58, 59, 60, 61, 62, 63), \
  C8("v", 64, 65, 66, 67, 68, 69, 70, 71), C8("v", 72, 73, 74, 75, 76, 77, 78, 79), C4("v", 80, 81, 82, 83)
#define CLOB_V4_83 C4("v", 4, 5, 6, 7), C8("v", 8, 9, 10, 11, 12, 13, 14, 15), C8("v", 16, 17, 18, 19, 20, 21, 22, 23), C8("v", 24, 25, 26, 27, 28, 29, 30, 31), \
  C4("v", 32, 33, 34, 35), CLOB_V36_83
#define CLOB_A4_47 C4("a", 4, 5, 6, 7), C8("a", 8, 9, 10, 11, 12, 13, 14, 15), C8("a", 16, 17, 18, 19, 20, 21, 22, 23), C8("a", 24, 25, 26, 27, 28, 29, 30, 31), \
  C8("a", 32, 33, 34, 35, 36, 37, 38, 39), C8("a", 40, 41, 42, 43, 44, 45, 46, 47)
typedef int i32x4 __attribute__((ext_vector_type(4)));
__device__ __forceinline__ i32x4 make_srd(const void* p, unsigned bytes) {
  const unsigned long long a = (unsigned long long)p;
  return (i32x4){(int)(unsigned)a, (int)((unsigned)(a >> 32) & 0xffffu), (int)bytes, 0x00020000};
}

#define BUF0(o) "a[4+" #o ":7+" #o "]"
#define BUF1(o) "a[16+" #o ":19+" #o "]"
#define BUF2(o) "v[100+" #o ":103+" #o "]"
#define A_EX(S, i) "v_exp_f32 " VR(S, i) ", " VR(S, i) "\n\t"
#define A_QK(S, B, q, C) "v_mfma_f32_32x32x16_bf16 " VRR(S, 0, 16) ", " B(0) ", " AR4(28 + 4 * q) ", " C "\n\t"
#define A_PV0(o, B, S) "v_smfmac_f32_32x32x32_bf16 " VRR(4, 16 * o, 16) ", " B(4) ", " VRR(S, 0, 8) ", v116\n\t"
#define A_PV1(o, B, S) "v_smfmac_f32_32x32x32_bf16 " VRR(4, 16 * o, 16) ", " B(8) ", " VRR(S, 8, 8) ", v116\n\t"
#define A_RD4(o, q) "v_accvgpr_read_b32 " VR(100, o) ", a[44+" #q "]\n\tv_accvgpr_read_b32 " VR(101, o) ", a[44+" #q "]\n\t" \
                    "v_accvgpr_read_b32 " VR(102, o) ", a[44+" #q "]\n\tv_accvgpr_read_b32 " VR(103, o) ", a[44+" #q "]\n\t"
#define A_SHIFT_EXACT(q) A_RD4(0, q) A_RD4(4, q) A_RD4(8, q) A_RD4(12, q) "s_nop 1\n\t"
#define A_SHIFT_NONE(q) ""
#define A_BLOCK(Sc, Sp, op, Bp, Bn, qn, oc, Bc, SH, C, XM, X3, X5, X7, X10, X12) \
  A_EX(Sc, 0) A_EX(Sc, 1) A_PV1(op, Bp, Sp) XM A_EX(Sc, 2) A_EX(Sc, 3) X3 A_EX(Sc, 4) A_EX(Sc, 5) X5 SH(qn) A_QK(Sp, Bn, qn, C) \
  A_EX(Sc, 6) A_EX(Sc, 7) X7 A_EX(Sc, 8) A_EX(Sc, 9) A_EX(Sc, 10) X10 A_PV0(oc, Bc, Sc) \
  A_EX(Sc, 11) A_EX(Sc, 12) X12 A_EX(Sc, 13) A_EX(Sc, 14) A_EX(Sc, 15)
#define A_LK(B, so) "buffer_load_dwordx4 " B(0) ", %[ko], %[rk], " so " offen sc1\n\t"
#define A_LV0(B, so) "buffer_load_dwordx4 " B(4) ", %[vo], %[rk], " so " offen sc1\n\t"
#define A_LV1(B, so) "buffer_load_dwordx4 " B(8) ", %[vo], %[rk], " so " offen offset:640 sc1\n\t"
#define A_NEXT "s_add_u32 %[tl], %[tl], 1\n\ts_and_b32 %[tl], %[tl], 63\n\t"
#define A_OFF(r) "s_mul_i32 %[" #r "], %[tl], 2304\n\t"
#define A_STEP(B, Bp, Bn, SH, C, W, XM, E) \
  A_BLOCK(68, 84, 3, Bp, B, 1, 0, B, SH, C, XM, A_LV1(Bp, "%[sa]"), "", "", W, "") \
  A_BLOCK(84, 68, 0, B, B, 2, 1, B, SH, C, "", "s_mov_b32 %[sa], %[sb]\n\t", "", A_NEXT, "", "") \
  A_BLOCK(68, 84, 1, B, B, 3, 2, B, SH, C, "", "", "", A_LK(B, "%[sb]"), "", "") \
  A_BLOCK(84, 68, 2, B, Bn, 0, 3, B, SH, C, "", "", W, E, "", A_LV0(B, "%[sb]") A_OFF(sb))
#define A_ZERO(o) "v_mfma_f32_32x32x8_bf16 " VRR(4, 16 * o, 16) ", v[116:117], v[116:117], 0\n\t"
#define A_FLUSH(o) "ds_write_b128 v118, " VRR(4, 16 * o, 4) " offset:2304*" #o "\n\tds_write_b128 v118, " VRR(8, 16 * o, 4) " offset:2304*" #o "+1024\n\t" \
                   "ds_write_b32 v119, " VR(12, 16 * o) " offset:2304*" #o "+2048\n\t"
#define A_FIRST \
  A_OFF(sb) A_NEXT A_OFF(sa) A_LK(BUF0, "%[sb]") A_LV0(BUF0, "%[sb]") A_LV1(BUF0, "%[sb]") A_NEXT A_OFF(sb) \
  A_LK(BUF1, "%[sa]") A_LV0(BUF1, "%[sa]") A_LV1(BUF1, "%[sa]") A_LK(BUF2, "%[sb]") A_LV0(BUF2, "%[sb]")
#define A_SETUP \
  "v_add_u32 v118, %[qb], %[ko]\n\tv_mov_b32 v116, 0\n\tv_mov_b32 v117, 0\n\t" \
  "ds_read_b128 a[28:31], v118\n\tds_read_b128 a[32:35], v118 offset:1024\n\tds_read_b128 a[36:39], v118 offset:2048\n\tds_read_b128 a[40:43], v118 offset:3072\n\t" \
  A_ZERO(0) A_ZERO(1) A_ZERO(2) A_ZERO(3) \
  "v_lshrrev_b32 v119, 2, %[ko]\n\tv_add_u32 v118, %[ob], %[ko]\n\tv_add_u32 v119, %[ob], v119\n\t"
#define A_GO "v_mov_b32 v116, 0xdddd\n\ts_setprio 1\n\ts_cmp_eq_u32 %[sw], 0\n\ts_cbranch_scc1 14f\n\ts_setprio 2\n\t14:\n\ts_nop 13\n\t" \
  A_EX(68, 0) A_EX(68, 1) "s_branch 11f\n\t"
#define A_W6 "s_waitcnt vmcnt(6)\n\t"
#define A_W3 "s_waitcnt vmcnt(3)\n\t"
#define A_DEC "s_sub_u32 %[n], %[n], 1\n\t"
#define A_ATT \
  A_SETUP "s_cmp_lg_u32 %[ex], 0\n\ts_cbranch_scc1 90f\n\t" \
  A_NEXT A_NEXT A_OFF(sa) A_NEXT A_OFF(sb) \
  "s_waitcnt vmcnt(7) lgkmcnt(0)\n\t" A_QK(68, BUF0, 0, "0") A_GO \
  "10:\n\t" \
  A_STEP(BUF0, BUF2, BUF1, A_SHIFT_NONE, "0", A_W6, "11:\n\t", A_DEC) \
  "s_cmp_eq_u32 %[n], 0\n\ts_cbranch_scc1 16f\n\t" \
  A_STEP(BUF1, BUF0, BUF2, A_SHIFT_NONE, "0", A_W6, "", "") \
  A_STEP(BUF2, BUF1, BUF0, A_SHIFT_NONE, "0", A_W6, "", "s_cmp_lg_u32 %[n], %[sw]\n\t") "s_cbranch_scc1 10b\n\ts_setprio 0\n\ts_branch 10b\n\t" \
  "16:\n\t" A_PV1(3, BUF0, 84) "s_branch 91f\n\t" \
  "90:\n\ts_mov_b32 %[n], 8\n\t" A_NEXT A_OFF(sa) A_NEXT A_OFF(sb) \
  "s_waitcnt vmcnt(0) lgkmcnt(0)\n\t" A_SHIFT_EXACT(0) A_QK(68, BUF0, 0, "v[100:115]") A_GO \
  "10:\n\t" \
  A_STEP(BUF0, BUF1, BUF1, A_SHIFT_EXACT, "v[100:115]", A_W3, "11:\n\t", "") \
  A_STEP(BUF1, BUF0, BUF0, A_SHIFT_EXACT, "v[100:115]", A_W3, "", A_DEC) "s_cmp_lg_u32 %[n], 0\n\ts_cbranch_scc1 10b\n\t" \
  A_PV1(3, BUF1, 84) \
  "91:\n\ts_setprio 0\n\tv_mov_b32 v117, %[pf]\n\ts_mov_b32 %[n], 0x100000\n\t" \
  "12:\n\tds_read_b32 v112, v117\n\ts_waitcnt lgkmcnt(0)\n\t" \
  "s_sub_u32 %[n], %[n], 1\n\ts_cbranch_scc1 13f\n\tv_cmp_gt_u32 vcc, 4, v112\n\ts_cbranch_vccnz 12b\n\t13:\n\ts_nop 7\n\t" \
  A_FLUSH(0) A_FLUSH(1) A_FLUSH(2) A_FLUSH(3)

#pragma clang diagnostic ignored "-Winline-asm"
__global__ __launch_bounds__(768, 3) void fused_kernel(
    const unsigned short* __restrict__ u, const unsigned short* __restrict__ Are, const unsigned short* __restrict__ Aim,
    const unsigned short* __restrict__ Bre, const unsigned short* __restrict__ Bim, const unsigned short* __restrict__ Cre,
    const unsigned short* __restrict__ Cim, const unsigned* __restrict__ logdt2, const unsigned* __restrict__ dskip2,
    const unsigned short* __restrict__ x, const unsigned short* __restrict__ wq, const unsigned short* __restrict__ wk,
    const unsigned short* __restrict__ wv, const unsigned short* __restrict__ wo, unsigned short* __restrict__ out,
    unsigned char* __restrict__ ws) {
  __shared__ __attribute__((aligned(128))) unsigned char smem[LDS_BYTES];
  unsigned long long pc;
  asm volatile("s_getpc_b64 %0" : "=s"(pc) :: "v119", "a47");
  const unsigned sb = (unsigned)(size_t)(LAS unsigned char*)smem;
  const int w = __builtin_amdgcn_readfirstlane(threadIdx.x >> 6);
  const int b = blockIdx.x, xcd = b & 7, bi = b >> 3;
  const int grp = xcd * 4 + (bi >> 3), hg = bi & 7, cp = xcd * 32 + bi;
#define LANE_VARS() const int lane = lane_id(), h = lane >> 5, g4 = lane >> 4, l15 = lane & 15, l31 = lane & 31; \
  const unsigned r8 = lane >> 3, c8 = lane & 7; (void)h; (void)g4; (void)l15; (void)l31; (void)r8; (void)c8
  STAMP_INIT();
  asm volatile("" :: "s"(u), "s"(Are), "s"(Aim), "s"(Bre), "s"(Bim), "s"(Cre), "s"(Cim), "s"(logdt2), "s"(dskip2), "s"(x), "s"(wq),
               "s"(wk), "s"(wv), "s"(wo), "s"(out), "s"(ws));

  if (w >= 8) {
    LANE_VARS();
    const int j = w - 8, tid = j * 64 + lane, tch = j >> 1, par = j & 1;
    unsigned pAr, pAi, pBr, pBi, pCr, pCi, ld2, d2, cpf = 0;
    const unsigned po = (unsigned)((2 * cp + tch) * 256 + lane * 4);
    {
      const unsigned uo = (64 * bi + 16 * j + r8) * 1024 + xcd * 128 + ((c8 ^ wswz(r8)) << 4);
      gdma2(uo, (uo ^ 64) + 8192, u, sb + L_TA0 + j * 2048);
    }
    GLD1(pAr, po, Are); GLD1(pAi, po, Aim); GLD1(pBr, po, Bre); GLD1(pBi, po, Bim); GLD1(pCr, po, Cre); GLD1(pCi, po, Cim);
    LD32(sb + L_M + l31 * 4) = 0;
    asm volatile("s_load_dword %0, %2, %4\n\ts_load_dword %1, %3, %4\n\ts_waitcnt lgkmcnt(0)\n\ts_barrier"
                 : "=&s"(ld2), "=&s"(d2) : "s"(logdt2), "s"(dskip2), "s"(cp * 4) : "memory");
    const float ldt = tch ? bfhi(ld2) : bflo(ld2);
    const float eh = ldt * 1.44269502162933349609f, el = fmaf(ldt, 1.44269502162933349609f, -eh) + ldt * 1.925963033500011e-8f;
    const float e0 = __builtin_amdgcn_exp2f(eh);
    const float dt = bflo(pkbf(fmaf(e0, el * 0.693147180559945f, e0), 0.f));
    asm volatile("s_waitcnt vmcnt(0)" : "+v"(pAr), "+v"(pAi), "+v"(pBr), "+v"(pBi), "+v"(pCr), "+v"(pCi), "+v"(cpf) :: "memory");
    STAMP(1);
    const float ar0 = dt * bflo(pAr), ar1 = dt * bfhi(pAr);
    float lam = 0.f;
    if (__any(fmaxf(ar0, ar1) > 0.f)) lam = __uint_as_float(wave_umax(__float_as_uint(fmaxf(fmaxf(ar0, ar1), 0.f))));
    const float ar = (par ? ar1 : ar0) - lam, ai = dt * (par ? bfhi(pAi) : bflo(pAi));
    unsigned T[21];
    {
      cf c0 = cmul({bflo(pCr), bflo(pCi)}, {bflo(pBr), bflo(pBi)});
      cf c1 = cmul({bfhi(pCr), bfhi(pCi)}, {bfhi(pBr), bfhi(pBi)});
      int ex = (int)(wave_umax(__float_as_uint(fmaxf(fmaxf(fabsf(c0.x), fabsf(c0.y)), fmaxf(fabsf(c1.x), fabsf(c1.y))))) >> 23);
      ex = ex < 8 ? 8 : (ex > 245 ? 245 : ex);
      const float sdn = __uint_as_float((unsigned)(252 - ex) << 23), sup = __uint_as_float((unsigned)(ex + 2) << 23) * dt;
      LD64(sb + L_M + M_SCALE + tch * 8) = (u32x2){__float_as_uint(sup), __float_as_uint(lam * 1.44269504088896f)};
      cf c = par ? c1 : c0; c.x *= sdn; c.y *= sdn;
      const cf w1 = cexp_(ar, ai), W = cexp_(256.f * ar, 256.f * ai);
      const cf w2 = cmul(w1, w1), w4 = cmul(w2, w2), w6 = cmul(w4, w2);
      cf v = c;
#pragma unroll
      for (int k = 0; k < 8; ++k) { T[k] = pkh(v.x, v.y); if (k < 7) v = cmul(v, w1); }
      v = cmul(c, W); T[8] = pkh(v.x, v.y);
      cf e = cmul(v, w2); T[9] = pkh(e.x, e.y);
      e = cmul(v, w4); T[10] = pkh(e.x, e.y);
      e = cmul(v, w6); T[11] = pkh(e.x, e.y);
      v = cmul(v, W); T[12] = pkh(v.x, v.y);
      e = cmul(v, w4); T[13] = pkh(e.x, e.y);
      v = cmul(v, W); T[14] = pkh(v.x, v.y);
      e = cmul(v, w4); T[15] = pkh(e.x, e.y);
#pragma unroll
      for (int k = 16; k < 21; ++k) { v = cmul(v, W); T[k] = pkh(v.x, v.y); }
    }
    {
      cf p[16];
      cf g = cexp_(8.f * ar, -8.f * ai);
      p[0] = {1.f, 0.f}; p[1] = g;
      g = cmul(g, g); p[2] = g; p[3] = cmul(p[1], g);
      g = cmul(g, g);
      p[4] = g;
#pragma unroll
      for (int i = 1; i < 4; ++i) p[4 + i] = cmul(p[i], g);
      g = cmul(g, g);
      p[8] = g;
#pragma unroll
      for (int i = 1; i < 8; ++i) p[8 + i] = cmul(p[i], g);
      g = cmul(g, g);
#pragma unroll
      for (int i = 0; i < 16; ++i) {
        const cf q = i ? cmul(p[i], g) : g;
        asm volatile("v_cvt_pk_f16_f32 v[84+%c4], %0, %1\n\tv_cvt_pk_f16_f32 v[100+%c4], %2, %3" :: "v"(p[i].x), "v"(p[i].y), "v"(q.x), "v"(q.y), "i"(i));
      }
    }
#pragma unroll
    for (int k = 0; k < 21; ++k) asm volatile("" : "+v"(T[k]));
    STAMP(2);
    STAMP(3);
    {
      asm volatile("s_waitcnt vmcnt(0)" ::: "memory");
      sync4(sb, 5);
      u32x4 o0, o1;
      transpose_out(sb + L_TA0, sb + L_TB0 + j * 1024, j, lane, o0, o1);
      const __amdgpu_buffer_rsrc_t rs = __builtin_amdgcn_make_buffer_rsrc(ws + W_UT, 0, 512 * TROW, 0x00020000);
      const unsigned to = (64 * xcd + 16 * j + r8) * TROW + bi * 128 + c8 * 16;
      __builtin_amdgcn_raw_buffer_store_b128(o0, rs, to, 0, 16);
      __builtin_amdgcn_raw_buffer_store_b128(o1, rs, to + 8 * TROW, 0, 16);
    }
    await<1>(sb + L_M + M_AB + 12, 1u);
    const unsigned tag = __builtin_amdgcn_readfirstlane(LD32(sb + L_M + M_TAG));
    if (j == 0) {
      const unsigned fo = W_F1 + (b & (NREP - 1)) * 2048 + lane * 16;
      u32x4 f0, f1;
#define LOOK(T) asm volatile("global_load_dwordx4 %0, %2, %3 sc1\n\tglobal_load_dwordx4 %1, %2, %3 offset:1024 sc1\n\t" T "s_waitcnt vmcnt(0)" \
                     : "=&v"(f0), "=&v"(f1) : "v"(fo), "s"(ws) : "memory")
#define SEEN() __all(((f0[1] ^ tag) | (f0[3] ^ tag) | (f1[1] ^ tag) | (f1[3] ^ tag)) == 0u)
      __builtin_amdgcn_s_sleep(WSLEEP);
      LOOK("s_barrier\n\t");
      arrive(sb + L_M + M_SW + 24);
      _Pragma("unroll 1") for (unsigned spins = 0; spins < kSpinCap; ++spins) {
        if (SEEN()) break;
        LOOK("");
      }
      LD32(sb + L_M + M_KMAX) = wave_umax(umax(umax(f0[0], f0[2]), umax(f1[0], f1[2])));
      if (b == 0 && lane == 0) __hip_atomic_store((gu32*)(ws + W_E), tag, RLX_AGENT);
    } else {
      asm volatile("s_barrier\n\ts_waitcnt vmcnt(0)" ::: "memory");
      arrive(sb + L_M + M_SW + 24);
      if (j == 1) {
        await<1>(sb + L_M + M_SW + 24, 4u);
        if (lane == 0) __hip_atomic_store((gu32*)(ws + W_F3) + (xcd * 32 + bi) * 8, tag, RLX_AGENT);
      }
    }
    STAMP(4);
    BAR();
    __builtin_amdgcn_s_setprio(SSM_PRIO);
#define F3LOOK() if (j == 1) { wait_flags32<4>(ws + W_F3 + xcd * 1024, tag, lane); arrive(sb + L_M + M_SW + 40); }
    if (F3POS == 0) F3LOOK();
    {
      const unsigned c4 = (unsigned)(par * 64 + lane) * 4, tlo = sb + L_TLO + tch * TLO_CH + c4, thi = sb + L_THI + tch * THI_CH + c4;
#pragma unroll
      for (int i = 0; i < 32; ++i) asm volatile("ds_write_b32 %0, v[84+%c1] offset:%c2" :: "v"(tlo), "i"(i), "i"(i * 528) : "memory");
#pragma unroll
      for (int i = 0; i < 21; ++i) LD32(thi + i * 528) = T[i];
    }
    if (tid < 248) { const int c = tid >= 124, i = tid - 124 * c; LD128(sb + L_U + c * U_CH + (i >> 2) * 80 + (i & 3) * 16) = (u32x4){0u, 0u, 0u, 0u}; }
    u32x4 ua, ub;
#define ULOAD() { await<2>(sb + L_M + M_SW + 40, 1u); \
      const __amdgpu_buffer_rsrc_t rs = __builtin_amdgcn_make_buffer_rsrc(ws + W_UT, 0, 512 * TROW, 0x00020000); \
      ua = __builtin_amdgcn_raw_buffer_load_b128(rs, (2 * cp + tch) * TROW + par * 2048 + lane * 16, 0, 16); \
      ub = __builtin_amdgcn_raw_buffer_load_b128(rs, (2 * cp + tch) * TROW + par * 2048 + lane * 16 + 1024, 0, 16); }
    if (F3POS == 1) F3LOOK();
    if (F3POS <= 1) ULOAD();
    sync4(sb, 0);
    STAMP(5);
    const unsigned KT = sb + L_P;
    {
      const unsigned aa = sb + L_TLO + tch * TLO_CH + (16 * par + l15) * 528 + g4 * 16;
      const unsigned b0 = sb + L_THI + tch * THI_CH + l15 * 528 + g4 * 16, b1 = sb + L_THI + tch * THI_CH + (l15 < 4 ? 16 + l15 : 20) * 528 + g4 * 16;
      const u32x2 sc = LD64(sb + L_M + M_SCALE + tch * 8);
      f32x4 k0, k1;
#pragma unroll
      for (int ks = 0; ks < 8; ++ks) {
        const u32x4 fa = LD128(aa + ks * 64), fb = LD128(b0 + ks * 64), fc = LD128(b1 + ks * 64);
        if (ks == 0) asm volatile("v_mfma_f32_16x16x32_f16 %0, %3, %2, 0\n\tv_mfma_f32_16x16x32_f16 %1, %4, %2, 0" : "=&v"(k0), "=&v"(k1) : "v"(fa), "v"(fb), "v"(fc));
        else asm volatile("v_mfma_f32_16x16x32_f16 %0, %3, %2, %0\n\tv_mfma_f32_16x16x32_f16 %1, %4, %2, %1" : "+v"(k0), "+v"(k1) : "v"(fa), "v"(fb), "v"(fc));
      }
      asm volatile("s_nop 7" : "+v"(k0), "+v"(k1));
      const int lb = 128 * par + 8 * l15;
      const int l0 = lb + (g4 < 2 ? 4 * g4 : 256 * (g4 - 1)), s1 = g4 < 2 ? 1 : 2 * (g4 - 1), s2 = g4 < 3 ? 2 * s1 : 256, l1 = lb + 1024 + 1024 * g4;
      k0 *= __uint_as_float(sc[0]); k1 *= __uint_as_float(sc[0]);
      if (__builtin_amdgcn_readfirstlane(sc[1])) {
#pragma unroll
        for (int r = 0; r < 4; ++r) {
          k0[r] *= __builtin_amdgcn_exp2f(__uint_as_float(sc[1]) * (float)(l0 + (r & 1) * s1 + (r >> 1) * s2));
          k1[r] *= __builtin_amdgcn_exp2f(__uint_as_float(sc[1]) * (float)(l1 + 256 * r));
        }
      }
      if (l0 == 0) k0[0] += tch ? bfhi(d2) : bflo(d2);
      const unsigned long long g23 = 0xffffffff00000000ull, g3 = 0xffff000000000000ull;
      const unsigned long long g23d = par ? g23 : g23 & ~(1ull << 32), g3d = par ? g3 : g3 & ~(1ull << 48);
      const unsigned long long a0 = par ? 0xffffull : 0x1ffffull, a0d = par ? 0xffffull : 0x1fffeull, e0 = par ? 0x5555ull : 0x15555ull, e0d = par ? 0x5555ull : 0x15554ull;
      const unsigned ka0 = KT + l0 * 4 + 2 * tch, ka2 = ka0 + s2 * 4;
#define KW(R, o) "ds_write_b16" R " offset:" STR(o) "\n\t"
#define KR0 " %0, %5"
#define KR1 "_d16_hi %1, %5"
#define KR2 " %2, %6"
#define KR3 "_d16_hi %3, %6"
#define KS0 " %4, %7"
#define KS1 "_d16_hi %4, %7"
#define KS2 " %4, %8"
#define KS3 "_d16_hi %4, %8"
#define KW6(R, o) KW(R, o - 8) KW(R, o - 4) KW(R, o) KW(R, o + 4) KW(R, o + 8) KW(R, o + 12)
      asm volatile(KW(KR0, 16) KW(KR1, 16) KW(KR2, 16) KW(KR3, 16)
                   "s_mov_b64 exec, %9\n\t" KW(KR0, 20) KW(KR2, 20) KW(KR2, 12)
                   "s_mov_b64 exec, %10\n\t" KW(KR0, 12)
                   "s_mov_b64 exec, %11\n\t" KW(KR1, 20) KW(KR1, 12) KW(KR3, 20) KW(KR3, 12) KW(KR0, 24) KW(KR2, 24) KW(KR2, 8)
                   "s_mov_b64 exec, %12\n\t" KW(KR0, 8)
                   "s_mov_b64 exec, %13\n\t" KW6(KS0, 16)
                   "s_mov_b64 exec, %14\n\t" KW(KS0, 4)
                   "s_mov_b64 exec, %15\n\t" KW(KS0, 32)
                   "s_mov_b64 exec, %16\n\t" KW(KS0, 0)
                   "s_mov_b64 exec, 0xffff\n\t" KW6(KS1, 1040) KW(KS1, 1028) KW6(KS2, 2064) KW(KS2, 2052) KW6(KS3, 3088) KW(KS3, 3076)
                   "s_mov_b64 exec, 0x5555\n\t" KW(KS1, 1056) KW(KS1, 1024) KW(KS2, 2080) KW(KS2, 2048) KW(KS3, 3104) KW(KS3, 3072)
                   "s_mov_b64 exec, -1"
                   :: "v"(ka0), "v"(ka0 + s1 * 4), "v"(ka2), "v"(ka2 + s1 * 4), "v"(KT + l1 * 4 + 2 * tch), "v"(pkbf(k0[0], k0[1])), "v"(pkbf(k0[2], k0[3])),
                      "v"(pkbf(k1[0], k1[1])), "v"(pkbf(k1[2], k1[3])), "s"(g23), "s"(g23d), "s"(g3), "s"(g3d), "s"(a0), "s"(a0d), "s"(e0), "s"(e0d) : "memory");
      if (j == 0) LD128(KT) = (u32x4){0u, 0u, 0u, 0u};
    }
    arrive(sb + L_M + M_SW + 4);
    if (F3POS == 2) { F3LOOK(); ULOAD(); }
    await<1>(sb + L_M + M_SW + 4, 4u);
    {
      const unsigned uw = sb + L_U + tch * U_CH + (31 + 32 * par + (lane >> 2)) * 80 + (lane & 3) * 16;
      LD128(uw) = ua; LD128(uw + 16 * 80) = ub;
      const unsigned wo0 = (128 * j + r8) * 1024 + hg * 128 + ((c8 ^ wswz(r8)) << 4);
      gdma_rows16(wo0, (wo0 ^ 64) + 8192, wo, sb + L_WO + j * 16384);
    }
    STAMP(6);
#pragma unroll
    for (int k = 0; k < 2; ++k) {
      const u32x4 da = LD128(KT + tid * 16 + k * 4096), db = LD128(KT + tid * 16 + k * 4096 + 16);
      const unsigned d[7] = {da[1], da[2], da[3], db[0], db[1], db[2], db[3]};
      const unsigned ka = sb + L_KC + 224 + tid * 32 + k * 8192;
#pragma unroll
      for (int c = 0; c < 2; ++c) {
        unsigned P[6];
#pragma unroll
        for (int i = 0; i < 6; ++i) P[i] = __builtin_amdgcn_perm(d[i], d[i + 1], c ? 0x07060302u : 0x05040100u);
        LD128(ka + c * KC_CH) = (u32x4){P[2], P[0], P[3], P[1]};
        LD128(ka + c * KC_CH + 16) = (u32x4){P[4], P[2], P[5], P[3]};
      }
    }
    if (tid < 28) LD128(sb + L_KC + (tid >= 14) * (KC_CH - 224) + tid * 16) = (u32x4){0u, 0u, 0u, 0u};
    STAMP(7);
    sync4(sb, 2);
    STAMP(8);
    unsigned aA = sb + L_KC + tch * KC_CH + (unsigned)(8 * (16 * (32 * par - 1) + l31 - 8 * h + 24));
    unsigned aF = sb + L_U + tch * U_CH + (unsigned)(80 * (l31 + 31 - 16 * par) + 16 * h);
    f32x16 y0, y1;
    unsigned cl;
    const u32x2 zz = {0u, 0u};
#define CLD(R0, R1, R2, R3, R4, R5, OA, OF) \
      "ds_read_b64 v[" #R0 "], %3 offset:" #OA "+32\n\tds_read_b64 v[" #R1 "], %3 offset:" #OA "\n\tds_read_b128 v[" #R4 "], %4 offset:" #OF "\n\t" \
      "ds_read_b128 v[" #R5 "], %4 offset:" #OF "+2560\n\tds_read_b64 v[" #R2 "], %3 offset:" #OA "+8224\n\tds_read_b64 v[" #R3 "], %3 offset:" #OA "+8192\n\t"
#define CMM(A0, A1, F0, F1) "s_waitcnt lgkmcnt(6)\n\tv_mfma_f32_32x32x16_bf16 %0, v[" #A0 "], v[" #F0 "], %0\n\t" \
      CPAD "v_mfma_f32_32x32x16_bf16 %1, v[" #A0 "], v[" #F1 "], %1\n\t" CPAD "v_mfma_f32_32x32x16_bf16 %1, v[" #A1 "], v[" #F0 "], %1\n\t" CPAD
    asm volatile(
        "s_setprio " STR(CONV_PRIO) "\n\ts_nop 1\n\tv_mfma_f32_32x32x8_bf16 %0, %5, %5, 0\n\tv_mfma_f32_32x32x8_bf16 %1, %5, %5, 0\n\ts_mov_b32 %2, " STR(CONV_N) "\n\t"
        CLD(84:85, 86:87, 88:89, 90:91, 92:95, 96:99, 0, 32)
        "1:\n\t"
        CLD(100:101, 102:103, 104:105, 106:107, 108:111, 112:115, 128, 0)
        CMM(84:87, 88:91, 92:95, 96:99)
        "v_add_u32 %3, 0x100, %3\n\tv_add_u32 %4, 0xffffffb0, %4\n\t"
        CLD(84:85, 86:87, 88:89, 90:91, 92:95, 96:99, 0, 32)
        CMM(100:103, 104:107, 108:111, 112:115)
        "s_sub_u32 %2, %2, 1\n\ts_cmp_lg_u32 %2, 0\n\ts_cbranch_scc1 1b\n\t"
        "s_setprio " STR(SSM_PRIO) "\n\ts_waitcnt lgkmcnt(0)\n\ts_nop 15"
        : "=&v"(y0), "=&v"(y1), "=&s"(cl), "+v"(aA), "+v"(aF) : "v"(zz) : "memory", "scc");
    const unsigned pa = opaque(sb + L_P + lane * 16 + j * 4096), pb = opaque(sb + L_P + lane * 16 + (j ^ 1) * 4096);
#define YSEND(ys) _Pragma("unroll") for (int q = 0; q < 4; ++q) LDF4(pa + q * 1024) = (f32x4){ys[4 * q], ys[4 * q + 1], ys[4 * q + 2], ys[4 * q + 3]}
    if (par) { YSEND(y0); asm volatile("" ::: "memory"); } else { YSEND(y1); }
    STAMP(9);
    sync4(sb, 3);
    STAMP(12);
    {
      const unsigned yb = sb + L_P + 16384 + j * 2048, yw = opaque(yb + l31 * 64 + h * 8);
#define YKEEP(yk) _Pragma("unroll") for (int q = 0; q < 4; ++q) { \
        const f32x4 v = LDF4(pb + q * 1024) + (f32x4){yk[4 * q], yk[4 * q + 1], yk[4 * q + 2], yk[4 * q + 3]}; \
        LD64(yw + q * 16) = (u32x2){pkbf(v[0], v[1]), pkbf(v[2], v[3])}; }
      if (par) { YKEEP(y1); asm volatile("" ::: "memory"); } else { YKEEP(y0); }
      const u32x4 r0 = LD128(yb + lane * 16), r1 = LD128(yb + lane * 16 + 1024);
      asm volatile("" ::: "memory");
      arrive(sb + L_M + M_SW + 28);
      __builtin_amdgcn_s_setprio(YPRIO);
      const __amdgpu_buffer_rsrc_t rs = __builtin_amdgcn_make_buffer_rsrc(ws + W_YT, 0, 512 * TROW, 0x00020000);
      __builtin_amdgcn_raw_buffer_store_b128(r0, rs, (2 * cp + tch) * TROW + par * 2048 + lane * 16, 0, 16);
      __builtin_amdgcn_raw_buffer_store_b128(r1, rs, (2 * cp + tch) * TROW + par * 2048 + lane * 16 + 1024, 0, 16);
      STAMP(14);
      asm volatile("s_waitcnt vmcnt(0)" ::: "memory");
      STAMP(15);
      sync4(sb, 4);
      if (j == 0 && lane == 0) __hip_atomic_store((gu32*)(ws + W_F4) + (xcd * 32 + bi) * 8, tag, RLX_AGENT);
      STAMP(10);
      if (j == 0) { __builtin_amdgcn_s_sleep(16); wait_flags32<FSLEEP>(ws + W_F4 + xcd * 1024, tag, lane); arrive(sb + L_M + M_SW + 44); }
      else await<3>(sb + L_M + M_SW + 44, 1u);
      STAMP(16);
      const unsigned to = (64 * xcd + 16 * j + r8) * TROW + bi * 128;
      const u32x4 i0 = __builtin_amdgcn_raw_buffer_load_b128(rs, to + ((c8 ^ wswz(r8)) << 4), 0, 16);
      const u32x4 i1 = __builtin_amdgcn_raw_buffer_load_b128(rs, to + 8 * TROW + ((c8 ^ wswz(r8) ^ 4) << 4), 0, 16);
      LD128(sb + L_U + j * 2048 + lane * 16) = i0;
      LD128(sb + L_U + j * 2048 + 1024 + lane * 16) = i1;
      STAMP(17);
      sync4(sb, 1 + 8);
      STAMP(18);
      u32x4 o0, o1;
      transpose_out(sb + L_U, sb + L_U + 8192 + j * 1024, j, lane, o0, o1);
      const __amdgpu_buffer_rsrc_t ro = __builtin_amdgcn_make_buffer_rsrc(out, 0, 4194304, 0x00020000);
      const unsigned oo = (64 * bi + 16 * j + r8) * 2048 + xcd * 128 + c8 * 16;
      __builtin_amdgcn_raw_buffer_store_b128(o0, ro, oo, 0, YAUX);
      __builtin_amdgcn_raw_buffer_store_b128(o1, ro, oo + 16384, 0, YAUX);
    }
    STAMP(11);
    STAMP_OUT();
    return;
  }

  LANE_VARS();
  const int qq = w & 3, kh = w >> 2, chain = hg >> 2;
  const bool crit = w == (hg & 3);
  if (!crit) asm volatile("s_barrier" ::: "memory");
  else __builtin_amdgcn_s_setprio(3);
  if (kh == 0) {
    const unsigned xo = (64 * grp + 16 * qq + r8) * 1024 + ((c8 ^ (r8 >> 1)) << 4);
    gdma_x(xo, (xo ^ 64) + 8192, x, sb + L_X + qq * 16384);
    gdma_x(xo + 512, (xo ^ 64) + 8704, x, sb + L_X + qq * 16384 + 8192);
  } else {
    const unsigned wq0 = (64 * qq + r8) * 1024 + hg * 128 + ((c8 ^ ((r8 & 2) << 1)) << 4);
    gdma_rows8(wq0, wq0 + 8192, wq, sb + L_WQ + qq * 8192);
    gdma_rows8(wq0 + 262144, wq0 + 270336, wq, sb + L_WQ + 32768 + qq * 8192);
  }
  u32x2 fword;
  if (!crit) asm volatile("" : "=v"(fword));
  if (crit) {
    const unsigned short* wkv = chain ? wv : wk;
#pragma unroll
    for (int i = 0; i < 4; ++i) gdma4((unsigned)(i * 4096 + lane * 16), wkv, sb + L_R2 + i * 4096);
    asm volatile("s_barrier" ::: "memory");
    unsigned E;
    asm volatile("s_load_dword %0, %1, 0x0\n\ts_waitcnt lgkmcnt(0)" : "=s"(E) : "s"(ws) : "memory");
    const unsigned tg = E + (((E * 0x9E3779B1u ^ (E >> 15) ^ 0x5bd1e995u) & 0x7fffffffu) | 0x101u);
    LD32(sb + L_M + M_TAG) = tg;
    const unsigned wa = sb + L_R2 + (8 * g4 + (l15 >> 2)) * 32 + (lane & 3) * 8;
    const unsigned xe = sb + L_X + qq * 16384 + (l15 >> 3) * 4096 + (l15 & 7) * 128 + ((g4 ^ (l15 >> 1)) << 4);
    const unsigned tb = (2 * grp + (qq >> 1)) * KV_TILE;
    const unsigned off = chain ? ((g4 & 1) == 0 ? tb + 1024 + (qq & 1) * 640 + (g4 >> 1) * 256 + l15 * 16 : (lane >> 3) == 2 ? tb + 1536 + (qq & 1) * 640 + c8 * 16 : 0x80000000u)
                               : (g4 < 2 ? tb + (qq & 1) * 256 + g4 * 512 + l15 * 16 : 0x80000000u);
    f32x4 acc, acd;
#define KR(s, o) "v[36+8*(" #s "%%6)+" #o ":36+8*(" #s "%%6)+" #o "+3]"
#define K_LD(s, X) "ds_read_b128 " KR(s, 0) ", " X " offset:8192*(" #s "/8)+1024*((" #s "&7)/2)\n\t" \
      "ds_read_b64_tr_b16 v[36+8*(" #s "%%6)+4:36+8*(" #s "%%6)+5], %4 offset:1024*" #s "\n\tds_read_b64_tr_b16 v[36+8*(" #s "%%6)+6:36+8*(" #s "%%6)+7], %4 offset:1024*" #s "+128\n\t"
#define K_LD2(s, t) K_LD(s, "%2") K_LD(t, "%3")
#define K_MM(N, s, t, A, B, C0, C1) "s_waitcnt lgkmcnt(" #N ")\n\tv_mfma_f32_16x16x32_bf16 %0, " KR(s, A) ", " KR(s, B) ", " C0 "\n\t" \
      "v_mfma_f32_16x16x32_bf16 %1, " KR(t, A) ", " KR(t, B) ", " C1 "\n\t"
#define K_ALL(A, B) asm volatile("s_waitcnt lgkmcnt(0)\n\t" K_LD2(0, 1) K_LD2(2, 3) K_LD2(4, 5) K_MM(12, 0, 1, A, B, "0", "0") K_LD2(6, 7) K_MM(12, 2, 3, A, B, "%0", "%1") \
        K_LD2(8, 9) K_MM(12, 4, 5, A, B, "%0", "%1") K_LD2(10, 11) K_MM(12, 6, 7, A, B, "%0", "%1") K_LD2(12, 13) K_MM(12, 8, 9, A, B, "%0", "%1") \
        K_LD2(14, 15) K_MM(12, 10, 11, A, B, "%0", "%1") K_MM(6, 12, 13, A, B, "%0", "%1") K_MM(0, 14, 15, A, B, "%0", "%1") "s_nop 7" \
        : "=&v"(acc), "=&v"(acd) : "v"(xe), "v"(xe ^ 64), "v"(wa) : "memory", CLOB_V36_83)
    asm volatile("s_waitcnt vmcnt(0)" ::: "memory");
    arrive(sb + L_M + M_HA); arrive(sb + L_M + M_HA + 4);
    if (chain) K_ALL(0, 4); else K_ALL(4, 0);
    STAMP(1);
    acc = (acc + acd) * (chain ? 1.0f : 0.25f * 1.44269504088896f);
    const u32x2 pk = {pkbf(acc[0], acc[1]), pkbf(acc[2], acc[3])};
    const __amdgpu_buffer_rsrc_t rs = __builtin_amdgcn_make_buffer_rsrc(ws + W_KV, 0, KV_BYTES, 0x00020000);
    u32x4 pv;
    unsigned kn2 = 0;
    if (chain) {
      auto x0 = __builtin_amdgcn_permlane16_swap(pk[0], pk[0], false, false);
      auto x1 = __builtin_amdgcn_permlane16_swap(pk[1], pk[1], false, false);
      pv = (g4 & 1) ? (u32x4){0x3f803f80u, 0x3f803f80u, 0x3f803f80u, 0x3f803f80u} : (u32x4){x0[0], x1[0], x0[1], x1[1]};
    } else {
      auto x0 = __builtin_amdgcn_permlane32_swap(pk[0], pk[0], false, false);
      auto x1 = __builtin_amdgcn_permlane32_swap(pk[1], pk[1], false, false);
      pv = (u32x4){x0[0], x1[0], x0[1], x1[1]};
    }
    __builtin_amdgcn_raw_buffer_store_b128(pv, rs, off, 0, 16);
    if (!chain) {
      const float n2 = sq2(pk[0], sq2(pk[1], 0.f));
      kn2 = row_umax(__float_as_uint(sum32(sum16(n2, n2))));
    }
    fword = (u32x2){kn2, tg};
    STAMP(2);
  } else {
    asm volatile("s_waitcnt vmcnt(8)" ::: "memory");
    arrive(sb + L_M + M_HA);
    if (kh) { asm volatile("s_waitcnt vmcnt(0)" ::: "memory"); arrive(sb + L_M + M_HA + 4); }
  }
  STAMP(3);
  await<0>(sb + L_M + M_HA + 4 * kh, 8u);
  STAMP(4);

  f32x16 qa16;
  {
    const unsigned A0 = sb + L_WQ + (256 * kh + 8 * h + (l15 >> 2)) * 128 + (((4 * (qq & 1) + 2 * ((g4 & 1) ^ kh) + ((lane & 3) >> 1)) ^ ((l15 >> 3) << 2)) << 4) + (lane & 1) * 8;
    const unsigned X0 = sb + L_X + ((qq & 2) + (g4 & 1)) * 16384 + kh * 8192 + (l15 >> 3) * 4096 + (l15 & 7) * 128 + ((h ^ (l15 >> 1)) << 4);
#define Q_LD(s, X) "ds_read_b128 " KR(s, 0) ", " X " offset:1024*(" #s "/4)\n\t" \
      "ds_read_b64_tr_b16 v[36+8*(" #s "%%6)+4:36+8*(" #s "%%6)+5], %1 offset:2048*" #s "\n\tds_read_b64_tr_b16 v[36+8*(" #s "%%6)+6:36+8*(" #s "%%6)+7], %1 offset:2048*" #s "+512\n\t"
#define Q_MM(N, s, C) "s_waitcnt lgkmcnt(" #N ")\n\tv_mfma_f32_32x32x16_bf16 %0, " KR(s, 4) ", " KR(s, 0) ", " C "\n\t"
    asm volatile(Q_LD(0, "%2") Q_LD(1, "%3") Q_LD(2, "%4") Q_LD(3, "%5") Q_LD(4, "%2") Q_MM(12, 0, "0") Q_LD(5, "%3") Q_MM(12, 1, "%0") Q_LD(6, "%4") Q_MM(12, 2, "%0")
                 Q_LD(7, "%5") Q_MM(12, 3, "%0") Q_LD(8, "%2") Q_MM(12, 4, "%0")
                 "s_cmp_eq_u32 %6, 0\n\ts_cbranch_scc1 1f\n\ts_waitcnt vmcnt(0)\n\ts_cmp_eq_u32 %6, 1\n\ts_cbranch_scc1 2f\n\t"
                 "s_mov_b64 exec, " STR((1 << NREP) - 1) "\n\tglobal_store_dwordx2 %9, %10, %11 sc1\n\ts_setprio 0\n\t"
                 "2:\n\ts_mov_b64 exec, 1\n\tds_add_u32 %7, %8\n\ts_mov_b64 exec, -1\n\t1:\n\t"
                 Q_LD(9, "%3") Q_MM(12, 5, "%0") Q_LD(10, "%4") Q_MM(12, 6, "%0") Q_LD(11, "%5") Q_MM(12, 7, "%0")
                 Q_LD(12, "%2") Q_MM(12, 8, "%0") Q_LD(13, "%3") Q_MM(12, 9, "%0") Q_LD(14, "%4") Q_MM(12, 10, "%0") Q_LD(15, "%5") Q_MM(12, 11, "%0")
                 Q_MM(9, 12, "%0") Q_MM(6, 13, "%0") Q_MM(3, 14, "%0") Q_MM(0, 15, "%0") "s_nop 11"
                 : "=&v"(qa16) : "v"(A0), "v"(X0), "v"(X0 ^ 32), "v"(X0 ^ 64), "v"(X0 ^ 96),
                   "s"(__builtin_amdgcn_readfirstlane(opaque(crit ? 2 : kh ^ 1))), "v"(sb + L_M + (crit ? M_AB + 12 : M_HA + 4)), "v"(1u),
                   "v"(W_F1 + lane * 2048 + (bi * 8 + xcd) * 8), "v"(fword), "s"(ws) : "memory", "scc", CLOB_V36_83);
  }
  STAMP(5);
  const unsigned xa = opaque(sb + L_R2 + lane * 16 + w * 2048);
  LDF4(xa) = __builtin_shufflevector(qa16, qa16, 8, 9, 10, 11);
  LDF4(xa + 1024) = __builtin_shufflevector(qa16, qa16, 12, 13, 14, 15);
  BAR();
  STAMP(6);
  const i32x4 rsKV = make_srd(ws + W_KV, KV_BYTES);
  const unsigned ko = lane * 16, vo = l31 < 16 ? 1024 + h * 256 + l31 * 16 : l31 == 16 ? 1536u : 0x80000000u;
  const unsigned t0 = (16 * qq + 2 * bi) & 63;
  const unsigned tag = __builtin_amdgcn_readfirstlane(LD32(sb + L_M + M_TAG));
#define KV_FIRST() { unsigned tl_ = t0, sa_, sb_; asm volatile(A_FIRST : [tl] "+s"(tl_), [sa] "=&s"(sa_), [sb] "=&s"(sb_) \
    : [ko] "v"(ko), [vo] "v"(vo), [rk] "s"(rsKV) : "memory", "scc", CLOB_A4_47); }
  {
    const f32x4 q0 = __builtin_shufflevector(qa16, qa16, 0, 1, 2, 3) + LDF4(xa ^ 8192), q1 = __builtin_shufflevector(qa16, qa16, 4, 5, 6, 7) + LDF4((xa ^ 8192) + 1024);
    const u32x4 o = {pkbf(q0[0], q0[1]), pkbf(q0[2], q0[3]), pkbf(q1[0], q1[1]), pkbf(q1[2], q1[3])};
    LD128(sb + L_Q + (2 * qq + kh) * 1024 + lane * 16) = o;
    const unsigned n2 = row_umax(__float_as_uint(sum32(sq2(o[0], sq2(o[1], sq2(o[2], sq2(o[3], 0.f)))))));
    asm volatile("s_mov_b64 exec, 0x10001\n\tds_max_u32 %0, %1\n\ts_mov_b64 exec, -1" :: "v"(sb + L_M + M_QMAX), "v"(n2) : "memory");
  }
  STAMP(7);
  BAR();
  STAMP(8);
  KV_FIRST();

  const u32x2 qk2 = LD64(sb + L_M + M_QMAX);
  const float bound = fmaf(__builtin_amdgcn_sqrtf(__uint_as_float(qk2[0]) * __uint_as_float(qk2[1])), 1.002f, 1e-3f);
  const unsigned exact = __builtin_amdgcn_readfirstlane(opaque(bound <= 32.0f ? 0u : 1u));
  {
    unsigned tl = t0, n = 6, sa, sbo, sw = kh * PSW;
    const unsigned qb = sb + L_Q + kh * 4096, ob = sb + L_OA + w * 4 * OA_SLOT;
    if (exact) {
      const unsigned maddr = sb + L_MX + kh * 512 + l31 * 4;
      if (w < 4) LDF(sb + L_MX + (w * 64 + lane) * 4) = -3.0e38f;
      arrive(sb + L_M + M_AB + 4);
      await<0>(sb + L_M + M_AB + 4, 8u);
      float m[4] = {-3.0e38f, -3.0e38f, -3.0e38f, -3.0e38f};
      const __amdgpu_buffer_rsrc_t rs = __builtin_amdgcn_make_buffer_rsrc(ws + W_KV, 0, KV_BYTES, 0x00020000);
#pragma unroll 1
      for (unsigned i = 0; i < 16; ++i) {
        u32x4 kf = __builtin_amdgcn_raw_buffer_load_b128(rs, ko, ((tl + i) & 63) * KV_TILE, 16);
#pragma unroll
        for (int t = 0; t < 4; ++t) {
          const u32x4 q = LD128(qb + ko + t * 1024);
          f32x16 s_;
          asm volatile("s_nop 1\n\tv_mfma_f32_32x32x16_bf16 %0, %1, %2, 0\n\ts_nop 15" : "=&v"(s_) : "v"(kf), "v"(q) : CLOB_A4_47);
#pragma unroll
          for (int r = 0; r < 16; ++r) m[t] = fmaxf(m[t], s_[r]);
        }
      }
#pragma unroll
      for (int t = 0; t < 4; ++t) __builtin_amdgcn_ds_fmaxf((LAS float*)(size_t)(maddr + t * 128), m[t], __ATOMIC_RELAXED, __HIP_MEMORY_SCOPE_WORKGROUP, false);
      arrive(sb + L_M + M_AB + 8);
      await<0>(sb + L_M + M_AB + 8, 8u);
      asm volatile("v_accvgpr_write_b32 a44, %0\n\tv_accvgpr_write_b32 a45, %1\n\tv_accvgpr_write_b32 a46, %2\n\tv_accvgpr_write_b32 a47, %3"
                   :: "v"(-LDF(maddr)), "v"(-LDF(maddr + 128)), "v"(-LDF(maddr + 256)), "v"(-LDF(maddr + 384)) : CLOB_A4_47);
    }
    asm volatile(A_ATT
                 : [tl] "+s"(tl), [n] "+s"(n), [sa] "=&s"(sa), [sb] "=&s"(sbo), [sw] "+s"(sw)
                 : [ko] "v"(ko), [vo] "v"(vo), [rk] "s"(rsKV), [qb] "s"(qb), [ob] "s"(ob), [pf] "s"(sb + L_M + M_SW + 28), [ex] "s"(exact)
                 : "memory", "scc", "vcc", CLOB_V4_83, CLOB_A4_47);
  }
  STAMP(9);
  RSTAMP(17);
  BAR();
  STAMP(10);

  {
    LANE_VARS();
    {
      const unsigned s0 = opaque(sb + L_OA + (16 * kh + qq) * OA_SLOT + lane * 16), s1 = opaque(sb + L_OA + (16 * kh + qq) * OA_SLOT + 2048 + lane * 4);
      constexpr unsigned W1 = 4 * OA_SLOT;
      f32x4 o0 = (LDF4(s0) + LDF4(s0 + W1)) + (LDF4(s0 + 2 * W1) + LDF4(s0 + 3 * W1));
      f32x4 o1 = (LDF4(s0 + 1024) + LDF4(s0 + W1 + 1024)) + (LDF4(s0 + 2 * W1 + 1024) + LDF4(s0 + 3 * W1 + 1024));
      const float inv = __builtin_amdgcn_rcpf(sum32((LDF(s1) + LDF(s1 + W1)) + (LDF(s1 + 2 * W1) + LDF(s1 + 3 * W1))));
      o0 *= inv; o1 *= inv;
      u32x4 pk = {pkbf(o0[0], o0[1]), pkbf(o0[2], o0[3]), pkbf(o1[0], o1[1]), pkbf(o1[2], o1[3])};
      const __amdgpu_buffer_rsrc_t rsO = __builtin_amdgcn_make_buffer_rsrc(ws + W_OX, 0, 2097152, 0x00020000);
      __builtin_amdgcn_raw_buffer_store_b128(pk, rsO, lane * 16 + ((2 * grp + kh) * 32 + 4 * hg + qq) * 1024, 0, 16);
    }
    STAMP(11);
    RSTAMP(13);
    const unsigned r0 = 256 * kh + 16 * (g4 >> 1) + 4 * (g4 & 1) + (l15 >> 2);
    const unsigned A0 = sb + L_WO + r0 * 128 + (((4 * kh + ((lane & 3) >> 1)) ^ wswz(r0)) << 4) + (lane & 1) * 8;
    const unsigned oo = (g4 >> 1) * 1024 + ((g4 & 1) * 32 + 16 * (qq & 1) + l15) * 16 + ((2 * grp + (qq >> 1)) * 32 + 16 * kh) * 1024;
    const unsigned xa = sb + L_U + lane * 16;
    const unsigned go = (64 * grp + 16 * qq + l15) * 2048 + 1024 + 128 * hg + 64 * kh + 32 * (g4 & 1) + 16 * (g4 >> 1);
    const unsigned char* f2 = ws + W_F2 + grp * 1024;
    unsigned t0, t1;
#define T_WF1(F, B, s, e, P, Q) "ds_read_b64_tr_b16 " F "[" #B "+4*" #e ":" #B "+4*" #e "+1], " P " offset:4096*" #s "\n\t" \
                                "ds_read_b64_tr_b16 " F "[" #B "+4*" #e "+2:" #B "+4*" #e "+3], " Q " offset:4096*" #s "+1024\n\t"
#define T_WF(F, B, s) T_WF1(F, B, s, 0, "%[a0]", "v6") T_WF1(F, B, s, 1, "v5", "v7") T_WF1(F, B, s, 2, "v6", "%[a0]") T_WF1(F, B, s, 3, "v7", "v5")
#define T_M1(F, B, s, e, C) "v_mfma_f32_16x16x32_bf16 v[104+4*" #e ":107+4*" #e "], " F "[" #B "+4*" #e ":" #B "+4*" #e "+3], v[72+4*" #s ":75+4*" #s "], " C "\n\t"
#define T_MA(e) "v[104+4*" #e ":107+4*" #e "]"
#define T_MM(F, B, s, N) "s_waitcnt vmcnt(" #N ")\n\t" T_M1(F, B, s, 0, T_MA(0)) T_M1(F, B, s, 1, T_MA(1)) T_M1(F, B, s, 2, T_MA(2)) T_M1(F, B, s, 3, T_MA(3))
#define OPF "v_and_b32 v72, 0x3f0, %[xa]\n\tbuffer_load_dwordx4 v[108:111], v72, %[ro], %[own] offen sc1\n\t"
#define T_OL(s, so, imm) "buffer_load_dwordx4 v[72+4*" #s ":75+4*" #s "], %[oo], %[ro], " so " offen offset:" #imm " sc1\n\t"
    asm volatile(
        "v_xor_b32 v5, 32, %[a0]\n\tv_xor_b32 v6, 64, %[a0]\n\tv_xor_b32 v7, 0x60, %[a0]\n\t"
        T_WF("v", 8, 0) T_WF("v", 24, 1) T_WF("v", 40, 2)
        "s_waitcnt vmcnt(0)\n\t" OPF "s_barrier\n\t"
        TS(80) "s_cmp_lg_u32 %[w], 0\n\ts_cbranch_scc1 1f\n\t"
        "v_mov_b32 v72, %[fo]\n\tv_mov_b32 v73, %[tg]\n\ts_mov_b64 exec, 1\n\tglobal_store_dword v72, v73, %[f2] sc1\n\ts_mov_b64 exec, -1\n\t"
        "1:\n\t"
        T_WF("v", 56, 3) T_WF("a", 8, 4) T_WF("a", 24, 5)
        "s_cbranch_scc1 3f\n\t"
        "v_and_b32 v4, 0x70, %[xa]\n\tv_lshlrev_b32 v4, 3, v4\n\ts_mov_b32 %[t0], 0x100000\n\ts_sleep " STR(TSLEEP) "\n\t"
        "2:\n\tglobal_load_dword v104, v4, %[f2] sc1\n\ts_waitcnt vmcnt(0)\n\tv_cmp_ne_u32 vcc, %[tg], v104\n\ts_cbranch_vccz 3f\n\t"
        "s_sub_u32 %[t0], %[t0], 1\n\ts_cbranch_scc0 2b\n\t"
        "3:\n\ts_barrier\n\t" TS(82) "s_movk_i32 %[t0], 0x1000\n\ts_movk_i32 %[t1], 0x2000\n\t"
        T_OL(0, "0", 0) T_OL(1, "0", 2048) T_OL(2, "%[t0]", 0) T_OL(3, "%[t0]", 2048) "s_movk_i32 %[t0], 0x3000\n\t"
        T_OL(4, "%[t1]", 0) T_OL(5, "%[t1]", 2048) T_OL(6, "%[t0]", 0) T_OL(7, "%[t0]", 2048)
        "s_lshl_b32 %[t0], %[w], 11\n\ts_xor_b32 %[t1], %[t0], 0x2000\n\t"
        "s_waitcnt lgkmcnt(0) vmcnt(7)\n\t" T_M1("v", 8, 0, 0, "0") T_M1("v", 8, 0, 1, "0") T_M1("v", 8, 0, 2, "0") T_M1("v", 8, 0, 3, "0")
        T_WF("v", 8, 6) T_MM("v", 24, 1, 6) T_WF("v", 24, 7) T_MM("v", 40, 2, 5) T_MM("v", 56, 3, 4) T_MM("a", 8, 4, 3) T_MM("a", 24, 5, 2)
        "s_waitcnt lgkmcnt(0)\n\t" T_MM("v", 8, 6, 1) T_MM("v", 24, 7, 0)
        TS(84) "v_add_u32 v4, %[t0], %[xa]\n\tv_add_u32 v5, %[t1], %[xa]\n\ts_nop 6\n\t"
        "ds_write_b128 v4, v[112:115]\n\tds_write_b128 v4, v[116:119] offset:1024\n\ts_waitcnt lgkmcnt(0)\n\ts_barrier\n\t"
        "ds_read_b128 v[72:75], v5\n\tds_read_b128 v[76:79], v5 offset:1024\n\ts_waitcnt lgkmcnt(1)\n\t"
        "v_add_f32 v72, v72, v104\n\tv_add_f32 v73, v73, v105\n\tv_add_f32 v74, v74, v106\n\tv_add_f32 v75, v75, v107\n\ts_waitcnt lgkmcnt(0)\n\t"
        "v_add_f32 v76, v76, v108\n\tv_add_f32 v77, v77, v109\n\tv_add_f32 v78, v78, v110\n\tv_add_f32 v79, v79, v111\n\t"
        "v_cvt_pk_bf16_f32 v80, v72, v73\n\tv_cvt_pk_bf16_f32 v81, v74, v75\n\tv_cvt_pk_bf16_f32 v82, v76, v77\n\tv_cvt_pk_bf16_f32 v83, v78, v79\n\ts_nop 1\n\t"
        "v_permlane16_swap_b32 v80, v82\n\tv_permlane16_swap_b32 v81, v83\n\ts_nop 1\n\t"
        "global_store_dwordx4 %[go], v[80:83], %[out] " OAUX "\n\ts_nop 1"
        : [t0] "=&s"(t0), [t1] "=&s"(t1)
        : [a0] "v"(A0), [oo] "v"(oo), [xa] "v"(xa), [go] "v"(go), [w] "s"(w), [fo] "s"(hg * 128), [own] "s"(((2 * grp + kh) * 32 + 4 * hg + qq) * 1024), [tg] "s"(tag), [f2] "s"(f2),
          [ro] "s"(make_srd(ws + W_OX, 2097152)), [out] "s"(out)
        : "memory", "scc", "vcc", CLOB_V4_83, CLOB_A4_47 TSCLOB);
    TSTAMP_OUT();
    STAMP(12);
    STAMP_OUT();
  }
}

extern "C" void kernel_launch(void* const* d_in, const int* in_sizes, int n_in, void* d_out, int out_size, void* d_ws,
                              size_t ws_size, hipStream_t stream) {
  typedef const unsigned short* P;
  fused_kernel<<<256, 768, 0, stream>>>((P)d_in[0], (P)d_in[1], (P)d_in[2], (P)d_in[3], (P)d_in[4], (P)d_in[5], (P)d_in[6],
                                        (const unsigned*)d_in[7], (const unsigned*)d_in[8], (P)d_in[9], (P)d_in[10], (P)d_in[11],
                                        (P)d_in[12], (P)d_in[13], (unsigned short*)d_out, (unsigned char*)d_ws);
}
```

```cpp
#include <hip/hip_runtime.h>
#include <hip/hip_bf16.h>
#include <stdint.h>

typedef float f32x16 __attribute__((ext_vector_type(16)));
typedef float f32x8 __attribute__((ext_vector_type(8)));
typedef float f32x4 __attribute__((ext_vector_type(4)));
typedef float f32x2 __attribute__((ext_vector_type(2)));
typedef __bf16 bf16x8 __attribute__((ext_vector_type(8)));
typedef __bf16 bf16x2 __attribute__((ext_vector_type(2)));
typedef _Float16 f16x8 __attribute__((ext_vector_type(8)));
typedef _Float16 f16x2 __attribute__((ext_vector_type(2)));
typedef short s16x4 __attribute__((ext_vector_type(4)));
typedef unsigned u32x4 __attribute__((ext_vector_type(4)));
typedef unsigned u32x2 __attribute__((ext_vector_type(2)));
#define LAS __attribute__((address_space(3)))
#define GAS __attribute__((address_space(1)))
typedef GAS unsigned gu32;
typedef GAS unsigned long long gu64;
#define RLX_AGENT __ATOMIC_RELAXED, __HIP_MEMORY_SCOPE_AGENT

constexpr unsigned L_X = 0;
constexpr unsigned L_TLO = 0, TLO_CH = 16896, L_THI = 33792, THI_CH = 11088;
constexpr unsigned L_WO = 0;
constexpr unsigned L_B = 65536;
constexpr unsigned L_WQ = L_B;
constexpr unsigned L_R2 = L_B + 65536;
constexpr unsigned L_TA0 = L_B + 53248, L_TB0 = L_TA0 + 8192;
constexpr unsigned L_KC = L_B, KC_CH = 16640;
constexpr unsigned L_OA = L_B, OA_SLOT = 2304;
constexpr unsigned L_P = L_B + 40960;
constexpr unsigned L_U = L_B + 73728, U_CH = 7600;
constexpr unsigned L_Q = L_B + 88928;
constexpr unsigned L_MX = L_Q + 8192;
constexpr unsigned L_M = L_MX + 1024;
constexpr unsigned LDS_BYTES = L_M + 128;
static_assert(LDS_BYTES <= 163840, "lds");
constexpr unsigned M_TAG = 0, M_RDY = 4, M_QMAX = 8, M_KMAX = 12, M_SCALE = 16  , M_SW = 32  , M_HA = 96  , M_AB = 112  ;
#define CONV_PRIO 3
#define CONV_N 16
#define CNOP 9
#define CPAD "s_nop " STR(CNOP) "\n\t"
#define PSW 2
#define NREP 8
#define WSLEEP 10
#define FSLEEP 6
#define YPRIO 3
#define TSLEEP 8
#define YAUX 16
#define OAUX "sc1"
#define EARLYPUB 1
#define WHELP 0
#define PDELAY -1
#define TWPOS 0
#define WOPOS 1
#define UWAIT "s_waitcnt vmcnt(0)"
#define SSM_PRIO 3

constexpr unsigned KV_TILE = 2304, KV_BYTES = 64 * KV_TILE;
static_assert(NREP <= 8, "flag copies");
#define TROW_ 4224
constexpr unsigned TROW = TROW_;
constexpr unsigned W_E = 0, W_F1 = 8192  , W_F2 = W_F1 + 16384  , W_F3 = W_F2 + 32768  ,
                   W_F4 = W_F3 + 8192, W_KV = W_F1 + 65536, W_OX = W_KV + KV_BYTES,
                   W_UT = W_OX + 2097152, W_YT = W_UT + 512 * TROW, W_END = W_YT + 512 * TROW;
constexpr unsigned W_DBG = W_END;

constexpr int CODE_LINES = 150;
constexpr unsigned kSpinCap = 1u << 20;

#define LD32(a) (*(LAS unsigned*)(size_t)(a))
#define LDF(a) (*(LAS float*)(size_t)(a))
#define LD64(a) (*(LAS u32x2*)(size_t)(a))
#define LD128(a) (*(LAS u32x4*)(size_t)(a))
#define LDF4(a) (*(LAS f32x4*)(size_t)(a))
#define BAR() asm volatile("s_waitcnt lgkmcnt(0)\n\ts_barrier" ::: "memory")

#define RSTAMP(i)
#define TS(k)
#define TSTAMP_OUT()
#define TSCLOB
#define STAMP(i)
#define STAMP_INIT()
#define STAMP_OUT()

__device__ __forceinline__ float bflo(unsigned v) { return __uint_as_float(v << 16); }
__device__ __forceinline__ float bfhi(unsigned v) { return __uint_as_float(v & 0xffff0000u); }
__device__ __forceinline__ unsigned pkbf(float a, float b) { f32x2 x = {a, b}; return __builtin_bit_cast(unsigned, __builtin_convertvector(x, bf16x2)); }
__device__ __forceinline__ unsigned pkh(float a, float b) { f32x2 x = {a, b}; return __builtin_bit_cast(unsigned, __builtin_convertvector(x, f16x2)); }
template <int CTRL> __device__ __forceinline__ unsigned dppu(unsigned v) { return __builtin_amdgcn_update_dpp(0u, v, CTRL, 0xf, 0xf, true); }
__device__ __forceinline__ unsigned umax(unsigned a, unsigned b) { return a > b ? a : b; }
__device__ __forceinline__ unsigned row_umax(unsigned v) {
  v = umax(v, dppu<0x128>(v)); v = umax(v, dppu<0x124>(v)); v = umax(v, dppu<0x122>(v)); return umax(v, dppu<0x121>(v));
}
__device__ __forceinline__ unsigned wave_umax(unsigned v) {
  v = row_umax(v);
  auto a = __builtin_amdgcn_permlane16_swap(v, v, false, false);
  v = umax(a[0], a[1]);
  auto c = __builtin_amdgcn_permlane32_swap(v, v, false, false);
  return umax(c[0], c[1]);
}
__device__ __forceinline__ float sum16(float a, float b) {
  auto r = __builtin_amdgcn_permlane16_swap(__float_as_uint(a), __float_as_uint(b), false, false);
  return __uint_as_float(r[0]) + __uint_as_float(r[1]);
}
__device__ __forceinline__ float sum32(float v) {
  auto c = __builtin_amdgcn_permlane32_swap(__float_as_uint(v), __float_as_uint(v), false, false);
  return __uint_as_float(c[0]) + __uint_as_float(c[1]);
}
__device__ __forceinline__ float sq2(unsigned v, float c) {
  return __builtin_amdgcn_fdot2_f32_bf16(__builtin_bit_cast(bf16x2, v), __builtin_bit_cast(bf16x2, v), c, false);
}
__device__ __forceinline__ unsigned opaque(unsigned v) { asm volatile("" : "+v"(v)); return v; }
__device__ __forceinline__ int lane_id() {
  int l;
  asm volatile("v_mbcnt_lo_u32_b32 %0, -1, 0\n\tv_mbcnt_hi_u32_b32 %0, -1, %0" : "=v"(l));
  __builtin_assume(l >= 0 && l < 64);
  return l;
}
struct cf { float x, y; };
__device__ __forceinline__ cf cmul(cf a, cf b) { return {a.x * b.x - a.y * b.y, a.x * b.y + a.y * b.x}; }
__device__ __forceinline__ cf cexp_(float re, float im) {
  float m = __builtin_amdgcn_exp2f(re * 1.44269504088896f);
  float f = __builtin_amdgcn_fractf(im * 0.159154943091895f);
  return {m * __builtin_amdgcn_cosf(f), m * __builtin_amdgcn_sinf(f)};
}
#define GLD1(dst, voff, base) asm volatile("global_load_dword %0, %1, %2" : "=v"(dst) : "v"(voff), "s"(base))
typedef int i32x4 __attribute__((ext_vector_type(4)));
__device__ __forceinline__ i32x4 make_srd(const void* p, unsigned bytes) {
  const unsigned long long a = (unsigned long long)p;
  return (i32x4){(int)(unsigned)a, (int)((unsigned)(a >> 32) & 0xffffu), (int)bytes, 0x00020000};
}

__device__ __forceinline__ void gdma4(unsigned voff, const void* base, unsigned lds_dst) {
  unsigned keep;
  asm volatile("s_mov_b32 %0, m0\n\ts_mov_b32 m0, %3\n\ts_nop 0\n\tglobal_load_lds_dwordx4 %1, %2\n\tglobal_load_lds_dwordx4 %1, %2 offset:1024\n\t"
               "global_load_lds_dwordx4 %1, %2 offset:2048\n\tglobal_load_lds_dwordx4 %1, %2 offset:3072\n\ts_mov_b32 m0, %0"
               : "=&s"(keep) : "v"(voff), "s"(base), "s"(lds_dst) : "memory");
}
#define DMA_PAIR "buffer_load_dwordx4 %[v0], %[rs], %[so] offen lds\n\ts_add_u32 m0, m0, 0x400\n\ts_nop 0\n\t" \
                 "buffer_load_dwordx4 %[v1], %[rs], %[so] offen lds\n\ts_add_u32 m0, m0, 0x400\n\ts_add_u32 %[so], %[so], 0x4000\n\t"
__device__ __forceinline__ void gdma_rows16(unsigned v0, unsigned v1, const void* base, unsigned lds_dst) {
  unsigned keep, so;
  asm volatile("s_mov_b32 %[k], m0\n\ts_mov_b32 m0, %[d]\n\ts_mov_b32 %[so], 0\n\t" DMA_PAIR DMA_PAIR DMA_PAIR DMA_PAIR DMA_PAIR DMA_PAIR DMA_PAIR DMA_PAIR "s_mov_b32 m0, %[k]"
               : [k] "=&s"(keep), [so] "=&s"(so) : [rs] "s"(make_srd(base, 524288)), [v0] "v"(v0), [v1] "v"(v1), [d] "s"(lds_dst) : "memory", "scc");
}
#define DMA_X(v, o) "global_load_lds_dwordx4 " v ", %1 offset:" #o "\n\ts_add_u32 m0, m0, "
__device__ __forceinline__ void gdma_x(unsigned v0, unsigned v1, const void* base, unsigned lds_dst) {
  unsigned keep;
  asm volatile("s_mov_b32 %0, m0\n\ts_mov_b32 m0, %4\n\ts_nop 0\n\t"
               DMA_X("%2", 0) "896\n\ts_nop 0\n\t" DMA_X("%2", 128) "896\n\ts_nop 0\n\t" DMA_X("%2", 256) "896\n\ts_nop 0\n\t" DMA_X("%2", 384) "1408\n\ts_nop 0\n\t"
               DMA_X("%3", 0) "896\n\ts_nop 0\n\t" DMA_X("%3", 128) "896\n\ts_nop 0\n\t" DMA_X("%3", 256) "896\n\ts_nop 0\n\t" DMA_X("%3", 384) "0\n\t"
               "s_mov_b32 m0, %0"
               : "=&s"(keep) : "s"(base), "v"(v0), "v"(v1), "s"(lds_dst) : "memory", "scc");
}
__device__ __forceinline__ void gdma_rows8(unsigned v0, unsigned v1, const void* base, unsigned lds_dst) {
  unsigned keep, so;
  asm volatile("s_mov_b32 %[k], m0\n\ts_mov_b32 m0, %[d]\n\ts_mov_b32 %[so], 0\n\t" DMA_PAIR DMA_PAIR DMA_PAIR DMA_PAIR "s_mov_b32 m0, %[k]"
               : [k] "=&s"(keep), [so] "=&s"(so) : [rs] "s"(make_srd(base, 524288)), [v0] "v"(v0), [v1] "v"(v1), [d] "s"(lds_dst) : "memory", "scc");
}
__device__ __forceinline__ unsigned wswz(unsigned row) { return ((((row >> 1) & 3u) ^ (((row >> 3) & 1u) << 1)) << 1); }

__device__ __forceinline__ void arrive(unsigned addr) {
  asm volatile("s_mov_b64 exec, 1\n\tds_add_u32 %0, %1\n\ts_mov_b64 exec, -1" :: "v"(addr), "v"(1u) : "memory");
}
template <int SLEEP>
__device__ __forceinline__ void await(unsigned addr, unsigned count) {
  _Pragma("unroll 1") for (unsigned spins = 0; spins < kSpinCap; ++spins) {
    unsigned v;
    asm volatile("ds_read_b32 %0, %1\n\ts_waitcnt lgkmcnt(0)" : "=v"(v) : "v"(addr) : "memory");
    if (__builtin_amdgcn_readfirstlane(v) >= count) break;
    if (SLEEP) __builtin_amdgcn_s_sleep(SLEEP);
  }
}
template <int OFF>
__device__ __forceinline__ void arrive_(unsigned mb, unsigned one) {
  asm volatile("s_mov_b64 exec, 1\n\tds_add_u32 %0, %1 offset:%c2\n\ts_mov_b64 exec, -1" :: "v"(mb), "v"(one), "i"(OFF) : "memory");
}
template <int OFF, int SLEEP>
__device__ __forceinline__ void await_(unsigned mb, unsigned count) {
  _Pragma("unroll 1") for (unsigned spins = 0; spins < kSpinCap; ++spins) {
    unsigned v;
    asm volatile("ds_read_b32 %0, %1 offset:%c2\n\ts_waitcnt lgkmcnt(0)" : "=v"(v) : "v"(mb), "i"(OFF) : "memory");
    if (__builtin_amdgcn_readfirstlane(v) >= count) break;
    __builtin_amdgcn_s_sleep(SLEEP);
  }
}
#define SYNC4(k) { arrive_<M_SW + 4 * (k)>(mb, one); await_<M_SW + 4 * (k), 2>(mb, 4u); }
template <int SLEEP>
__device__ __forceinline__ void wait_flags32(const unsigned char* base, unsigned voff, unsigned tag) {
  _Pragma("unroll 1") for (unsigned spins = 0; spins < kSpinCap; ++spins) {
    unsigned f;
    asm volatile("global_load_dword %0, %1, %2 sc1\n\ts_waitcnt vmcnt(0)" : "=v"(f) : "v"(voff), "s"(base) : "memory");
    if (__all(f == tag)) break;
    __builtin_amdgcn_s_sleep(SLEEP);
  }
  __builtin_amdgcn_fence(__ATOMIC_ACQUIRE, "wavefront");
}
struct TrAddr { unsigned ra, rx, wa, wx, rb; };
__device__ __forceinline__ TrAddr transpose_addr(unsigned img, unsigned scr, int j, int lane) {
  const unsigned g = lane >> 4, i = lane & 15, row = 8 * g + (i >> 2);
  const unsigned ra = img + row * 128 + (((2 * j + ((lane & 3) >> 1)) ^ wswz(row)) << 4) + (lane & 1) * 8;
  const unsigned wa = scr + (i & 7) * 128 + ((g ^ (i & 7)) << 4), rb = scr + (lane >> 3) * 128 + (((lane & 7) ^ (lane >> 3)) << 4);
  return {opaque(ra), opaque(ra ^ 64), opaque(wa), opaque(wa ^ 64), opaque(rb)};
}
template <int OFF>
__device__ __forceinline__ void transpose_out(const TrAddr& t, u32x4& o0, u32x4& o1) {
  u32x2 t0, t1, t2, t3;
  asm volatile("ds_read_b64_tr_b16 %0, %4 offset:%c6\n\tds_read_b64_tr_b16 %1, %5 offset:%c6+512\n\tds_read_b64_tr_b16 %2, %4 offset:%c6+4096\n\t"
               "ds_read_b64_tr_b16 %3, %5 offset:%c6+4608\n\ts_waitcnt lgkmcnt(0)"
               : "=&v"(t0), "=&v"(t1), "=&v"(t2), "=&v"(t3) : "v"(t.ra), "v"(t.rx), "i"(OFF) : "memory");
  const u32x4 c0 = {t0[0], t0[1], t1[0], t1[1]}, c1 = {t2[0], t2[1], t3[0], t3[1]};
  asm volatile("s_mov_b64 exec, %6\n\tds_write_b128 %2, %4 offset:%c8\n\tds_write_b128 %3, %5 offset:%c8\n\ts_mov_b64 exec, -1\n\tds_read_b128 %0, %7 offset:%c8\n\t"
               "s_not_b64 exec, %6\n\tds_write_b128 %2, %4 offset:%c8\n\tds_write_b128 %3, %5 offset:%c8\n\ts_mov_b64 exec, -1\n\tds_read_b128 %1, %7 offset:%c8\n\ts_waitcnt lgkmcnt(0)"
               : "=&v"(o0), "=&v"(o1) : "v"(t.wa), "v"(t.wx), "v"(c0), "v"(c1), "s"(0x00ff00ff00ff00ffull), "v"(t.rb), "i"(OFF) : "memory", "scc");
}

#define STR_(x) #x
#define STR(x) STR_(x)
#define VR(b, o) "v[" STR(b) "+" STR(o) "]"
#define VRR(b, o, n) "v[" STR(b) "+" STR(o) ":" STR(b) "+" STR(o) "+" STR(n) "-1]"
#define AR4(i) "a[" STR(i) ":" STR(i) "+3]"
#define C4(p, a, b, c, d) p #a, p #b, p #c, p #d
#define C8(p, a, b, c, d, e, f, g, h) p #a, p #b, p #c, p #d, p #e, p #f, p #g, p #h
#define CLOB_V36_83 C4("v", 36, 37, 38, 39), C8("v", 40, 41, 42, 43, 44, 45, 46, 47), C8("v", 48, 49, 50, 51, 52, 53, 54, 55), C8("v", 56, 57, 58, 59, 60, 61, 62, 63), \
  C8("v", 64, 65, 66, 67, 68, 69, 70, 71), C8("v", 72, 73, 74, 75, 76, 77, 78, 79), C4("v", 80, 81, 82, 83)
#define CLOB_V4_83 C4("v", 4, 5, 6, 7), C8("v", 8, 9, 10, 11, 12, 13, 14, 15), C8("v", 16, 17, 18, 19, 20, 21, 22, 23), C8("v", 24, 25, 26, 27, 28, 29, 30, 31), \
  C4("v", 32, 33, 34, 35), CLOB_V36_83
#define CLOB_A4_47 C4("a", 4, 5, 6, 7), C8("a", 8, 9, 10, 11, 12, 13, 14, 15), C8("a", 16, 17, 18, 19, 20, 21, 22, 23), C8("a", 24, 25, 26, 27, 28, 29, 30, 31), \
  C8("a", 32, 33, 34, 35, 36, 37, 38, 39), C8("a", 40, 41, 42, 43, 44, 45, 46, 47)
#define BUF0(o) "a[4+" #o ":7+" #o "]"
#define BUF1(o) "a[16+" #o ":19+" #o "]"
#define BUF2(o) "v[100+" #o ":103+" #o "]"
#define A_EX(S, i) "v_exp_f32 " VR(S, i) ", " VR(S, i) "\n\t"
#define A_QK(S, B, q, C) "v_mfma_f32_32x32x16_bf16 " VRR(S, 0, 16) ", " B(0) ", " AR4(28 + 4 * q) ", " C "\n\t"
#define A_PV0(o, B, S) "v_smfmac_f32_32x32x32_bf16 " VRR(4, 16 * o, 16) ", " B(4) ", " VRR(S, 0, 8) ", v116\n\t"
#define A_PV1(o, B, S) "v_smfmac_f32_32x32x32_bf16 " VRR(4, 16 * o, 16) ", " B(8) ", " VRR(S, 8, 8) ", v116\n\t"
#define A_RD4(o, q) "v_accvgpr_read_b32 " VR(100, o) ", a[44+" #q "]\n\tv_accvgpr_read_b32 " VR(101, o) ", a[44+" #q "]\n\t" \
                    "v_accvgpr_read_b32 " VR(102, o) ", a[44+" #q "]\n\tv_accvgpr_read_b32 " VR(103, o) ", a[44+" #q "]\n\t"
#define A_SHIFT_EXACT(q) A_RD4(0, q) A_RD4(4, q) A_RD4(8, q) A_RD4(12, q) "s_nop 1\n\t"
#define A_SHIFT_NONE(q) ""
#define A_BLOCK(Sc, Sp, op, Bp, Bn, qn, oc, Bc, SH, C, XM, X3, X5, X7, X10, X12) \
  A_EX(Sc, 0) A_EX(Sc, 1) A_PV1(op, Bp, Sp) XM A_EX(Sc, 2) A_EX(Sc, 3) X3 A_EX(Sc, 4) A_EX(Sc, 5) X5 SH(qn) A_QK(Sp, Bn, qn, C) \
  A_EX(Sc, 6) A_EX(Sc, 7) X7 A_EX(Sc, 8) A_EX(Sc, 9) A_EX(Sc, 10) X10 A_PV0(oc, Bc, Sc) \
  A_EX(Sc, 11) A_EX(Sc, 12) X12 A_EX(Sc, 13) A_EX(Sc, 14) A_EX(Sc, 15)
#define A_LK(B, so) "buffer_load_dwordx4 " B(0) ", %[ko], %[rk], " so " offen sc1\n\t"
#define A_LV0(B, so) "buffer_load_dwordx4 " B(4) ", %[vo], %[rk], " so " offen sc1\n\t"
#define A_LV1(B, so) "buffer_load_dwordx4 " B(8) ", %[vo], %[rk], " so " offen offset:640 sc1\n\t"
#define A_NEXT "s_add_u32 %[tl], %[tl], 1\n\ts_and_b32 %[tl], %[tl], 63\n\t"
#define A_OFF(r) "s_mul_i32 %[" #r "], %[tl], 2304\n\t"
#define A_STEP(B, Bp, Bn, SH, C, W, XM, E) \
  A_BLOCK(68, 84, 3, Bp, B, 1, 0, B, SH, C, XM, A_LV1(Bp, "%[sa]"), "", "", W, "") \
  A_BLOCK(84, 68, 0, B, B, 2, 1, B, SH, C, "", "s_mov_b32 %[sa], %[sb]\n\t", "", A_NEXT, "", "") \
  A_BLOCK(68, 84, 1, B, B, 3, 2, B, SH, C, "", "", "", A_LK(B, "%[sb]"), "", "") \
  A_BLOCK(84, 68, 2, B, Bn, 0, 3, B, SH, C, "", "", W, E, "", A_LV0(B, "%[sb]") A_OFF(sb))
#define A_ZERO(o) "v_mfma_f32_32x32x8_bf16 " VRR(4, 16 * o, 16) ", v[116:117], v[116:117], 0\n\t"
#define A_FLUSH(o) "ds_write_b128 v118, " VRR(4, 16 * o, 4) " offset:2304*" #o "\n\tds_write_b128 v118, " VRR(8, 16 * o, 4) " offset:2304*" #o "+1024\n\t" \
                   "ds_write_b32 v119, " VR(12, 16 * o) " offset:2304*" #o "+2048\n\t"
#define A_FIRST \
  A_OFF(sb) A_NEXT A_OFF(sa) A_LK(BUF0, "%[sb]") A_LV0(BUF0, "%[sb]") A_LV1(BUF0, "%[sb]") A_NEXT A_OFF(sb) \
  A_LK(BUF1, "%[sa]") A_LV0(BUF1, "%[sa]") A_LV1(BUF1, "%[sa]") A_LK(BUF2, "%[sb]") A_LV0(BUF2, "%[sb]")
#define A_SETUP \
  "v_add_u32 v118, %[qb], %[ko]\n\tv_mov_b32 v116, 0\n\tv_mov_b32 v117, 0\n\t" \
  "ds_read_b128 a[28:31], v118\n\tds_read_b128 a[32:35], v118 offset:1024\n\tds_read_b128 a[36:39], v118 offset:2048\n\tds_read_b128 a[40:43], v118 offset:3072\n\t" \
  A_ZERO(0) A_ZERO(1) A_ZERO(2) A_ZERO(3) \
  "v_lshrrev_b32 v119, 2, %[ko]\n\tv_add_u32 v118, %[ob], %[ko]\n\tv_add_u32 v119, %[ob], v119\n\t"
#define A_GO "v_mov_b32 v116, 0xdddd\n\ts_setprio 1\n\ts_cmp_eq_u32 %[sw], 0\n\ts_cbranch_scc1 14f\n\ts_setprio 2\n\t14:\n\ts_nop 13\n\t" \
  A_EX(68, 0) A_EX(68, 1) "s_branch 11f\n\t"
#define A_W6 "s_waitcnt vmcnt(6)\n\t"
#define A_W3 "s_waitcnt vmcnt(3)\n\t"
#define A_DEC "s_sub_u32 %[n], %[n], 1\n\t"
#define A_ATT \
  A_SETUP "s_cmp_lg_u32 %[ex], 0\n\ts_cbranch_scc1 90f\n\t" \
  A_NEXT A_NEXT A_OFF(sa) A_NEXT A_OFF(sb) \
  "s_waitcnt vmcnt(7) lgkmcnt(0)\n\t" A_QK(68, BUF0, 0, "0") A_GO \
  "10:\n\t" \
  A_STEP(BUF0, BUF2, BUF1, A_SHIFT_NONE, "0", A_W6, "11:\n\t", A_DEC) \
  "s_cmp_eq_u32 %[n], 0\n\ts_cbranch_scc1 16f\n\t" \
  A_STEP(BUF1, BUF0, BUF2, A_SHIFT_NONE, "0", A_W6, "", "") \
  A_STEP(BUF2, BUF1, BUF0, A_SHIFT_NONE, "0", A_W6, "", "s_cmp_lg_u32 %[n], %[sw]\n\t") "s_cbranch_scc1 10b\n\ts_setprio 0\n\ts_branch 10b\n\t" \
  "16:\n\t" A_PV1(3, BUF0, 84) "s_branch 91f\n\t" \
  "90:\n\ts_mov_b32 %[n], 8\n\t" A_NEXT A_OFF(sa) A_NEXT A_OFF(sb) \
  "s_waitcnt vmcnt(0) lgkmcnt(0)\n\t" A_SHIFT_EXACT(0) A_QK(68, BUF0, 0, "v[100:115]") A_GO \
  "10:\n\t" \
  A_STEP(BUF0, BUF1, BUF1, A_SHIFT_EXACT, "v[100:115]", A_W3, "11:\n\t", "") \
  A_STEP(BUF1, BUF0, BUF0, A_SHIFT_EXACT, "v[100:115]", A_W3, "", A_DEC) "s_cmp_lg_u32 %[n], 0\n\ts_cbranch_scc1 10b\n\t" \
  A_PV1(3, BUF1, 84) \
  "91:\n\ts_setprio 0\n\tv_mov_b32 v117, %[pf]\n\ts_mov_b32 %[n], 0x100000\n\t" \
  "12:\n\tds_read_b32 v112, v117\n\ts_waitcnt lgkmcnt(0)\n\t" \
  "s_sub_u32 %[n], %[n], 1\n\ts_cbranch_scc1 13f\n\tv_cmp_gt_u32 vcc, 4, v112\n\ts_cbranch_vccnz 12b\n\t13:\n\ts_nop 7\n\t" \
  A_FLUSH(0) A_FLUSH(1) A_FLUSH(2) A_FLUSH(3)

#pragma clang diagnostic ignored "-Winline-asm"
__global__ __launch_bounds__(768, 3) void fused_kernel(
    const unsigned short* __restrict__ u, const unsigned short* __restrict__ Are, const unsigned short* __restrict__ Aim,
    const unsigned short* __restrict__ Bre, const unsigned short* __restrict__ Bim, const unsigned short* __restrict__ Cre,
    const unsigned short* __restrict__ Cim, const unsigned* __restrict__ logdt2, const unsigned* __restrict__ dskip2,
    const unsigned short* __restrict__ x, const unsigned short* __restrict__ wq, const unsigned short* __restrict__ wk,
    const unsigned short* __restrict__ wv, const unsigned short* __restrict__ wo, unsigned short* __restrict__ out,
    unsigned char* __restrict__ ws) {
  __shared__ __attribute__((aligned(128))) unsigned char smem[LDS_BYTES];
  unsigned long long pc;
  asm volatile("s_getpc_b64 %0" : "=s"(pc) :: "v119", "a47");
  const unsigned sb = (unsigned)(size_t)(LAS unsigned char*)smem;
  const int w = __builtin_amdgcn_readfirstlane(threadIdx.x >> 6);
  const int b = blockIdx.x, xcd = b & 7, bi = b >> 3;
  const int grp = xcd * 4 + (bi >> 3), hg = bi & 7, cp = xcd * 32 + bi;
#define LANE_VARS() const int lane = lane_id(), h = lane >> 5, g4 = lane >> 4, l15 = lane & 15, l31 = lane & 31; \
  const unsigned r8 = lane >> 3, c8 = lane & 7; (void)h; (void)g4; (void)l15; (void)l31; (void)r8; (void)c8
  STAMP_INIT();
  asm volatile("" :: "s"(u), "s"(Are), "s"(Aim), "s"(Bre), "s"(Bim), "s"(Cre), "s"(Cim), "s"(logdt2), "s"(dskip2), "s"(x), "s"(wq),
               "s"(wk), "s"(wv), "s"(wo), "s"(out), "s"(ws));

  if (w >= 8) {
    LANE_VARS();
    const int j = w - 8, tid = j * 64 + lane, tch = j >> 1, par = j & 1;
    unsigned pAr, pAi, pBr, pBi, pCr, pCi, ld2, d2, cpf = 0;
    const unsigned po = (unsigned)((2 * cp + tch) * 256 + lane * 4);
    {
      const unsigned uo = (64 * bi + 16 * j + r8) * 1024 + xcd * 128 + ((c8 ^ wswz(r8)) << 4);
      asm volatile("global_load_dwordx4 a[8:11], %0, %2\n\tglobal_load_dwordx4 a[12:15], %1, %2" :: "v"(uo), "v"((uo ^ 64) + 8192), "s"(u) : "memory", CLOB_A4_47);
    }
    GLD1(pAr, po, Are); GLD1(pAi, po, Aim); GLD1(pBr, po, Bre); GLD1(pBi, po, Bim); GLD1(pCr, po, Cre); GLD1(pCi, po, Cim);
    LD32(sb + L_M + l31 * 4) = 0;
    asm volatile("s_load_dword %0, %2, %4\n\ts_load_dword %1, %3, %4\n\ts_waitcnt lgkmcnt(0)\n\ts_barrier"
                 : "=&s"(ld2), "=&s"(d2) : "s"(logdt2), "s"(dskip2), "s"(cp * 4) : "memory");
#define PARWAIT "s_waitcnt vmcnt(0)"
    const float ldt = tch ? bfhi(ld2) : bflo(ld2);
    const float eh = ldt * 1.44269502162933349609f, el = fmaf(ldt, 1.44269502162933349609f, -eh) + ldt * 1.925963033500011e-8f;
    const float e0 = __builtin_amdgcn_exp2f(eh);
    const float dt = bflo(pkbf(fmaf(e0, el * 0.693147180559945f, e0), 0.f));
    asm volatile(PARWAIT : "+v"(pAr), "+v"(pAi), "+v"(pBr), "+v"(pBi), "+v"(pCr), "+v"(pCi), "+v"(cpf) :: "memory");
    STAMP(1);
    const float ar0 = dt * bflo(pAr), ar1 = dt * bfhi(pAr);
    float lam = 0.f;
    if (__any(fmaxf(ar0, ar1) > 0.f)) lam = __uint_as_float(wave_umax(__float_as_uint(fmaxf(fmaxf(ar0, ar1), 0.f))));
    const float ar = (par ? ar1 : ar0) - lam, ai = dt * (par ? bfhi(pAi) : bflo(pAi));
    unsigned T[21];
    {
      cf c0 = cmul({bflo(pCr), bflo(pCi)}, {bflo(pBr), bflo(pBi)});
      cf c1 = cmul({bfhi(pCr), bfhi(pCi)}, {bfhi(pBr), bfhi(pBi)});
      int ex = (int)(wave_umax(__float_as_uint(fmaxf(fmaxf(fabsf(c0.x), fabsf(c0.y)), fmaxf(fabsf(c1.x), fabsf(c1.y))))) >> 23);
      ex = ex < 8 ? 8 : (ex > 245 ? 245 : ex);
      const float sdn = __uint_as_float((unsigned)(252 - ex) << 23), sup = __uint_as_float((unsigned)(ex + 2) << 23) * dt;
      LD64(sb + L_M + M_SCALE + tch * 8) = (u32x2){__float_as_uint(sup), __float_as_uint(lam * 1.44269504088896f)};
      cf c = par ? c1 : c0; c.x *= sdn; c.y *= sdn;
      const cf w1 = cexp_(ar, ai), W = cexp_(256.f * ar, 256.f * ai);
      const cf w2 = cmul(w1, w1), w4 = cmul(w2, w2), w6 = cmul(w4, w2);
      cf v = c;
#pragma unroll
      for (int k = 0; k < 8; ++k) { T[k] = pkh(v.x, v.y); if (k < 7) v = cmul(v, w1); }
      v = cmul(c, W); T[8] = pkh(v.x, v.y);
      cf e = cmul(v, w2); T[9] = pkh(e.x, e.y);
      e = cmul(v, w4); T[10] = pkh(e.x, e.y);
      e = cmul(v, w6); T[11] = pkh(e.x, e.y);
      v = cmul(v, W); T[12] = pkh(v.x, v.y);
      e = cmul(v, w4); T[13] = pkh(e.x, e.y);
      v = cmul(v, W); T[14] = pkh(v.x, v.y);
      e = cmul(v, w4); T[15] = pkh(e.x, e.y);
#pragma unroll
      for (int k = 16; k < 21; ++k) { v = cmul(v, W); T[k] = pkh(v.x, v.y); }
    }
    {
      cf p[16];
      cf g = cexp_(8.f * ar, -8.f * ai);
      p[0] = {1.f, 0.f}; p[1] = g;
      g = cmul(g, g); p[2] = g; p[3] = cmul(p[1], g);
      g = cmul(g, g);
      p[4] = g;
#pragma unroll
      for (int i = 1; i < 4; ++i) p[4 + i] = cmul(p[i], g);
      g = cmul(g, g);
      p[8] = g;
#pragma unroll
      for (int i = 1; i < 8; ++i) p[8 + i] = cmul(p[i], g);
      g = cmul(g, g);
#pragma unroll
      for (int i = 0; i < 16; ++i) {
        const cf q = i ? cmul(p[i], g) : g;
        asm volatile("v_cvt_pk_f16_f32 v[84+%c4], %0, %1\n\tv_cvt_pk_f16_f32 v[100+%c4], %2, %3" :: "v"(p[i].x), "v"(p[i].y), "v"(q.x), "v"(q.y), "i"(i));
      }
    }
#pragma unroll
    for (int k = 0; k < 21; ++k) asm volatile("" : "+v"(T[k]));
    STAMP(2);
    STAMP(3);
    const unsigned mb = opaque(sb + L_M), one = opaque(1u), l16 = opaque(lane * 16);
    const TrAddr tra = transpose_addr(sb + L_TA0, sb + L_TB0 + j * 1024, j, lane);
    const unsigned img = opaque(sb + L_TA0 + j * 2048 + lane * 16);
    const unsigned tro = opaque((64 * xcd + 16 * j + r8) * TROW + bi * 128 + c8 * 16);
    const unsigned tri = opaque((64 * xcd + 16 * j + r8) * TROW + bi * 128 + ((c8 ^ wswz(r8)) << 4));
    const unsigned chr = opaque((2 * cp + tch) * TROW + par * 2048 + lane * 16);
    const unsigned c4 = (unsigned)(par * 64 + lane) * 4, tlo = opaque(sb + L_TLO + tch * TLO_CH + c4), thi = opaque(sb + L_THI + tch * THI_CH + c4);
    const unsigned uz = opaque(tid < 248 ? sb + L_U + (tid >= 124) * (U_CH - 31 * 80) + (tid >> 2) * 80 + (tid & 3) * 16 : sb + L_U + 64);
    const unsigned KT = sb + L_P;
    const unsigned kz = opaque(tid < 28 ? sb + L_KC + (tid >= 14) * (KC_CH - 224) + tid * 16 : KT);
    const unsigned aa = opaque(sb + L_TLO + tch * TLO_CH + (16 * par + l15) * 528 + g4 * 16);
    const unsigned b0 = opaque(sb + L_THI + tch * THI_CH + l15 * 528 + g4 * 16), b1 = opaque(sb + L_THI + tch * THI_CH + (l15 < 4 ? 16 + l15 : 20) * 528 + g4 * 16);
    const int lb = 128 * par + 8 * l15;
    const int l0 = lb + (g4 < 2 ? 4 * g4 : 256 * (g4 - 1)), s1 = g4 < 2 ? 1 : 2 * (g4 - 1), s2 = g4 < 3 ? 2 * s1 : 256, l1 = lb + 1024 + 1024 * g4;
    const unsigned ka0 = opaque(KT + l0 * 4 + 2 * tch), ka1 = opaque(ka0 + s1 * 4), ka2 = opaque(ka0 + s2 * 4), ka3 = opaque(ka2 + s1 * 4), ka4 = opaque(KT + l1 * 4 + 2 * tch);
    const float dsk = __uint_as_float(opaque(l0 == 0 ? (tch ? d2 & 0xffff0000u : d2 << 16) : 0u));
    const unsigned ktr = opaque(KT + tid * 16), kcw = opaque(sb + L_KC + 224 + tid * 32);
    const unsigned wo0 = opaque((128 * j + r8) * 1024 + hg * 128 + ((c8 ^ wswz(r8)) << 4)), wo1 = opaque((wo0 ^ 64) + 8192);
    const unsigned uw = opaque(sb + L_U + tch * U_CH + (31 + 32 * par + (lane >> 2)) * 80 + (lane & 3) * 16);
    unsigned aA = opaque(sb + L_KC + tch * KC_CH + (unsigned)(8 * (16 * (32 * par - 1) + l31 - 8 * h + 24)));
    unsigned aF = opaque(sb + L_U + tch * U_CH + (unsigned)(80 * (l31 + 31 - 16 * par) + 16 * h));
    const unsigned pa = opaque(sb + L_P + lane * 16 + j * 4096), pb = opaque(sb + L_P + lane * 16 + (j ^ 1) * 4096);
    const unsigned yw = opaque(sb + L_P + 16384 + j * 2048 + l31 * 64 + h * 8), yr = opaque(sb + L_P + 16384 + j * 2048 + lane * 16);
    const unsigned fl32 = opaque(l31 * 32), oo = opaque((64 * bi + 16 * j + r8) * 2048 + xcd * 128 + c8 * 16);
    u32x4 zero4 = {0u, 0u, 0u, 0u};
    asm volatile("" : "+v"(zero4));
    await_<M_AB + 12, 1>(mb, 1u);
    const unsigned tag = __builtin_amdgcn_readfirstlane(LD32(mb + M_TAG));
#define IMGWR "s_barrier\n\tds_write_b128 %[im], a[8:11]\n\tds_write_b128 %[im], a[12:15] offset:1024\n\t"
    if (j == 0) {
      const unsigned fo = W_F1 + (b & (NREP - 1)) * 2048 + l16;
      u32x4 f0, f1;
#define LOOK(T) asm volatile("global_load_dwordx4 %0, %[fo], %[ws] sc1\n\tglobal_load_dwordx4 %1, %[fo], %[ws] offset:1024 sc1\n\t" T "s_waitcnt vmcnt(0)" \
                     : "=&v"(f0), "=&v"(f1) : [fo] "v"(fo), [ws] "s"(ws), [im] "v"(img) : "memory", CLOB_A4_47)
#define SEEN() __all(((f0[1] ^ tag) | (f0[3] ^ tag) | (f1[1] ^ tag) | (f1[3] ^ tag)) == 0u)
      __builtin_amdgcn_s_sleep(WSLEEP);
      STAMP(13);
      LOOK(IMGWR);
      _Pragma("unroll 1") for (unsigned spins = 0; spins < kSpinCap; ++spins) {
        if (SEEN()) break;
        LOOK("");
      }
      LD32(mb + M_KMAX) = wave_umax(umax(umax(f0[0], f0[2]), umax(f1[0], f1[2])));
      if (b == 0 && lane == 0) __hip_atomic_store((gu32*)(ws + W_E), tag, RLX_AGENT);
    } else {
      STAMP(13);
      asm volatile(IMGWR :: [im] "v"(img) : "memory", CLOB_A4_47);
    }
    LD128(kz) = zero4;
#define TABLES_OUT() { _Pragma("unroll") for (int i = 0; i < 32; ++i) asm volatile("ds_write_b32 %0, v[84+%c1] offset:%c2" :: "v"(tlo), "i"(i), "i"(i * 528) : "memory"); \
      _Pragma("unroll") for (int i = 0; i < 21; ++i) LD32(thi + i * 528) = T[i]; }
    if (TWPOS == 1 && j) TABLES_OUT();
    STAMP(4);
    BAR();
    __builtin_amdgcn_s_setprio(SSM_PRIO);
    const __amdgpu_buffer_rsrc_t rsU = __builtin_amdgcn_make_buffer_rsrc(ws + W_UT, 0, 512 * TROW, 0x00020000);
    {
      u32x4 o0, o1;
      transpose_out<0>(tra, o0, o1);
      __builtin_amdgcn_raw_buffer_store_b128(o0, rsU, tro, 0, 16);
      asm volatile("buffer_store_dwordx4 %0, %1, %2, %3 offen sc1\n\ts_nop 1" :: "v"(o1), "v"(tro), "s"(make_srd(ws + W_UT, 512 * TROW)), "s"(8 * TROW) : "memory");
    }
    if (TWPOS == 0 || j == 0) TABLES_OUT();
    LD128(uz) = zero4;
    asm volatile("s_waitcnt vmcnt(0)" ::: "memory");
    SYNC4(0);
    if (j == 1 && lane == 0) __hip_atomic_store((gu32*)(ws + W_F3) + (xcd * 32 + bi) * 8, tag, RLX_AGENT);
    STAMP(5);
    {
      const u32x2 sc = LD64(mb + M_SCALE + tch * 8);
      f32x4 k0, k1;
#pragma unroll
      for (int ks = 0; ks < 8; ++ks) {
        const u32x4 fa = LD128(aa + ks * 64), fb = LD128(b0 + ks * 64), fc = LD128(b1 + ks * 64);
        if (ks == 0) asm volatile("v_mfma_f32_16x16x32_f16 %0, %3, %2, 0\n\tv_mfma_f32_16x16x32_f16 %1, %4, %2, 0" : "=&v"(k0), "=&v"(k1) : "v"(fa), "v"(fb), "v"(fc));
        else asm volatile("v_mfma_f32_16x16x32_f16 %0, %3, %2, %0\n\tv_mfma_f32_16x16x32_f16 %1, %4, %2, %1" : "+v"(k0), "+v"(k1) : "v"(fa), "v"(fb), "v"(fc));
      }
      asm volatile("s_nop 7" : "+v"(k0), "+v"(k1));
      k0 *= __uint_as_float(sc[0]); k1 *= __uint_as_float(sc[0]);
      if (__builtin_amdgcn_readfirstlane(sc[1])) {
        const unsigned kb = KT + 2 * tch;
        const unsigned la[4] = {ka0, ka1, ka2, ka3};
#pragma unroll
        for (int r = 0; r < 4; ++r) {
          k0[r] *= __builtin_amdgcn_exp2f(__uint_as_float(sc[1]) * (float)((la[r] - kb) >> 2));
          k1[r] *= __builtin_amdgcn_exp2f(__uint_as_float(sc[1]) * (float)(((ka4 - kb) >> 2) + 256 * r));
        }
      }
      k0[0] += dsk;
      const unsigned long long g23 = 0xffffffff00000000ull, g3 = 0xffff000000000000ull;
      const unsigned long long g23d = par ? g23 : g23 & ~(1ull << 32), g3d = par ? g3 : g3 & ~(1ull << 48);
      const unsigned long long a0 = par ? 0xffffull : 0x1ffffull, a0d = par ? 0xffffull : 0x1fffeull, e0 = par ? 0x5555ull : 0x15555ull, e0d = par ? 0x5555ull : 0x15554ull;
#define KW(R, o) "ds_write_b16" R " offset:" STR(o) "\n\t"
#define KR0 " %0, %5"
#define KR1 "_d16_hi %1, %5"
#define KR2 " %2, %6"
#define KR3 "_d16_hi %3, %6"
#define KS0 " %4, %7"
#define KS1 "_d16_hi %4, %7"
#define KS2 " %4, %8"
#define KS3 "_d16_hi %4, %8"
#define KW6(R, o) KW(R, o - 8) KW(R, o - 4) KW(R, o) KW(R, o + 4) KW(R, o + 8) KW(R, o + 12)
      asm volatile(KW(KR0, 16) KW(KR1, 16) KW(KR2, 16) KW(KR3, 16)
                   "s_mov_b64 exec, %9\n\t" KW(KR0, 20) KW(KR2, 20) KW(KR2, 12)
                   "s_mov_b64 exec, %10\n\t" KW(KR0, 12)
                   "s_mov_b64 exec, %11\n\t" KW(KR1, 20) KW(KR1, 12) KW(KR3, 20) KW(KR3, 12) KW(KR0, 24) KW(KR2, 24) KW(KR2, 8)
                   "s_mov_b64 exec, %12\n\t" KW(KR0, 8)
                   "s_mov_b64 exec, %13\n\t" KW6(KS0, 16)
                   "s_mov_b64 exec, %14\n\t" KW(KS0, 4)
                   "s_mov_b64 exec, %15\n\t" KW(KS0, 32)
                   "s_mov_b64 exec, %16\n\t" KW(KS0, 0)
                   "s_mov_b64 exec, 0xffff\n\t" KW6(KS1, 1040) KW(KS1, 1028) KW6(KS2, 2064) KW(KS2, 2052) KW6(KS3, 3088) KW(KS3, 3076)
                   "s_mov_b64 exec, 0x5555\n\t" KW(KS1, 1056) KW(KS1, 1024) KW(KS2, 2080) KW(KS2, 2048) KW(KS3, 3104) KW(KS3, 3072)
                   "s_mov_b64 exec, -1"
                   :: "v"(ka0), "v"(ka1), "v"(ka2), "v"(ka3), "v"(ka4), "v"(pkbf(k0[0], k0[1])), "v"(pkbf(k0[2], k0[3])),
                      "v"(pkbf(k1[0], k1[1])), "v"(pkbf(k1[2], k1[3])), "s"(g23), "s"(g23d), "s"(g3), "s"(g3d), "s"(a0), "s"(a0d), "s"(e0), "s"(e0d) : "memory");
    }
    SYNC4(1);
    if (j == 1) { wait_flags32<4>(ws + W_F3 + xcd * 1024, fl32, tag); arrive_<M_SW + 40>(mb, one); }
    else await_<M_SW + 40, 2>(mb, 1u);
    asm volatile("global_load_dwordx4 a[8:11], %0, %1 sc1\n\tglobal_load_dwordx4 a[12:15], %0, %1 offset:1024 sc1" :: "v"(chr), "s"(ws + W_UT) : "memory", CLOB_A4_47);
    if (WOPOS == 0) gdma_rows16(wo0, wo1, wo, sb + L_WO + j * 16384);
    STAMP(6);
#pragma unroll
    for (int k = 0; k < 2; ++k) {
      const u32x4 da = LD128(ktr + k * 4096), db = LD128(ktr + k * 4096 + 16);
      const unsigned d[7] = {da[1], da[2], da[3], db[0], db[1], db[2], db[3]};
#pragma unroll
      for (int c = 0; c < 2; ++c) {
        unsigned P[8];
#pragma unroll
        for (int i = 0; i < 8; ++i) {
          const int m = i < 6 ? i : i - 4;
          asm("v_perm_b32 %0, %1, %2, %3" : "=v"(P[i]) : "v"(d[m]), "v"(d[m + 1]), "s"(c ? 0x07060302u : 0x05040100u), "i"(i));
        }
        LD128(kcw + k * 8192 + c * KC_CH) = (u32x4){P[2], P[0], P[3], P[1]};
        LD128(kcw + k * 8192 + c * KC_CH + 16) = (u32x4){P[4], P[6], P[5], P[7]};
      }
    }
    STAMP(7);
    asm volatile(UWAIT "\n\tds_write_b128 %0, a[8:11]\n\tds_write_b128 %0, a[12:15] offset:1280" :: "v"(uw) : "memory", CLOB_A4_47);
    SYNC4(2);
    STAMP(8);
    f32x16 y0, y1;
    unsigned cl, wk_, wso_;
#define WOSTEP "s_bitcmp1_b32 %2, 0\n\ts_cbranch_scc1 2f\n\tbuffer_load_dwordx4 %[w0], %[rw], %[so] offen lds\n\ts_branch 3f\n\t" \
               "2:\n\tbuffer_load_dwordx4 %[w1], %[rw], %[so] offen lds\n\t3:\n\ts_add_u32 m0, m0, 0x400\n\ts_add_u32 %[so], %[so], 0x2000\n\t"
#define CLD(R0, R1, R2, R3, R4, R5, OA, OF) \
      "ds_read_b64 v[" #R0 "], %3 offset:" #OA "+32\n\tds_read_b64 v[" #R1 "], %3 offset:" #OA "\n\tds_read_b128 v[" #R4 "], %4 offset:" #OF "\n\t" \
      "ds_read_b128 v[" #R5 "], %4 offset:" #OF "+2560\n\tds_read_b64 v[" #R2 "], %3 offset:" #OA "+8224\n\tds_read_b64 v[" #R3 "], %3 offset:" #OA "+8192\n\t"
#define CMM(A0, A1, F0, F1) "s_waitcnt lgkmcnt(6)\n\tv_mfma_f32_32x32x16_bf16 %1, v[" #A0 "], v[" #F1 "], %1\n\t" \
      CPAD "v_mfma_f32_32x32x16_bf16 %0, v[" #A0 "], v[" #F0 "], %0\n\t" CPAD "v_mfma_f32_32x32x16_bf16 %1, v[" #A1 "], v[" #F0 "], %1\n\t" CPAD
    asm volatile(
        "s_setprio " STR(CONV_PRIO) "\n\ts_nop 1\n\tv_mfma_f32_32x32x8_bf16 %0, %[z], %[z], 0\n\tv_mfma_f32_32x32x8_bf16 %1, %[z], %[z], 0\n\ts_mov_b32 %2, " STR(CONV_N) "\n\t"
        "s_mov_b32 %[k], m0\n\ts_mov_b32 m0, %[wd]\n\ts_mov_b32 %[so], 0\n\t"
        CLD(84:85, 86:87, 88:89, 90:91, 92:95, 96:99, 0, 32)
        "1:\n\t"
        CLD(100:101, 102:103, 104:105, 106:107, 108:111, 112:115, 128, 0)
        CMM(84:87, 88:91, 92:95, 96:99)
        "v_add_u32 %3, 0x100, %3\n\tv_add_u32 %4, 0xffffffb0, %4\n\t" WOSTEP
        CLD(84:85, 86:87, 88:89, 90:91, 92:95, 96:99, 0, 32)
        CMM(100:103, 104:107, 108:111, 112:115)
        "s_sub_u32 %2, %2, 1\n\ts_cmp_lg_u32 %2, 0\n\ts_cbranch_scc1 1b\n\t"
        "s_mov_b32 m0, %[k]\n\ts_setprio " STR(SSM_PRIO) "\n\ts_waitcnt lgkmcnt(0)\n\ts_nop 15"
        : "=&v"(y0), "=&v"(y1), "=&s"(cl), "+v"(aA), "+v"(aF), [k] "=&s"(wk_), [so] "=&s"(wso_)
        : [z] "v"(__builtin_shufflevector(zero4, zero4, 0, 1)), [w0] "v"(wo0), [w1] "v"(wo1 - 8192), [rw] "s"(make_srd(wo, 524288)), [wd] "s"(sb + L_WO + j * 16384)
        : "memory", "scc");
#define YSEND(ys) _Pragma("unroll") for (int q = 0; q < 4; ++q) LDF4(pa + q * 1024) = (f32x4){ys[4 * q], ys[4 * q + 1], ys[4 * q + 2], ys[4 * q + 3]}
    if (par) { YSEND(y0); asm volatile("" ::: "memory"); } else { YSEND(y1); }
    STAMP(9);
    SYNC4(3);
    STAMP(12);
    {
#define YKEEP(yk) _Pragma("unroll") for (int q = 0; q < 4; ++q) { \
        const f32x4 v = LDF4(pb + q * 1024) + (f32x4){yk[4 * q], yk[4 * q + 1], yk[4 * q + 2], yk[4 * q + 3]}; \
        LD64(yw + q * 16) = (u32x2){pkbf(v[0], v[1]), pkbf(v[2], v[3])}; }
      if (par) { YKEEP(y1); asm volatile("" ::: "memory"); } else { YKEEP(y0); }
      const u32x4 r0 = LD128(yr), r1 = LD128(yr + 1024);
      asm volatile("" ::: "memory");
      arrive_<M_SW + 28>(mb, one);
      const __amdgpu_buffer_rsrc_t rs = __builtin_amdgcn_make_buffer_rsrc(ws + W_YT, 0, 512 * TROW, 0x00020000);
      __builtin_amdgcn_raw_buffer_store_b128(r0, rs, chr, 0, 16);
      __builtin_amdgcn_raw_buffer_store_b128(r1, rs, chr + 1024, 0, 16);
      STAMP(14);
      asm volatile("s_waitcnt vmcnt(0)" ::: "memory");
      STAMP(15);
      SYNC4(4);
      if (j == 0 && lane == 0) __hip_atomic_store((gu32*)(ws + W_F4) + (xcd * 32 + bi) * 8, tag, RLX_AGENT);
      STAMP(10);
      if (j == 0) { __builtin_amdgcn_s_sleep(16); wait_flags32<FSLEEP>(ws + W_F4 + xcd * 1024, fl32, tag); arrive_<M_SW + 44>(mb, one); }
      else await_<M_SW + 44, 3>(mb, 1u);
      STAMP(16);
      asm volatile("buffer_load_dwordx4 a[8:11], %0, %2, 0 offen sc1\n\tbuffer_load_dwordx4 a[12:15], %1, %2, %3 offen sc1\n\ts_waitcnt vmcnt(0)\n\t"
                   "ds_write_b128 %4, a[8:11] offset:%c5\n\tds_write_b128 %4, a[12:15] offset:%c5+1024"
                   :: "v"(tri), "v"(tri ^ 64), "s"(make_srd(ws + W_YT, 512 * TROW)), "s"(8 * TROW), "v"(img), "i"(L_U - L_TA0) : "memory", CLOB_A4_47);
      STAMP(17);
      SYNC4(9);
      STAMP(18);
      u32x4 o0, o1;
      transpose_out<L_U - L_TA0>(tra, o0, o1);
      asm volatile("buffer_store_dwordx4 %0, %2, %3, 0 offen sc1\n\tbuffer_store_dwordx4 %1, %2, %3, %4 offen sc1\n\ts_nop 1"
                   :: "v"(o0), "v"(o1), "v"(oo), "s"(make_srd(out, 4194304)), "s"(16384) : "memory");
    }
    STAMP(11);
    STAMP_OUT();
    return;
  }

  LANE_VARS();
  const int qq = w & 3, kh = w >> 2, chain = hg >> 2;
  const bool crit = w == (hg & 3);
  const unsigned short* wkv = chain ? wv : wk;
  const bool helper = WHELP && w == (hg & 3) + 4;
  if (helper) {
#pragma unroll
    for (int i = 0; i < 4; ++i) gdma4((unsigned)(i * 4096 + lane * 16), wkv, sb + L_R2 + i * 4096);
  }
  if (!crit) asm volatile("s_barrier" ::: "memory");
  else __builtin_amdgcn_s_setprio(3);
  if (kh == 0) {
    const unsigned xo = (64 * grp + 16 * qq + r8) * 1024 + ((c8 ^ (r8 >> 1)) << 4);
    gdma_x(xo, (xo ^ 64) + 8192, x, sb + L_X + qq * 16384);
    gdma_x(xo + 512, (xo ^ 64) + 8704, x, sb + L_X + qq * 16384 + 8192);
  } else {
    const unsigned wq0 = (64 * qq + r8) * 1024 + hg * 128 + ((c8 ^ ((r8 & 2) << 1)) << 4);
    gdma_rows8(wq0, wq0 + 8192, wq, sb + L_WQ + qq * 8192);
    gdma_rows8(wq0 + 262144, wq0 + 270336, wq, sb + L_WQ + 32768 + qq * 8192);
  }
  if (helper) {
    asm volatile("s_waitcnt vmcnt(16)" ::: "memory");
    arrive(sb + L_M + M_HA + 8);
  }
  u32x2 fword;
  unsigned hook = crit ? 2 : kh ^ 1;
  if (!crit) asm volatile("" : "=v"(fword));
  if (crit) {
    if (!WHELP) {
#pragma unroll
      for (int i = 0; i < 4; ++i) gdma4((unsigned)(i * 4096 + lane * 16), wkv, sb + L_R2 + i * 4096);
    }
    asm volatile("s_barrier" ::: "memory");
    unsigned E;
    asm volatile("s_load_dword %0, %1, 0x0\n\ts_waitcnt lgkmcnt(0)" : "=s"(E) : "s"(ws) : "memory");
    const unsigned tg = E + (((E * 0x9E3779B1u ^ (E >> 15) ^ 0x5bd1e995u) & 0x7fffffffu) | 0x101u);
    LD32(sb + L_M + M_TAG) = tg;
    const unsigned wa = sb + L_R2 + (8 * g4 + (l15 >> 2)) * 32 + (lane & 3) * 8;
    const unsigned xe = sb + L_X + qq * 16384 + (l15 >> 3) * 4096 + (l15 & 7) * 128 + ((g4 ^ (l15 >> 1)) << 4);
    const unsigned tb = (2 * grp + (qq >> 1)) * KV_TILE;
    const unsigned off = chain ? ((g4 & 1) == 0 ? tb + 1024 + (qq & 1) * 640 + (g4 >> 1) * 256 + l15 * 16 : (lane >> 3) == 2 ? tb + 1536 + (qq & 1) * 640 + c8 * 16 : 0x80000000u)
                               : (g4 < 2 ? tb + (qq & 1) * 256 + g4 * 512 + l15 * 16 : 0x80000000u);
    f32x4 acc, acd;
#define KR(s, o) "v[36+8*(" #s "%%6)+" #o ":36+8*(" #s "%%6)+" #o "+3]"
#define K_LD(s, X) "ds_read_b128 " KR(s, 0) ", " X " offset:8192*(" #s "/8)+1024*((" #s "&7)/2)\n\t" \
      "ds_read_b64_tr_b16 v[36+8*(" #s "%%6)+4:36+8*(" #s "%%6)+5], %4 offset:1024*" #s "\n\tds_read_b64_tr_b16 v[36+8*(" #s "%%6)+6:36+8*(" #s "%%6)+7], %4 offset:1024*" #s "+128\n\t"
#define K_LD2(s, t) K_LD(s, "%2") K_LD(t, "%3")
#define K_MM(N, s, t, A, B, C0, C1) "s_waitcnt lgkmcnt(" #N ")\n\tv_mfma_f32_16x16x32_bf16 %0, " KR(s, A) ", " KR(s, B) ", " C0 "\n\t" \
      "v_mfma_f32_16x16x32_bf16 %1, " KR(t, A) ", " KR(t, B) ", " C1 "\n\t"
#define K_ALL(A, B) asm volatile("s_waitcnt lgkmcnt(0)\n\t" K_LD2(0, 1) K_LD2(2, 3) K_LD2(4, 5) K_MM(12, 0, 1, A, B, "0", "0") K_LD2(6, 7) K_MM(12, 2, 3, A, B, "%0", "%1") \
        K_LD2(8, 9) K_MM(12, 4, 5, A, B, "%0", "%1") K_LD2(10, 11) K_MM(12, 6, 7, A, B, "%0", "%1") K_LD2(12, 13) K_MM(12, 8, 9, A, B, "%0", "%1") \
        K_LD2(14, 15) K_MM(12, 10, 11, A, B, "%0", "%1") K_MM(6, 12, 13, A, B, "%0", "%1") K_MM(0, 14, 15, A, B, "%0", "%1") "s_nop 7" \
        : "=&v"(acc), "=&v"(acd) : "v"(xe), "v"(xe ^ 64), "v"(wa) : "memory", CLOB_V36_83)
    asm volatile("s_waitcnt vmcnt(0)" ::: "memory");
    arrive(sb + L_M + M_HA); arrive(sb + L_M + M_HA + 4);
    if (WHELP) await<0>(sb + L_M + M_HA + 8, 1u);
    if (chain) K_ALL(0, 4); else K_ALL(4, 0);
    STAMP(1);
    acc = (acc + acd) * (chain ? 1.0f : 0.25f * 1.44269504088896f);
    const u32x2 pk = {pkbf(acc[0], acc[1]), pkbf(acc[2], acc[3])};
    const __amdgpu_buffer_rsrc_t rs = __builtin_amdgcn_make_buffer_rsrc(ws + W_KV, 0, KV_BYTES, 0x00020000);
    u32x4 pv;
    unsigned kn2 = 0;
    if (chain) {
      auto x0 = __builtin_amdgcn_permlane16_swap(pk[0], pk[0], false, false);
      auto x1 = __builtin_amdgcn_permlane16_swap(pk[1], pk[1], false, false);
      pv = (g4 & 1) ? (u32x4){0x3f803f80u, 0x3f803f80u, 0x3f803f80u, 0x3f803f80u} : (u32x4){x0[0], x1[0], x0[1], x1[1]};
    } else {
      auto x0 = __builtin_amdgcn_permlane32_swap(pk[0], pk[0], false, false);
      auto x1 = __builtin_amdgcn_permlane32_swap(pk[1], pk[1], false, false);
      pv = (u32x4){x0[0], x1[0], x0[1], x1[1]};
    }
    __builtin_amdgcn_raw_buffer_store_b128(pv, rs, off, 0, 16);
    if (!chain) {
      const float n2 = sq2(pk[0], sq2(pk[1], 0.f));
      kn2 = row_umax(__float_as_uint(sum32(sum16(n2, n2))));
    }
    fword = (u32x2){kn2, tg};
    _Pragma("unroll 1") for (unsigned spins = 0; spins < kSpinCap; ++spins) {
      unsigned v, st;
      asm volatile("ds_read_b32 %0, %2\n\ts_waitcnt lgkmcnt(0)\n\ts_getreg_b32 %1, hwreg(HW_REG_IB_STS, 0, 4)" : "=v"(v), "=s"(st) : "v"(sb + L_M + M_HA) : "memory");
      if (EARLYPUB && st == 0) {
        asm volatile("s_waitcnt vmcnt(0)" ::: "memory");
        if (lane < NREP) __hip_atomic_store((gu64*)(ws + W_F1) + lane * 256 + bi * 8 + xcd, ((unsigned long long)tg << 32) | kn2, RLX_AGENT);
        arrive(sb + L_M + M_AB + 12);
        hook = 0;
        break;
      }
      if (__builtin_amdgcn_readfirstlane(v) >= 8u) break;
    }
    STAMP(2);
  } else {
    asm volatile("s_waitcnt vmcnt(8)" ::: "memory");
    arrive(sb + L_M + M_HA);
    if (kh) { asm volatile("s_waitcnt vmcnt(0)" ::: "memory"); arrive(sb + L_M + M_HA + 4); }
  }
  STAMP(3);
  await<0>(sb + L_M + M_HA + 4 * kh, 8u);
  STAMP(4);

  f32x16 qa16;
  {
    const unsigned A0 = sb + L_WQ + (256 * kh + 8 * h + (l15 >> 2)) * 128 + (((4 * (qq & 1) + 2 * ((g4 & 1) ^ kh) + ((lane & 3) >> 1)) ^ ((l15 >> 3) << 2)) << 4) + (lane & 1) * 8;
    const unsigned X0 = sb + L_X + ((qq & 2) + (g4 & 1)) * 16384 + kh * 8192 + (l15 >> 3) * 4096 + (l15 & 7) * 128 + ((h ^ (l15 >> 1)) << 4);
#define Q_LD(s, X) "ds_read_b128 " KR(s, 0) ", " X " offset:1024*(" #s "/4)\n\t" \
      "ds_read_b64_tr_b16 v[36+8*(" #s "%%6)+4:36+8*(" #s "%%6)+5], %1 offset:2048*" #s "\n\tds_read_b64_tr_b16 v[36+8*(" #s "%%6)+6:36+8*(" #s "%%6)+7], %1 offset:2048*" #s "+512\n\t"
#define Q_MM(N, s, C) "s_waitcnt lgkmcnt(" #N ")\n\tv_mfma_f32_32x32x16_bf16 %0, " KR(s, 4) ", " KR(s, 0) ", " C "\n\t"
    asm volatile(Q_LD(0, "%2") Q_LD(1, "%3") Q_LD(2, "%4") Q_LD(3, "%5") Q_LD(4, "%2") Q_MM(12, 0, "0") Q_LD(5, "%3") Q_MM(12, 1, "%0") Q_LD(6, "%4") Q_MM(12, 2, "%0")
                 Q_LD(7, "%5") Q_MM(12, 3, "%0") Q_LD(8, "%2") Q_MM(12, 4, "%0")
                 "s_cmp_eq_u32 %6, 0\n\ts_cbranch_scc1 1f\n\ts_waitcnt vmcnt(0)\n\ts_cmp_eq_u32 %6, 1\n\ts_cbranch_scc1 2f\n\t"
                 "s_mov_b64 exec, " STR((1 << NREP) - 1) "\n\tglobal_store_dwordx2 %9, %10, %11 sc1\n\t"
                 "2:\n\ts_mov_b64 exec, 1\n\tds_add_u32 %7, %8\n\ts_mov_b64 exec, -1\n\t1:\n\t"
                 Q_LD(9, "%3") Q_MM(12, 5, "%0") Q_LD(10, "%4") Q_MM(12, 6, "%0") Q_LD(11, "%5") Q_MM(12, 7, "%0")
                 Q_LD(12, "%2") Q_MM(12, 8, "%0") Q_LD(13, "%3") Q_MM(12, 9, "%0") Q_LD(14, "%4") Q_MM(12, 10, "%0") Q_LD(15, "%5") Q_MM(12, 11, "%0")
                 Q_MM(9, 12, "%0") Q_MM(6, 13, "%0") Q_MM(3, 14, "%0") Q_MM(0, 15, "%0") "s_setprio 0\n\ts_nop 10"
                 : "=&v"(qa16) : "v"(A0), "v"(X0), "v"(X0 ^ 32), "v"(X0 ^ 64), "v"(X0 ^ 96),
                   "s"(__builtin_amdgcn_readfirstlane(opaque(hook))), "v"(sb + L_M + (crit ? M_AB + 12 : M_HA + 4)), "v"(1u),
                   "v"(W_F1 + lane * 2048 + (bi * 8 + xcd) * 8), "v"(fword), "s"(ws) : "memory", "scc", CLOB_V36_83);
  }
  STAMP(5);
  const unsigned xa = opaque(sb + L_R2 + lane * 16 + w * 2048);
  LDF4(xa) = __builtin_shufflevector(qa16, qa16, 8, 9, 10, 11);
  LDF4(xa + 1024) = __builtin_shufflevector(qa16, qa16, 12, 13, 14, 15);
  BAR();
  STAMP(6);
  const i32x4 rsKV = make_srd(ws + W_KV, KV_BYTES);
  const unsigned ko = lane * 16, vo = l31 < 16 ? 1024 + h * 256 + l31 * 16 : l31 == 16 ? 1536u : 0x80000000u;
  const unsigned t0 = (16 * qq + 2 * bi) & 63;
  const unsigned tag = __builtin_amdgcn_readfirstlane(LD32(sb + L_M + M_TAG));
#define KV_FIRST() { unsigned tl_ = t0, sa_, sb_; asm volatile(A_FIRST : [tl] "+s"(tl_), [sa] "=&s"(sa_), [sb] "=&s"(sb_) \
    : [ko] "v"(ko), [vo] "v"(vo), [rk] "s"(rsKV) : "memory", "scc", CLOB_A4_47); }
  {
    const f32x4 q0 = __builtin_shufflevector(qa16, qa16, 0, 1, 2, 3) + LDF4(xa ^ 8192), q1 = __builtin_shufflevector(qa16, qa16, 4, 5, 6, 7) + LDF4((xa ^ 8192) + 1024);
    const u32x4 o = {pkbf(q0[0], q0[1]), pkbf(q0[2], q0[3]), pkbf(q1[0], q1[1]), pkbf(q1[2], q1[3])};
    LD128(sb + L_Q + (2 * qq + kh) * 1024 + lane * 16) = o;
    const unsigned n2 = row_umax(__float_as_uint(sum32(sq2(o[0], sq2(o[1], sq2(o[2], sq2(o[3], 0.f)))))));
    asm volatile("s_mov_b64 exec, 0x10001\n\tds_max_u32 %0, %1\n\ts_mov_b64 exec, -1" :: "v"(sb + L_M + M_QMAX), "v"(n2) : "memory");
  }
  STAMP(7);
  BAR();
  STAMP(8);
  RSTAMP(18);
  KV_FIRST();

  const u32x2 qk2 = LD64(sb + L_M + M_QMAX);
  const float bound = fmaf(__builtin_amdgcn_sqrtf(__uint_as_float(qk2[0]) * __uint_as_float(qk2[1])), 1.002f, 1e-3f);
  const unsigned exact = __builtin_amdgcn_readfirstlane(opaque(bound <= 32.0f ? 0u : 1u));
  {
    unsigned tl = t0, n = 6, sa, sbo, sw = kh * PSW;
    const unsigned qb = sb + L_Q + kh * 4096, ob = sb + L_OA + w * 4 * OA_SLOT;
    if (exact) {
      const unsigned maddr = sb + L_MX + kh * 512 + l31 * 4;
      if (w < 4) LDF(sb + L_MX + (w * 64 + lane) * 4) = -3.0e38f;
      arrive(sb + L_M + M_AB + 4);
      await<0>(sb + L_M + M_AB + 4, 8u);
      float m[4] = {-3.0e38f, -3.0e38f, -3.0e38f, -3.0e38f};
      const __amdgpu_buffer_rsrc_t rs = __builtin_amdgcn_make_buffer_rsrc(ws + W_KV, 0, KV_BYTES, 0x00020000);
#pragma unroll 1
      for (unsigned i = 0; i < 16; ++i) {
        u32x4 kf = __builtin_amdgcn_raw_buffer_load_b128(rs, ko, ((tl + i) & 63) * KV_TILE, 16);
#pragma unroll
        for (int t = 0; t < 4; ++t) {
          const u32x4 q = LD128(qb + ko + t * 1024);
          f32x16 s_;
          asm volatile("s_nop 1\n\tv_mfma_f32_32x32x16_bf16 %0, %1, %2, 0\n\ts_nop 15" : "=&v"(s_) : "v"(kf), "v"(q) : CLOB_A4_47);
#pragma unroll
          for (int r = 0; r < 16; ++r) m[t] = fmaxf(m[t], s_[r]);
        }
      }
#pragma unroll
      for (int t = 0; t < 4; ++t) __builtin_amdgcn_ds_fmaxf((LAS float*)(size_t)(maddr + t * 128), m[t], __ATOMIC_RELAXED, __HIP_MEMORY_SCOPE_WORKGROUP, false);
      arrive(sb + L_M + M_AB + 8);
      await<0>(sb + L_M + M_AB + 8, 8u);
      asm volatile("v_accvgpr_write_b32 a44, %0\n\tv_accvgpr_write_b32 a45, %1\n\tv_accvgpr_write_b32 a46, %2\n\tv_accvgpr_write_b32 a47, %3"
                   :: "v"(-LDF(maddr)), "v"(-LDF(maddr + 128)), "v"(-LDF(maddr + 256)), "v"(-LDF(maddr + 384)) : CLOB_A4_47);
    }
    asm volatile(A_ATT
                 : [tl] "+s"(tl), [n] "+s"(n), [sa] "=&s"(sa), [sb] "=&s"(sbo), [sw] "+s"(sw)
                 : [ko] "v"(ko), [vo] "v"(vo), [rk] "s"(rsKV), [qb] "s"(qb), [ob] "s"(ob), [pf] "s"(sb + L_M + M_SW + 28), [ex] "s"(exact)
                 : "memory", "scc", "vcc", CLOB_V4_83, CLOB_A4_47);
  }
  STAMP(9);
  RSTAMP(17);
  BAR();
  STAMP(10);

  {
    LANE_VARS();
    {
      const unsigned s0 = opaque(sb + L_OA + (16 * kh + qq) * OA_SLOT + lane * 16), s1 = opaque(sb + L_OA + (16 * kh + qq) * OA_SLOT + 2048 + lane * 4);
      constexpr unsigned W1 = 4 * OA_SLOT;
      f32x4 o0 = (LDF4(s0) + LDF4(s0 + W1)) + (LDF4(s0 + 2 * W1) + LDF4(s0 + 3 * W1));
      f32x4 o1 = (LDF4(s0 + 1024) + LDF4(s0 + W1 + 1024)) + (LDF4(s0 + 2 * W1 + 1024) + LDF4(s0 + 3 * W1 + 1024));
      const float inv = __builtin_amdgcn_rcpf(sum32((LDF(s1) + LDF(s1 + W1)) + (LDF(s1 + 2 * W1) + LDF(s1 + 3 * W1))));
      o0 *= inv; o1 *= inv;
      u32x4 pk = {pkbf(o0[0], o0[1]), pkbf(o0[2], o0[3]), pkbf(o1[0], o1[1]), pkbf(o1[2], o1[3])};
      const __amdgpu_buffer_rsrc_t rsO = __builtin_amdgcn_make_buffer_rsrc(ws + W_OX, 0, 2097152, 0x00020000);
      __builtin_amdgcn_raw_buffer_store_b128(pk, rsO, lane * 16 + ((2 * grp + kh) * 32 + 4 * hg + qq) * 1024, 0, 16);
    }
    STAMP(11);
    RSTAMP(13);
    const unsigned r0 = 256 * kh + 16 * (g4 >> 1) + 4 * (g4 & 1) + (l15 >> 2);
    const unsigned A0 = sb + L_WO + r0 * 128 + (((4 * kh + ((lane & 3) >> 1)) ^ wswz(r0)) << 4) + (lane & 1) * 8;
    const unsigned oo = (g4 >> 1) * 1024 + ((g4 & 1) * 32 + 16 * (qq & 1) + l15) * 16 + ((2 * grp + (qq >> 1)) * 32 + 16 * kh) * 1024;
    const unsigned xa = lane * 16;
    const unsigned xw = sb + L_OA + (2 * qq + kh) * 4352 + l15 * 272 + kh * 128 + g4 * 16;
    const unsigned char* f2 = ws + W_F2 + grp * 1024;
    unsigned t0, t1;
#define T_WF1(F, B, s, e, P, Q) "ds_read_b64_tr_b16 " F "[" #B "+4*" #e ":" #B "+4*" #e "+1], " P " offset:4096*" #s "\n\t" \
                                "ds_read_b64_tr_b16 " F "[" #B "+4*" #e "+2:" #B "+4*" #e "+3], " Q " offset:4096*" #s "+1024\n\t"
#define T_WF(F, B, s) T_WF1(F, B, s, 0, "%[a0]", "v6") T_WF1(F, B, s, 1, "v5", "v7") T_WF1(F, B, s, 2, "v6", "%[a0]") T_WF1(F, B, s, 3, "v7", "v5")
#define T_M1(F, B, s, e, C) "v_mfma_f32_16x16x32_bf16 v[104+4*" #e ":107+4*" #e "], " F "[" #B "+4*" #e ":" #B "+4*" #e "+3], v[72+4*" #s ":75+4*" #s "], " C "\n\t"
#define T_MA(e) "v[104+4*" #e ":107+4*" #e "]"
#define T_MM(F, B, s, N) "s_waitcnt vmcnt(" #N ")\n\t" T_M1(F, B, s, 0, T_MA(0)) T_M1(F, B, s, 1, T_MA(1)) T_M1(F, B, s, 2, T_MA(2)) T_M1(F, B, s, 3, T_MA(3))
#define OPF "v_and_b32 v72, 0x3f0, %[xa]\n\tbuffer_load_dwordx4 v[108:111], v72, %[ro], %[own] offen sc1\n\t"
#define T_OL(s, so, imm) "buffer_load_dwordx4 v[72+4*" #s ":75+4*" #s "], %[oo], %[ro], " so " offen offset:" #imm " sc1\n\t"
    asm volatile(
        "v_xor_b32 v5, 32, %[a0]\n\tv_xor_b32 v6, 64, %[a0]\n\tv_xor_b32 v7, 0x60, %[a0]\n\t"
        T_WF("v", 8, 0) T_WF("v", 24, 1) T_WF("v", 40, 2)
        "s_waitcnt vmcnt(0)\n\t" OPF "s_barrier\n\t"
        TS(80) "s_cmp_lg_u32 %[w], 0\n\ts_cbranch_scc1 1f\n\t"
        "v_mov_b32 v72, %[fo]\n\tv_mov_b32 v73, %[tg]\n\ts_mov_b64 exec, 1\n\tglobal_store_dword v72, v73, %[f2] sc1\n\ts_mov_b64 exec, -1\n\t"
        "1:\n\t"
        T_WF("v", 56, 3) T_WF("a", 8, 4) T_WF("a", 24, 5)
        "s_cbranch_scc1 3f\n\t"
        "v_and_b32 v4, 0x70, %[xa]\n\tv_lshlrev_b32 v4, 3, v4\n\ts_mov_b32 %[t0], 0x100000\n\ts_sleep " STR(TSLEEP) "\n\t"
        "2:\n\tglobal_load_dword v104, v4, %[f2] sc1\n\ts_waitcnt vmcnt(0)\n\tv_cmp_ne_u32 vcc, %[tg], v104\n\ts_cbranch_vccz 3f\n\t"
        "s_sub_u32 %[t0], %[t0], 1\n\ts_cbranch_scc0 2b\n\t"
        "3:\n\ts_barrier\n\t" TS(82) "s_movk_i32 %[t0], 0x1000\n\ts_movk_i32 %[t1], 0x2000\n\t"
        T_OL(0, "0", 0) T_OL(1, "0", 2048) T_OL(2, "%[t0]", 0) T_OL(3, "%[t0]", 2048) "s_movk_i32 %[t0], 0x3000\n\t"
        T_OL(4, "%[t1]", 0) T_OL(5, "%[t1]", 2048) T_OL(6, "%[t0]", 0) T_OL(7, "%[t0]", 2048)
        "s_waitcnt lgkmcnt(0) vmcnt(7)\n\t" T_M1("v", 8, 0, 0, "0") T_M1("v", 8, 0, 1, "0") T_M1("v", 8, 0, 2, "0") T_M1("v", 8, 0, 3, "0")
        T_WF("v", 8, 6) T_MM("v", 24, 1, 6) T_WF("v", 24, 7) T_MM("v", 40, 2, 5) T_MM("v", 56, 3, 4) T_MM("a", 8, 4, 3) T_MM("a", 24, 5, 2)
        "s_waitcnt lgkmcnt(0)\n\t" T_MM("v", 8, 6, 1) T_MM("v", 24, 7, 0)
        TS(84) "v_add_u32 v6, %[dk], %[xw]\n\ts_nop 6\n\t"
        "ds_write_b128 %[xw], v[104:107]\n\tds_write_b128 %[xw], v[108:111] offset:64\n\tds_write_b128 v6, v[112:115]\n\tds_write_b128 v6, v[116:119] offset:64\n\t"
        "v_lshrrev_b32 v4, 7, %[xa]\n\tv_mul_u32_u24 v4, 272, v4\n\tv_and_b32 v5, 0x70, %[xa]\n\tv_lshl_add_u32 v4, v5, 1, v4\n\tv_add_u32 v4, %[xq], v4\n\t"
        "s_waitcnt lgkmcnt(0)\n\ts_barrier\n\t"
        "ds_read_b128 v[72:75], v4\n\tds_read_b128 v[80:83], v4 offset:4352\n\tds_read_b128 v[76:79], v4 offset:16\n\tds_read_b128 v[84:87], v4 offset:4368\n\t"
        "s_waitcnt lgkmcnt(2)\n\t"
        "v_add_f32 v72, v72, v80\n\tv_add_f32 v73, v73, v81\n\tv_add_f32 v74, v74, v82\n\tv_add_f32 v75, v75, v83\n\ts_waitcnt lgkmcnt(0)\n\t"
        "v_add_f32 v76, v76, v84\n\tv_add_f32 v77, v77, v85\n\tv_add_f32 v78, v78, v86\n\tv_add_f32 v79, v79, v87\n\t"
        "v_cvt_pk_bf16_f32 v80, v72, v73\n\tv_cvt_pk_bf16_f32 v81, v74, v75\n\tv_cvt_pk_bf16_f32 v82, v76, v77\n\tv_cvt_pk_bf16_f32 v83, v78, v79\n\t"
        "v_lshl_add_u32 v4, %[xa], 4, %[go]\n\tv_and_b32 v5, 0x70, %[xa]\n\tv_and_b32 v4, 0xfffff800, v4\n\tv_add3_u32 v4, v4, v5, %[gc]\n\t"
        "global_store_dwordx4 v4, v[80:83], %[out] " OAUX "\n\ts_nop 1"
        : [t0] "=&s"(t0), [t1] "=&s"(t1)
        : [a0] "v"(A0), [oo] "v"(oo), [xa] "v"(xa), [xw] "v"(xw), [w] "s"(w), [go] "s"((64 * grp + 16 * qq + 8 * kh) * 2048), [gc] "s"(1024 + 128 * hg), [dk] "s"(128 - 256 * kh),
          [xq] "s"(sb + L_OA + 2 * qq * 4352 + 8 * kh * 272), [fo] "s"(hg * 128), [own] "s"(((2 * grp + kh) * 32 + 4 * hg + qq) * 1024), [tg] "s"(tag), [f2] "s"(f2),
          [ro] "s"(make_srd(ws + W_OX, 2097152)), [out] "s"(out)
        : "memory", "scc", "vcc", CLOB_V4_83, CLOB_A4_47 TSCLOB);
    TSTAMP_OUT();
    STAMP(12);
    STAMP_OUT();
  }
}

extern "C" void kernel_launch(void* const* d_in, const int* in_sizes, int n_in, void* d_out, int out_size, void* d_ws,
                              size_t ws_size, hipStream_t stream) {
  typedef const unsigned short* P;
  fused_kernel<<<256, 768, 0, stream>>>((P)d_in[0], (P)d_in[1], (P)d_in[2], (P)d_in[3], (P)d_in[4], (P)d_in[5], (P)d_in[6],
                                        (const unsigned*)d_in[7], (const unsigned*)d_in[8], (P)d_in[9], (P)d_in[10], (P)d_in[11],
                                        (P)d_in[12], (P)d_in[13], (unsigned short*)d_out, (unsigned char*)d_ws);
}
```

```cpp
#include <hip/hip_runtime.h>
#include <hip/hip_bf16.h>
#include <stdint.h>

typedef float f32x16 __attribute__((ext_vector_type(16)));
typedef float f32x8 __attribute__((ext_vector_type(8)));
typedef float f32x4 __attribute__((ext_vector_type(4)));
typedef float f32x2 __attribute__((ext_vector_type(2)));
typedef __bf16 bf16x8 __attribute__((ext_vector_type(8)));
typedef __bf16 bf16x2 __attribute__((ext_vector_type(2)));
typedef _Float16 f16x8 __attribute__((ext_vector_type(8)));
typedef _Float16 f16x2 __attribute__((ext_vector_type(2)));
typedef short s16x4 __attribute__((ext_vector_type(4)));
typedef unsigned u32x4 __attribute__((ext_vector_type(4)));
typedef unsigned u32x2 __attribute__((ext_vector_type(2)));
#define LAS __attribute__((address_space(3)))
#define GAS __attribute__((address_space(1)))
typedef GAS unsigned gu32;
typedef GAS unsigned long long gu64;
#define RLX_AGENT __ATOMIC_RELAXED, __HIP_MEMORY_SCOPE_AGENT

constexpr unsigned L_X = 0;
constexpr unsigned L_TLO = 0, TLO_CH = 16896, L_THI = 33792, THI_CH = 11088;
constexpr unsigned L_WO = 0;
constexpr unsigned L_B = 65536;
constexpr unsigned L_WQ = L_B;
constexpr unsigned L_R2 = L_B + 65536;
constexpr unsigned L_TA0 = L_B + 53248, L_TB0 = L_TA0 + 8192;
constexpr unsigned L_KC = L_B, KC_CH = 16640;
constexpr unsigned L_OA = L_B, OA_SLOT = 2304;
constexpr unsigned L_P = L_B + 40960;
constexpr unsigned L_U = L_B + 73728, U_CH = 4608;
constexpr unsigned L_Q = L_B + 88928;
constexpr unsigned L_MX = L_Q + 8192;
constexpr unsigned L_M = L_MX + 1024;
constexpr unsigned LDS_BYTES = L_M + 128;
static_assert(LDS_BYTES <= 163840, "lds");
constexpr unsigned M_TAG = 0, M_RDY = 4, M_QMAX = 8, M_KMAX = 12, M_SCALE = 16  , M_SW = 32  , M_HA = 96  , M_AB = 112  ;
#define CONV_PRIO 3
#define CONV_N 4
#define CNOP 5
#define CPAD "s_nop " STR(CNOP) "\n\t"
#define PSW 2
#define NREP 8
#define WSLEEP 10
#define FSLEEP 6
#define YPRIO 3
#define TSLEEP 8
#define YAUX 16
#define OAUX "sc1"
#define EARLYPUB 1
#define WHELP 0
#define PDELAY -1
#define TWPOS 0
#define SSM_PRIO 3

constexpr unsigned KV_TILE = 2304, KV_BYTES = 64 * KV_TILE;
static_assert(NREP <= 8, "flag copies");
#define TROW_ 4224
constexpr unsigned TROW = TROW_;
constexpr unsigned W_E = 0, W_F1 = 8192  , W_F2 = W_F1 + 16384  , W_F3 = W_F2 + 32768  ,
                   W_F4 = W_F3 + 8192, W_KV = W_F1 + 65536, W_OX = W_KV + KV_BYTES,
                   W_UT = W_OX + 2097152, W_YT = W_UT + 512 * TROW, W_END = W_YT + 512 * TROW;
constexpr unsigned W_DBG = W_END;

constexpr int CODE_LINES = 150;
constexpr unsigned kSpinCap = 1u << 20;

#define LD32(a) (*(LAS unsigned*)(size_t)(a))
#define LDF(a) (*(LAS float*)(size_t)(a))
#define LD64(a) (*(LAS u32x2*)(size_t)(a))
#define LD128(a) (*(LAS u32x4*)(size_t)(a))
#define LDF4(a) (*(LAS f32x4*)(size_t)(a))
#define BAR() asm volatile("s_waitcnt lgkmcnt(0)\n\ts_barrier" ::: "memory")

#define RSTAMP(i)
#define TS(k)
#define TSTAMP_OUT()
#define TSCLOB
#define STAMP(i)
#define STAMP_INIT()
#define STAMP_OUT()

__device__ __forceinline__ float bflo(unsigned v) { return __uint_as_float(v << 16); }
__device__ __forceinline__ float bfhi(unsigned v) { return __uint_as_float(v & 0xffff0000u); }
__device__ __forceinline__ unsigned pkbf(float a, float b) { f32x2 x = {a, b}; return __builtin_bit_cast(unsigned, __builtin_convertvector(x, bf16x2)); }
__device__ __forceinline__ unsigned pkh(float a, float b) { f32x2 x = {a, b}; return __builtin_bit_cast(unsigned, __builtin_convertvector(x, f16x2)); }
template <int CTRL> __device__ __forceinline__ unsigned dppu(unsigned v) { return __builtin_amdgcn_update_dpp(0u, v, CTRL, 0xf, 0xf, true); }
__device__ __forceinline__ unsigned umax(unsigned a, unsigned b) { return a > b ? a : b; }
__device__ __forceinline__ unsigned row_umax(unsigned v) {
  v = umax(v, dppu<0x128>(v)); v = umax(v, dppu<0x124>(v)); v = umax(v, dppu<0x122>(v)); return umax(v, dppu<0x121>(v));
}
__device__ __forceinline__ unsigned wave_umax(unsigned v) {
  v = row_umax(v);
  auto a = __builtin_amdgcn_permlane16_swap(v, v, false, false);
  v = umax(a[0], a[1]);
  auto c = __builtin_amdgcn_permlane32_swap(v, v, false, false);
  return umax(c[0], c[1]);
}
__device__ __forceinline__ float sum16(float a, float b) {
  auto r = __builtin_amdgcn_permlane16_swap(__float_as_uint(a), __float_as_uint(b), false, false);
  return __uint_as_float(r[0]) + __uint_as_float(r[1]);
}
__device__ __forceinline__ float sum32(float v) {
  auto c = __builtin_amdgcn_permlane32_swap(__float_as_uint(v), __float_as_uint(v), false, false);
  return __uint_as_float(c[0]) + __uint_as_float(c[1]);
}
__device__ __forceinline__ float sq2(unsigned v, float c) {
  return __builtin_amdgcn_fdot2_f32_bf16(__builtin_bit_cast(bf16x2, v), __builtin_bit_cast(bf16x2, v), c, false);
}
__device__ __forceinline__ unsigned opaque(unsigned v) { asm volatile("" : "+v"(v)); return v; }
__device__ __forceinline__ int lane_id() {
  int l;
  asm volatile("v_mbcnt_lo_u32_b32 %0, -1, 0\n\tv_mbcnt_hi_u32_b32 %0, -1, %0" : "=v"(l));
  __builtin_assume(l >= 0 && l < 64);
  return l;
}
struct cf { float x, y; };
__device__ __forceinline__ cf cmul(cf a, cf b) { return {a.x * b.x - a.y * b.y, a.x * b.y + a.y * b.x}; }
__device__ __forceinline__ cf cexp_(float re, float im) {
  float m = __builtin_amdgcn_exp2f(re * 1.44269504088896f);
  float f = __builtin_amdgcn_fractf(im * 0.159154943091895f);
  return {m * __builtin_amdgcn_cosf(f), m * __builtin_amdgcn_sinf(f)};
}
#define GLD1(dst, voff, base) asm volatile("global_load_dword %0, %1, %2" : "=v"(dst) : "v"(voff), "s"(base))
typedef int i32x4 __attribute__((ext_vector_type(4)));
__device__ __forceinline__ i32x4 make_srd(const void* p, unsigned bytes) {
  const unsigned long long a = (unsigned long long)p;
  return (i32x4){(int)(unsigned)a, (int)((unsigned)(a >> 32) & 0xffffu), (int)bytes, 0x00020000};
}

__device__ __forceinline__ void gdma4(unsigned voff, const void* base, unsigned lds_dst) {
  unsigned keep;
  asm volatile("s_mov_b32 %0, m0\n\ts_mov_b32 m0, %3\n\ts_nop 0\n\tglobal_load_lds_dwordx4 %1, %2\n\tglobal_load_lds_dwordx4 %1, %2 offset:1024\n\t"
               "global_load_lds_dwordx4 %1, %2 offset:2048\n\tglobal_load_lds_dwordx4 %1, %2 offset:3072\n\ts_mov_b32 m0, %0"
               : "=&s"(keep) : "v"(voff), "s"(base), "s"(lds_dst) : "memory");
}
#define DMA_PAIR "buffer_load_dwordx4 %[v0], %[rs], %[so] offen lds\n\ts_add_u32 m0, m0, 0x400\n\ts_nop 0\n\t" \
                 "buffer_load_dwordx4 %[v1], %[rs], %[so] offen lds\n\ts_add_u32 m0, m0, 0x400\n\ts_add_u32 %[so], %[so], 0x4000\n\t"
#define DMA_X(v, o) "global_load_lds_dwordx4 " v ", %1 offset:" #o "\n\ts_add_u32 m0, m0, "
__device__ __forceinline__ void gdma_x(unsigned v0, unsigned v1, const void* base, unsigned lds_dst) {
  unsigned keep;
  asm volatile("s_mov_b32 %0, m0\n\ts_mov_b32 m0, %4\n\ts_nop 0\n\t"
               DMA_X("%2", 0) "896\n\ts_nop 0\n\t" DMA_X("%2", 128) "896\n\ts_nop 0\n\t" DMA_X("%2", 256) "896\n\ts_nop 0\n\t" DMA_X("%2", 384) "1408\n\ts_nop 0\n\t"
               DMA_X("%3", 0) "896\n\ts_nop 0\n\t" DMA_X("%3", 128) "896\n\ts_nop 0\n\t" DMA_X("%3", 256) "896\n\ts_nop 0\n\t" DMA_X("%3", 384) "0\n\t"
               "s_mov_b32 m0, %0"
               : "=&s"(keep) : "s"(base), "v"(v0), "v"(v1), "s"(lds_dst) : "memory", "scc");
}
__device__ __forceinline__ void gdma_rows8(unsigned v0, unsigned v1, const void* base, unsigned lds_dst) {
  unsigned keep, so;
  asm volatile("s_mov_b32 %[k], m0\n\ts_mov_b32 m0, %[d]\n\ts_mov_b32 %[so], 0\n\t" DMA_PAIR DMA_PAIR DMA_PAIR DMA_PAIR "s_mov_b32 m0, %[k]"
               : [k] "=&s"(keep), [so] "=&s"(so) : [rs] "s"(make_srd(base, 524288)), [v0] "v"(v0), [v1] "v"(v1), [d] "s"(lds_dst) : "memory", "scc");
}
__device__ __forceinline__ unsigned wswz(unsigned row) { return ((((row >> 1) & 3u) ^ (((row >> 3) & 1u) << 1)) << 1); }

__device__ __forceinline__ void arrive(unsigned addr) {
  asm volatile("s_mov_b64 exec, 1\n\tds_add_u32 %0, %1\n\ts_mov_b64 exec, -1" :: "v"(addr), "v"(1u) : "memory");
}
template <int SLEEP>
__device__ __forceinline__ void await(unsigned addr, unsigned count) {
  _Pragma("unroll 1") for (unsigned spins = 0; spins < kSpinCap; ++spins) {
    unsigned v;
    asm volatile("ds_read_b32 %0, %1\n\ts_waitcnt lgkmcnt(0)" : "=v"(v) : "v"(addr) : "memory");
    if (__builtin_amdgcn_readfirstlane(v) >= count) break;
    if (SLEEP) __builtin_amdgcn_s_sleep(SLEEP);
  }
}
template <int OFF>
__device__ __forceinline__ void arrive_(unsigned mb, unsigned one) {
  asm volatile("s_mov_b64 exec, 1\n\tds_add_u32 %0, %1 offset:%c2\n\ts_mov_b64 exec, -1" :: "v"(mb), "v"(one), "i"(OFF) : "memory");
}
template <int OFF, int SLEEP>
__device__ __forceinline__ void await_(unsigned mb, unsigned count) {
  _Pragma("unroll 1") for (unsigned spins = 0; spins < kSpinCap; ++spins) {
    unsigned v;
    asm volatile("ds_read_b32 %0, %1 offset:%c2\n\ts_waitcnt lgkmcnt(0)" : "=v"(v) : "v"(mb), "i"(OFF) : "memory");
    if (__builtin_amdgcn_readfirstlane(v) >= count) break;
    __builtin_amdgcn_s_sleep(SLEEP);
  }
}
#define SYNC4(k) { arrive_<M_SW + 4 * (k)>(mb, one); await_<M_SW + 4 * (k), 2>(mb, 4u); }
template <int SLEEP>
__device__ __forceinline__ void wait_flags32(const unsigned char* base, unsigned voff, unsigned tag) {
  _Pragma("unroll 1") for (unsigned spins = 0; spins < kSpinCap; ++spins) {
    unsigned f;
    asm volatile("global_load_dword %0, %1, %2 sc1\n\ts_waitcnt vmcnt(0)" : "=v"(f) : "v"(voff), "s"(base) : "memory");
    if (__all(f == tag)) break;
    __builtin_amdgcn_s_sleep(SLEEP);
  }
  __builtin_amdgcn_fence(__ATOMIC_ACQUIRE, "wavefront");
}
struct TrAddr { unsigned ra, rx, wa, wx, rb; };
__device__ __forceinline__ TrAddr transpose_addr(unsigned img, unsigned scr, int j, int lane) {
  const unsigned g = lane >> 4, i = lane & 15, row = 8 * g + (i >> 2);
  const unsigned ra = img + row * 128 + (((2 * j + ((lane & 3) >> 1)) ^ wswz(row)) << 4) + (lane & 1) * 8;
  const unsigned wa = scr + (i & 7) * 128 + ((g ^ (i & 7)) << 4), rb = scr + (lane >> 3) * 128 + (((lane & 7) ^ (lane >> 3)) << 4);
  return {opaque(ra), opaque(ra ^ 64), opaque(wa), opaque(wa ^ 64), opaque(rb)};
}
template <int OFF>
__device__ __forceinline__ void transpose_out(const TrAddr& t, u32x4& o0, u32x4& o1) {
  u32x2 t0, t1, t2, t3;
  asm volatile("ds_read_b64_tr_b16 %0, %4 offset:%c6\n\tds_read_b64_tr_b16 %1, %5 offset:%c6+512\n\tds_read_b64_tr_b16 %2, %4 offset:%c6+4096\n\t"
               "ds_read_b64_tr_b16 %3, %5 offset:%c6+4608\n\ts_waitcnt lgkmcnt(0)"
               : "=&v"(t0), "=&v"(t1), "=&v"(t2), "=&v"(t3) : "v"(t.ra), "v"(t.rx), "i"(OFF) : "memory");
  const u32x4 c0 = {t0[0], t0[1], t1[0], t1[1]}, c1 = {t2[0], t2[1], t3[0], t3[1]};
  asm volatile("s_mov_b64 exec, %6\n\tds_write_b128 %2, %4 offset:%c8\n\tds_write_b128 %3, %5 offset:%c8\n\ts_mov_b64 exec, -1\n\tds_read_b128 %0, %7 offset:%c8\n\t"
               "s_not_b64 exec, %6\n\tds_write_b128 %2, %4 offset:%c8\n\tds_write_b128 %3, %5 offset:%c8\n\ts_mov_b64 exec, -1\n\tds_read_b128 %1, %7 offset:%c8\n\ts_waitcnt lgkmcnt(0)"
               : "=&v"(o0), "=&v"(o1) : "v"(t.wa), "v"(t.wx), "v"(c0), "v"(c1), "s"(0x00ff00ff00ff00ffull), "v"(t.rb), "i"(OFF) : "memory", "scc");
}

#define STR_(x) #x
#define STR(x) STR_(x)
#define VR(b, o) "v[" STR(b) "+" STR(o) "]"
#define VRR(b, o, n) "v[" STR(b) "+" STR(o) ":" STR(b) "+" STR(o) "+" STR(n) "-1]"
#define AR4(i) "a[" STR(i) ":" STR(i) "+3]"
#define C4(p, a, b, c, d) p #a, p #b, p #c, p #d
#define C8(p, a, b, c, d, e, f, g, h) p #a, p #b, p #c, p #d, p #e, p #f, p #g, p #h
#define CLOB_V36_83 C4("v", 36, 37, 38, 39), C8("v", 40, 41, 42, 43, 44, 45, 46, 47), C8("v", 48, 49, 50, 51, 52, 53, 54, 55), C8("v", 56, 57, 58, 59, 60, 61, 62, 63), \
  C8("v", 64, 65, 66, 67, 68, 69, 70, 71), C8("v", 72, 73, 74, 75, 76, 77, 78, 79), C4("v", 80, 81, 82, 83)
#define CLOB_V4_83 C4("v", 4, 5, 6, 7), C8("v", 8, 9, 10, 11, 12, 13, 14, 15), C8("v", 16, 17, 18, 19, 20, 21, 22, 23), C8("v", 24, 25, 26, 27, 28, 29, 30, 31), \
  C4("v", 32, 33, 34, 35), CLOB_V36_83
#define CLOB_A4_47 C4("a", 4, 5, 6, 7), C8("a", 8, 9, 10, 11, 12, 13, 14, 15), C8("a", 16, 17, 18, 19, 20, 21, 22, 23), C8("a", 24, 25, 26, 27, 28, 29, 30, 31), \
  C8("a", 32, 33, 34, 35, 36, 37, 38, 39), C8("a", 40, 41, 42, 43, 44, 45, 46, 47)
#define BUF0(o) "a[4+" #o ":7+" #o "]"
#define BUF1(o) "a[16+" #o ":19+" #o "]"
#define BUF2(o) "v[100+" #o ":103+" #o "]"
#define A_EX(S, i) "v_exp_f32 " VR(S, i) ", " VR(S, i) "\n\t"
#define A_QK(S, B, q, C) "v_mfma_f32_32x32x16_bf16 " VRR(S, 0, 16) ", " B(0) ", " AR4(28 + 4 * q) ", " C "\n\t"
#define A_PV0(o, B, S) "v_smfmac_f32_32x32x32_bf16 " VRR(4, 16 * o, 16) ", " B(4) ", " VRR(S, 0, 8) ", v116\n\t"
#define A_PV1(o, B, S) "v_smfmac_f32_32x32x32_bf16 " VRR(4, 16 * o, 16) ", " B(8) ", " VRR(S, 8, 8) ", v116\n\t"
#define A_RD4(o, q) "v_accvgpr_read_b32 " VR(100, o) ", a[44+" #q "]\n\tv_accvgpr_read_b32 " VR(101, o) ", a[44+" #q "]\n\t" \
                    "v_accvgpr_read_b32 " VR(102, o) ", a[44+" #q "]\n\tv_accvgpr_read_b32 " VR(103, o) ", a[44+" #q "]\n\t"
#define A_SHIFT_EXACT(q) A_RD4(0, q) A_RD4(4, q) A_RD4(8, q) A_RD4(12, q) "s_nop 1\n\t"
#define A_SHIFT_NONE(q) ""
#define A_BLOCK(Sc, Sp, op, Bp, Bn, qn, oc, Bc, SH, C, XM, X3, X5, X7, X10, X12) \
  A_EX(Sc, 0) A_EX(Sc, 1) A_PV1(op, Bp, Sp) XM A_EX(Sc, 2) A_EX(Sc, 3) X3 A_EX(Sc, 4) A_EX(Sc, 5) X5 SH(qn) A_QK(Sp, Bn, qn, C) \
  A_EX(Sc, 6) A_EX(Sc, 7) X7 A_EX(Sc, 8) A_EX(Sc, 9) A_EX(Sc, 10) X10 A_PV0(oc, Bc, Sc) \
  A_EX(Sc, 11) A_EX(Sc, 12) X12 A_EX(Sc, 13) A_EX(Sc, 14) A_EX(Sc, 15)
#define A_LK(B, so) "buffer_load_dwordx4 " B(0) ", %[ko], %[rk], " so " offen sc1\n\t"
#define A_LV0(B, so) "buffer_load_dwordx4 " B(4) ", %[vo], %[rk], " so " offen sc1\n\t"
#define A_LV1(B, so) "buffer_load_dwordx4 " B(8) ", %[vo], %[rk], " so " offen offset:640 sc1\n\t"
#define A_NEXT "s_add_u32 %[tl], %[tl], 1\n\ts_and_b32 %[tl], %[tl], 63\n\t"
#define A_OFF(r) "s_mul_i32 %[" #r "], %[tl], 2304\n\t"
#define A_STEP(B, Bp, Bn, SH, C, W, XM, E) \
  A_BLOCK(68, 84, 3, Bp, B, 1, 0, B, SH, C, XM, A_LV1(Bp, "%[sa]"), "", "", W, "") \
  A_BLOCK(84, 68, 0, B, B, 2, 1, B, SH, C, "", "s_mov_b32 %[sa], %[sb]\n\t", "", A_NEXT, "", "") \
  A_BLOCK(68, 84, 1, B, B, 3, 2, B, SH, C, "", "", "", A_LK(B, "%[sb]"), "", "") \
  A_BLOCK(84, 68, 2, B, Bn, 0, 3, B, SH, C, "", "", W, E, "", A_LV0(B, "%[sb]") A_OFF(sb))
#define A_ZERO(o) "v_mfma_f32_32x32x8_bf16 " VRR(4, 16 * o, 16) ", v[116:117], v[116:117], 0\n\t"
#define A_FLUSH(o) "ds_write_b128 v118, " VRR(4, 16 * o, 4) " offset:2304*" #o "\n\tds_write_b128 v118, " VRR(8, 16 * o, 4) " offset:2304*" #o "+1024\n\t" \
                   "ds_write_b32 v119, " VR(12, 16 * o) " offset:2304*" #o "+2048\n\t"
#define A_FIRST \
  A_OFF(sb) A_NEXT A_OFF(sa) A_LK(BUF0, "%[sb]") A_LV0(BUF0, "%[sb]") A_LV1(BUF0, "%[sb]") A_NEXT A_OFF(sb) \
  A_LK(BUF1, "%[sa]") A_LV0(BUF1, "%[sa]") A_LV1(BUF1, "%[sa]") A_LK(BUF2, "%[sb]") A_LV0(BUF2, "%[sb]")
#define A_SETUP \
  "v_add_u32 v118, %[qb], %[ko]\n\tv_mov_b32 v116, 0\n\tv_mov_b32 v117, 0\n\t" \
  "ds_read_b128 a[28:31], v118\n\tds_read_b128 a[32:35], v118 offset:1024\n\tds_read_b128 a[36:39], v118 offset:2048\n\tds_read_b128 a[40:43], v118 offset:3072\n\t" \
  A_ZERO(0) A_ZERO(1) A_ZERO(2) A_ZERO(3) \
  "v_lshrrev_b32 v119, 2, %[ko]\n\tv_add_u32 v118, %[ob], %[ko]\n\tv_add_u32 v119, %[ob], v119\n\t"
#define A_GO "v_mov_b32 v116, 0xdddd\n\ts_setprio 1\n\ts_cmp_eq_u32 %[sw], 0\n\ts_cbranch_scc1 14f\n\ts_setprio 2\n\t14:\n\ts_nop 13\n\t" \
  A_EX(68, 0) A_EX(68, 1) "s_branch 11f\n\t"
#define A_W6 "s_waitcnt vmcnt(6)\n\t"
#define A_W3 "s_waitcnt vmcnt(3)\n\t"
#define A_DEC "s_sub_u32 %[n], %[n], 1\n\t"
#define A_ATT \
  A_SETUP "s_cmp_lg_u32 %[ex], 0\n\ts_cbranch_scc1 90f\n\t" \
  A_NEXT A_NEXT A_OFF(sa) A_NEXT A_OFF(sb) \
  "s_waitcnt vmcnt(7) lgkmcnt(0)\n\t" A_QK(68, BUF0, 0, "0") A_GO \
  "10:\n\t" \
  A_STEP(BUF0, BUF2, BUF1, A_SHIFT_NONE, "0", A_W6, "11:\n\t", A_DEC) \
  "s_cmp_eq_u32 %[n], 0\n\ts_cbranch_scc1 16f\n\t" \
  A_STEP(BUF1, BUF0, BUF2, A_SHIFT_NONE, "0", A_W6, "", "") \
  A_STEP(BUF2, BUF1, BUF0, A_SHIFT_NONE, "0", A_W6, "", "s_cmp_lg_u32 %[n], %[sw]\n\t") "s_cbranch_scc1 10b\n\ts_setprio 0\n\ts_branch 10b\n\t" \
  "16:\n\t" A_PV1(3, BUF0, 84) "s_branch 91f\n\t" \
  "90:\n\ts_mov_b32 %[n], 8\n\t" A_NEXT A_OFF(sa) A_NEXT A_OFF(sb) \
  "s_waitcnt vmcnt(0) lgkmcnt(0)\n\t" A_SHIFT_EXACT(0) A_QK(68, BUF0, 0, "v[100:115]") A_GO \
  "10:\n\t" \
  A_STEP(BUF0, BUF1, BUF1, A_SHIFT_EXACT, "v[100:115]", A_W3, "11:\n\t", "") \
  A_STEP(BUF1, BUF0, BUF0, A_SHIFT_EXACT, "v[100:115]", A_W3, "", A_DEC) "s_cmp_lg_u32 %[n], 0\n\ts_cbranch_scc1 10b\n\t" \
  A_PV1(3, BUF1, 84) \
  "91:\n\ts_setprio 0\n\tv_mov_b32 v117, %[pf]\n\ts_mov_b32 %[n], 0x100000\n\t" \
  "12:\n\tds_read_b32 v112, v117\n\ts_waitcnt lgkmcnt(0)\n\t" \
  "s_sub_u32 %[n], %[n], 1\n\ts_cbranch_scc1 13f\n\tv_cmp_gt_u32 vcc, 4, v112\n\ts_cbranch_vccnz 12b\n\t13:\n\ts_nop 7\n\t" \
  A_FLUSH(0) A_FLUSH(1) A_FLUSH(2) A_FLUSH(3)

#pragma clang diagnostic ignored "-Winline-asm"
__global__ __launch_bounds__(768, 3) void fused_kernel(
    const unsigned short* __restrict__ u, const unsigned short* __restrict__ Are, const unsigned short* __restrict__ Aim,
    const unsigned short* __restrict__ Bre, const unsigned short* __restrict__ Bim, const unsigned short* __restrict__ Cre,
    const unsigned short* __restrict__ Cim, const unsigned* __restrict__ logdt2, const unsigned* __restrict__ dskip2,
    const unsigned short* __restrict__ x, const unsigned short* __restrict__ wq, const unsigned short* __restrict__ wk,
    const unsigned short* __restrict__ wv, const unsigned short* __restrict__ wo, unsigned short* __restrict__ out,
    unsigned char* __restrict__ ws) {
  __shared__ __attribute__((aligned(128))) unsigned char smem[LDS_BYTES];
  unsigned long long pc;
  asm volatile("s_getpc_b64 %0" : "=s"(pc) :: "v119", "a47");
  const unsigned sb = (unsigned)(size_t)(LAS unsigned char*)smem;
  const int w = __builtin_amdgcn_readfirstlane(threadIdx.x >> 6);
  const int b = blockIdx.x, xcd = b & 7, bi = b >> 3;
  const int grp = xcd * 4 + (bi >> 3), hg = bi & 7, cp = xcd * 32 + bi;
#define LANE_VARS() const int lane = lane_id(), h = lane >> 5, g4 = lane >> 4, l15 = lane & 15, l31 = lane & 31; \
  const unsigned r8 = lane >> 3, c8 = lane & 7; (void)h; (void)g4; (void)l15; (void)l31; (void)r8; (void)c8
  STAMP_INIT();
  asm volatile("" :: "s"(u), "s"(Are), "s"(Aim), "s"(Bre), "s"(Bim), "s"(Cre), "s"(Cim), "s"(logdt2), "s"(dskip2), "s"(x), "s"(wq),
               "s"(wk), "s"(wv), "s"(wo), "s"(out), "s"(ws));

  if (w >= 8) {
    LANE_VARS();
    const int j = w - 8, tid = j * 64 + lane, tch = j >> 1, par = j & 1;
    unsigned pAr, pAi, pBr, pBi, pCr, pCi, ld2, d2, cpf = 0;
    const unsigned po = (unsigned)((2 * cp + tch) * 256 + lane * 4);
    {
      const unsigned uo = (64 * bi + 16 * j + r8) * 1024 + xcd * 128 + ((c8 ^ wswz(r8)) << 4);
      asm volatile("global_load_dwordx4 a[8:11], %0, %2\n\tglobal_load_dwordx4 a[12:15], %1, %2" :: "v"(uo), "v"((uo ^ 64) + 8192), "s"(u) : "memory", CLOB_A4_47);
    }
    GLD1(pAr, po, Are); GLD1(pAi, po, Aim); GLD1(pBr, po, Bre); GLD1(pBi, po, Bim); GLD1(pCr, po, Cre); GLD1(pCi, po, Cim);
    LD32(sb + L_M + l31 * 4) = 0;
    asm volatile("s_load_dword %0, %2, %4\n\ts_load_dword %1, %3, %4\n\ts_waitcnt lgkmcnt(0)\n\ts_barrier"
                 : "=&s"(ld2), "=&s"(d2) : "s"(logdt2), "s"(dskip2), "s"(cp * 4) : "memory");
#define PARWAIT "s_waitcnt vmcnt(0)"
    const float ldt = tch ? bfhi(ld2) : bflo(ld2);
    const float eh = ldt * 1.44269502162933349609f, el = fmaf(ldt, 1.44269502162933349609f, -eh) + ldt * 1.925963033500011e-8f;
    const float e0 = __builtin_amdgcn_exp2f(eh);
    const float dt = bflo(pkbf(fmaf(e0, el * 0.693147180559945f, e0), 0.f));
    asm volatile(PARWAIT : "+v"(pAr), "+v"(pAi), "+v"(pBr), "+v"(pBi), "+v"(pCr), "+v"(pCi), "+v"(cpf) :: "memory");
    STAMP(1);
    const float ar0 = dt * bflo(pAr), ar1 = dt * bfhi(pAr);
    float lam = 0.f;
    if (__any(fmaxf(ar0, ar1) > 0.f)) lam = __uint_as_float(wave_umax(__float_as_uint(fmaxf(fmaxf(ar0, ar1), 0.f))));
    const float ar = (par ? ar1 : ar0) - lam, ai = dt * (par ? bfhi(pAi) : bflo(pAi));
    unsigned T[21];
    {
      cf c0 = cmul({bflo(pCr), bflo(pCi)}, {bflo(pBr), bflo(pBi)});
      cf c1 = cmul({bfhi(pCr), bfhi(pCi)}, {bfhi(pBr), bfhi(pBi)});
      int ex = (int)(wave_umax(__float_as_uint(fmaxf(fmaxf(fabsf(c0.x), fabsf(c0.y)), fmaxf(fabsf(c1.x), fabsf(c1.y))))) >> 23);
      ex = ex < 8 ? 8 : (ex > 245 ? 245 : ex);
      const float sdn = __uint_as_float((unsigned)(252 - ex) << 23), sup = __uint_as_float((unsigned)(ex + 2) << 23) * dt;
      LD64(sb + L_M + M_SCALE + tch * 8) = (u32x2){__float_as_uint(sup), __float_as_uint(lam * 1.44269504088896f)};
      cf c = par ? c1 : c0; c.x *= sdn; c.y *= sdn;
      const cf w1 = cexp_(ar, ai), W = cexp_(256.f * ar, 256.f * ai);
      const cf w2 = cmul(w1, w1), w4 = cmul(w2, w2), w6 = cmul(w4, w2);
      cf v = c;
#pragma unroll
      for (int k = 0; k < 8; ++k) { T[k] = pkh(v.x, v.y); if (k < 7) v = cmul(v, w1); }
      v = cmul(c, W); T[8] = pkh(v.x, v.y);
      cf e = cmul(v, w2); T[9] = pkh(e.x, e.y);
      e = cmul(v, w4); T[10] = pkh(e.x, e.y);
      e = cmul(v, w6); T[11] = pkh(e.x, e.y);
      v = cmul(v, W); T[12] = pkh(v.x, v.y);
      e = cmul(v, w4); T[13] = pkh(e.x, e.y);
      v = cmul(v, W); T[14] = pkh(v.x, v.y);
      e = cmul(v, w4); T[15] = pkh(e.x, e.y);
#pragma unroll
      for (int k = 16; k < 21; ++k) { v = cmul(v, W); T[k] = pkh(v.x, v.y); }
    }
    {
      cf p[16];
      cf g = cexp_(8.f * ar, -8.f * ai);
      p[0] = {1.f, 0.f}; p[1] = g;
      g = cmul(g, g); p[2] = g; p[3] = cmul(p[1], g);
      g = cmul(g, g);
      p[4] = g;
#pragma unroll
      for (int i = 1; i < 4; ++i) p[4 + i] = cmul(p[i], g);
      g = cmul(g, g);
      p[8] = g;
#pragma unroll
      for (int i = 1; i < 8; ++i) p[8 + i] = cmul(p[i], g);
      g = cmul(g, g);
#pragma unroll
      for (int i = 0; i < 16; ++i) {
        const cf q = i ? cmul(p[i], g) : g;
        asm volatile("v_cvt_pk_f16_f32 v[84+%c4], %0, %1\n\tv_cvt_pk_f16_f32 v[100+%c4], %2, %3" :: "v"(p[i].x), "v"(p[i].y), "v"(q.x), "v"(q.y), "i"(i));
      }
    }
#pragma unroll
    for (int k = 0; k < 21; ++k) asm volatile("" : "+v"(T[k]));
    STAMP(2);
    STAMP(3);
    const unsigned mb = opaque(sb + L_M), one = opaque(1u), l16 = opaque(lane * 16);
    const TrAddr tra = transpose_addr(sb + L_TA0, sb + L_TB0 + j * 1024, j, lane);
    const unsigned img = opaque(sb + L_TA0 + j * 2048 + lane * 16);
    const unsigned tro = opaque((64 * xcd + 16 * j + r8) * TROW + bi * 128 + c8 * 16);
    const unsigned tri = opaque((64 * xcd + 16 * j + r8) * TROW + bi * 128 + ((c8 ^ wswz(r8)) << 4));
    const unsigned chr = opaque((2 * cp + tch) * TROW + par * 2048 + lane * 16);
    const unsigned c4 = (unsigned)(par * 64 + lane) * 4, tlo = opaque(sb + L_TLO + tch * TLO_CH + c4), thi = opaque(sb + L_THI + tch * THI_CH + c4);
    const unsigned uz = opaque(sb + L_U + h * U_CH + l31 * 16);
    const unsigned KT = sb + L_P;
    const unsigned kz = opaque(tid < 28 ? sb + L_KC + (tid >= 14) * (KC_CH - 224) + tid * 16 : KT);
    const unsigned aa = opaque(sb + L_TLO + tch * TLO_CH + (16 * par + l15) * 528 + g4 * 16);
    const unsigned b0 = opaque(sb + L_THI + tch * THI_CH + l15 * 528 + g4 * 16), b1 = opaque(sb + L_THI + tch * THI_CH + (l15 < 4 ? 16 + l15 : 20) * 528 + g4 * 16);
    const int lb = 128 * par + 8 * l15;
    const int l0 = lb + (g4 < 2 ? 4 * g4 : 256 * (g4 - 1)), s1 = g4 < 2 ? 1 : 2 * (g4 - 1), s2 = g4 < 3 ? 2 * s1 : 256, l1 = lb + 1024 + 1024 * g4;
    const unsigned ka0 = opaque(KT + l0 * 4 + 2 * tch), ka1 = opaque(ka0 + s1 * 4), ka2 = opaque(ka0 + s2 * 4), ka3 = opaque(ka2 + s1 * 4), ka4 = opaque(KT + l1 * 4 + 2 * tch);
    const float dsk = __uint_as_float(opaque(l0 == 0 ? (tch ? d2 & 0xffff0000u : d2 << 16) : 0u));
    const unsigned ktr = opaque(KT + tid * 16), kcw = opaque(sb + L_KC + 224 + tid * 32);
    const unsigned wo0 = opaque((128 * j + r8) * 1024 + hg * 128 + ((c8 ^ wswz(r8)) << 4));
    const unsigned uw = opaque(sb + L_U + tch * U_CH + 512 + par * 2048 + lane * 16);
    unsigned aA = opaque(sb + L_KC + tch * KC_CH + (unsigned)(8 * (128 * par + 16 + l15 - 8 * g4 + 24)));
    unsigned aF = opaque(sb + L_U + tch * U_CH + (unsigned)(512 + 32 * l15 + 16 * g4 - 32 - 256 * par));
    const unsigned pa = opaque(sb + L_P + lane * 16 + j * 4096), pb = opaque(sb + L_P + lane * 16 + (j ^ 1) * 4096);
    const unsigned yw = opaque(sb + L_P + 16384 + j * 2048 + l15 * 32 + g4 * 8), yr = opaque(sb + L_P + 16384 + j * 2048 + lane * 16);
    const unsigned fl32 = opaque(l31 * 32), oo = opaque((64 * bi + 16 * j + r8) * 2048 + xcd * 128 + c8 * 16);
    u32x4 zero4 = {0u, 0u, 0u, 0u};
    asm volatile("" : "+v"(zero4));
    await_<M_AB + 12, 1>(mb, 1u);
    const unsigned tag = __builtin_amdgcn_readfirstlane(LD32(mb + M_TAG));
#define IMGWR "s_barrier\n\tds_write_b128 %[im], a[8:11]\n\tds_write_b128 %[im], a[12:15] offset:1024\n\t"
    if (j == 0) {
      const unsigned fo = W_F1 + (b & (NREP - 1)) * 2048 + l16;
      u32x4 f0, f1;
#define LOOK(T) asm volatile("global_load_dwordx4 %0, %[fo], %[ws] sc1\n\tglobal_load_dwordx4 %1, %[fo], %[ws] offset:1024 sc1\n\t" T "s_waitcnt vmcnt(0)" \
                     : "=&v"(f0), "=&v"(f1) : [fo] "v"(fo), [ws] "s"(ws), [im] "v"(img) : "memory", CLOB_A4_47)
#define SEEN() __all(((f0[1] ^ tag) | (f0[3] ^ tag) | (f1[1] ^ tag) | (f1[3] ^ tag)) == 0u)
      __builtin_amdgcn_s_sleep(WSLEEP);
      STAMP(13);
      LOOK(IMGWR);
      _Pragma("unroll 1") for (unsigned spins = 0; spins < kSpinCap; ++spins) {
        if (SEEN()) break;
        LOOK("");
      }
      LD32(mb + M_KMAX) = wave_umax(umax(umax(f0[0], f0[2]), umax(f1[0], f1[2])));
      if (b == 0 && lane == 0) __hip_atomic_store((gu32*)(ws + W_E), tag, RLX_AGENT);
    } else {
      STAMP(13);
      asm volatile(IMGWR :: [im] "v"(img) : "memory", CLOB_A4_47);
    }
    LD128(kz) = zero4;
#define TABLES_OUT() { _Pragma("unroll") for (int i = 0; i < 32; ++i) asm volatile("ds_write_b32 %0, v[84+%c1] offset:%c2" :: "v"(tlo), "i"(i), "i"(i * 528) : "memory"); \
      _Pragma("unroll") for (int i = 0; i < 21; ++i) LD32(thi + i * 528) = T[i]; }
    if (TWPOS == 1 && j) TABLES_OUT();
    STAMP(4);
    BAR();
    __builtin_amdgcn_s_setprio(SSM_PRIO);
    const __amdgpu_buffer_rsrc_t rsU = __builtin_amdgcn_make_buffer_rsrc(ws + W_UT, 0, 512 * TROW, 0x00020000);
    {
      u32x4 o0, o1;
      transpose_out<0>(tra, o0, o1);
      __builtin_amdgcn_raw_buffer_store_b128(o0, rsU, tro, 0, 16);
      asm volatile("buffer_store_dwordx4 %0, %1, %2, %3 offen sc1\n\ts_nop 1" :: "v"(o1), "v"(tro), "s"(make_srd(ws + W_UT, 512 * TROW)), "s"(8 * TROW) : "memory");
    }
    if (TWPOS == 0 || j == 0) TABLES_OUT();
    LD128(uz) = zero4;
    asm volatile("s_waitcnt vmcnt(0)" ::: "memory");
    SYNC4(0);
    if (j == 1 && lane == 0) __hip_atomic_store((gu32*)(ws + W_F3) + (xcd * 32 + bi) * 8, tag, RLX_AGENT);
    STAMP(5);
    {
      const u32x2 sc = LD64(mb + M_SCALE + tch * 8);
      f32x4 k0, k1;
#pragma unroll
      for (int ks = 0; ks < 8; ++ks) {
        const u32x4 fa = LD128(aa + ks * 64), fb = LD128(b0 + ks * 64), fc = LD128(b1 + ks * 64);
        if (ks == 0) asm volatile("v_mfma_f32_16x16x32_f16 %0, %3, %2, 0\n\tv_mfma_f32_16x16x32_f16 %1, %4, %2, 0" : "=&v"(k0), "=&v"(k1) : "v"(fa), "v"(fb), "v"(fc));
        else asm volatile("v_mfma_f32_16x16x32_f16 %0, %3, %2, %0\n\tv_mfma_f32_16x16x32_f16 %1, %4, %2, %1" : "+v"(k0), "+v"(k1) : "v"(fa), "v"(fb), "v"(fc));
      }
      asm volatile("s_nop 7" : "+v"(k0), "+v"(k1));
      k0 *= __uint_as_float(sc[0]); k1 *= __uint_as_float(sc[0]);
      if (__builtin_amdgcn_readfirstlane(sc[1])) {
        const unsigned kb = KT + 2 * tch;
        const unsigned la[4] = {ka0, ka1, ka2, ka3};
#pragma unroll
        for (int r = 0; r < 4; ++r) {
          k0[r] *= __builtin_amdgcn_exp2f(__uint_as_float(sc[1]) * (float)((la[r] - kb) >> 2));
          k1[r] *= __builtin_amdgcn_exp2f(__uint_as_float(sc[1]) * (float)(((ka4 - kb) >> 2) + 256 * r));
        }
      }
      k0[0] += dsk;
      const unsigned long long g23 = 0xffffffff00000000ull, g3 = 0xffff000000000000ull;
      const unsigned long long g23d = par ? g23 : g23 & ~(1ull << 32), g3d = par ? g3 : g3 & ~(1ull << 48);
      const unsigned long long a0 = par ? 0xffffull : 0x1ffffull, a0d = par ? 0xffffull : 0x1fffeull, e0 = par ? 0x5555ull : 0x15555ull, e0d = par ? 0x5555ull : 0x15554ull;
#define KW(R, o) "ds_write_b16" R " offset:" STR(o) "\n\t"
#define KR0 " %0, %5"
#define KR1 "_d16_hi %1, %5"
#define KR2 " %2, %6"
#define KR3 "_d16_hi %3, %6"
#define KS0 " %4, %7"
#define KS1 "_d16_hi %4, %7"
#define KS2 " %4, %8"
#define KS3 "_d16_hi %4, %8"
#define KW6(R, o) KW(R, o - 8) KW(R, o - 4) KW(R, o) KW(R, o + 4) KW(R, o + 8) KW(R, o + 12)
      asm volatile(KW(KR0, 16) KW(KR1, 16) KW(KR2, 16) KW(KR3, 16)
                   "s_mov_b64 exec, %9\n\t" KW(KR0, 20) KW(KR2, 20) KW(KR2, 12)
                   "s_mov_b64 exec, %10\n\t" KW(KR0, 12)
                   "s_mov_b64 exec, %11\n\t" KW(KR1, 20) KW(KR1, 12) KW(KR3, 20) KW(KR3, 12) KW(KR0, 24) KW(KR2, 24) KW(KR2, 8)
                   "s_mov_b64 exec, %12\n\t" KW(KR0, 8)
                   "s_mov_b64 exec, %13\n\t" KW6(KS0, 16)
                   "s_mov_b64 exec, %14\n\t" KW(KS0, 4)
                   "s_mov_b64 exec, %15\n\t" KW(KS0, 32)
                   "s_mov_b64 exec, %16\n\t" KW(KS0, 0)
                   "s_mov_b64 exec, 0xffff\n\t" KW6(KS1, 1040) KW(KS1, 1028) KW6(KS2, 2064) KW(KS2, 2052) KW6(KS3, 3088) KW(KS3, 3076)
                   "s_mov_b64 exec, 0x5555\n\t" KW(KS1, 1056) KW(KS1, 1024) KW(KS2, 2080) KW(KS2, 2048) KW(KS3, 3104) KW(KS3, 3072)
                   "s_mov_b64 exec, -1"
                   :: "v"(ka0), "v"(ka1), "v"(ka2), "v"(ka3), "v"(ka4), "v"(pkbf(k0[0], k0[1])), "v"(pkbf(k0[2], k0[3])),
                      "v"(pkbf(k1[0], k1[1])), "v"(pkbf(k1[2], k1[3])), "s"(g23), "s"(g23d), "s"(g3), "s"(g3d), "s"(a0), "s"(a0d), "s"(e0), "s"(e0d) : "memory");
    }
    SYNC4(1);
    if (j == 1) { wait_flags32<4>(ws + W_F3 + xcd * 1024, fl32, tag); arrive_<M_SW + 40>(mb, one); }
    else await_<M_SW + 40, 2>(mb, 1u);
    asm volatile("global_load_dwordx4 a[8:11], %0, %1 sc1\n\tglobal_load_dwordx4 a[12:15], %0, %1 offset:1024 sc1" :: "v"(chr), "s"(ws + W_UT) : "memory", CLOB_A4_47);
    STAMP(6);
#pragma unroll
    for (int k = 0; k < 2; ++k) {
      const u32x4 da = LD128(ktr + k * 4096), db = LD128(ktr + k * 4096 + 16);
      const unsigned d[7] = {da[1], da[2], da[3], db[0], db[1], db[2], db[3]};
#pragma unroll
      for (int c = 0; c < 2; ++c) {
        unsigned P[8];
#pragma unroll
        for (int i = 0; i < 8; ++i) {
          const int m = i < 6 ? i : i - 4;
          asm("v_perm_b32 %0, %1, %2, %3" : "=v"(P[i]) : "v"(d[m]), "v"(d[m + 1]), "s"(c ? 0x07060302u : 0x05040100u), "i"(i));
        }
        LD128(kcw + k * 8192 + c * KC_CH) = (u32x4){P[2], P[0], P[3], P[1]};
        LD128(kcw + k * 8192 + c * KC_CH + 16) = (u32x4){P[4], P[6], P[5], P[7]};
      }
    }
    STAMP(7);
    asm volatile("s_waitcnt vmcnt(0)\n\tds_write_b128 %0, a[8:11]\n\tds_write_b128 %0, a[12:15] offset:1024" :: "v"(uw) : "memory", CLOB_A4_47);
    SYNC4(2);
    STAMP(8);
    f32x4 y[8];
    unsigned cl, wk_, wso_;
#define WOSTEP "buffer_load_dwordx4 %[w0], %[rw], %[so] offen lds\n\tv_xor_b32 %[w0], 64, %[w0]\n\ts_add_u32 m0, m0, 0x400\n\ts_add_u32 %[so], %[so], 0x2000\n\t"
    unsigned wv0 = wo0;
    asm volatile(
        "s_setprio " STR(CONV_PRIO) "\n\ts_nop 1\n\tv_mfma_f32_16x16x16_bf16 %0, %[z], %[z], 0\n\tv_mfma_f32_16x16x16_bf16 %1, %[z], %[z], 0\n\tv_mfma_f32_16x16x16_bf16 %2, %[z], %[z], 0\n\tv_mfma_f32_16x16x16_bf16 %3, %[z], %[z], 0\n\tv_mfma_f32_16x16x16_bf16 %4, %[z], %[z], 0\n\tv_mfma_f32_16x16x16_bf16 %5, %[z], %[z], 0\n\tv_mfma_f32_16x16x16_bf16 %6, %[z], %[z], 0\n\tv_mfma_f32_16x16x16_bf16 %7, %[z], %[z], 0\n\ts_mov_b32 %[n], " STR(CONV_N) "\n\t"
        "s_mov_b32 %[k], m0\n\ts_mov_b32 m0, %[wd]\n\ts_mov_b32 %[so], 0\n\t"
        "1:\n\t"
        "ds_read_b64 a[16:17], %[aA] offset:32\n\tds_read_b64 a[18:19], %[aA] offset:0\n\tds_read_b128 v[84:87], %[aF] offset:0\n\tds_read_b128 v[88:91], %[aF] offset:512\n\tds_read_b128 v[92:95], %[aF] offset:1024\n\tds_read_b128 v[96:99], %[aF] offset:1536\n\tds_read_b128 v[100:103], %[aF] offset:2048\n\tds_read_b128 v[104:107], %[aF] offset:2560\n\tds_read_b128 v[108:111], %[aF] offset:3072\n\tds_read_b128 v[112:115], %[aF] offset:3584\n\tds_read_b64 a[20:21], %[aA] offset:2080\n\tds_read_b64 a[22:23], %[aA] offset:2048\n\t"
        "s_waitcnt lgkmcnt(2)\n\tds_read_b64 a[24:25], %[aA] offset:4128\n\tds_read_b64 a[26:27], %[aA] offset:4096\n\tv_mfma_f32_16x16x32_bf16 %0, a[16:19], v[84:87], %0\n\t" CPAD "v_mfma_f32_16x16x32_bf16 %1, a[16:19], v[88:91], %1\n\t" CPAD "v_mfma_f32_16x16x32_bf16 %2, a[16:19], v[92:95], %2\n\t" CPAD "v_mfma_f32_16x16x32_bf16 %3, a[16:19], v[96:99], %3\n\t" CPAD "v_mfma_f32_16x16x32_bf16 %4, a[16:19], v[100:103], %4\n\t" CPAD "v_mfma_f32_16x16x32_bf16 %5, a[16:19], v[104:107], %5\n\t" CPAD "v_mfma_f32_16x16x32_bf16 %6, a[16:19], v[108:111], %6\n\t" CPAD "v_mfma_f32_16x16x32_bf16 %7, a[16:19], v[112:115], %7\n\t" CPAD "ds_read_b64 a[28:29], %[aA] offset:6176\n\tds_read_b64 a[30:31], %[aA] offset:6144\n\ts_waitcnt lgkmcnt(4)\n\tv_mfma_f32_16x16x32_bf16 %1, a[20:23], v[84:87], %1\n\t" CPAD "v_mfma_f32_16x16x32_bf16 %2, a[20:23], v[88:91], %2\n\t" CPAD "v_mfma_f32_16x16x32_bf16 %3, a[20:23], v[92:95], %3\n\t" CPAD "v_mfma_f32_16x16x32_bf16 %4, a[20:23], v[96:99], %4\n\t" CPAD "v_mfma_f32_16x16x32_bf16 %5, a[20:23], v[100:103], %5\n\t" CPAD "v_mfma_f32_16x16x32_bf16 %6, a[20:23], v[104:107], %6\n\t" CPAD "v_mfma_f32_16x16x32_bf16 %7, a[20:23], v[108:111], %7\n\t" CPAD "" WOSTEP "ds_read_b64 a[32:33], %[aA] offset:8224\n\tds_read_b64 a[34:35], %[aA] offset:8192\n\ts_waitcnt lgkmcnt(4)\n\tv_mfma_f32_16x16x32_bf16 %2, a[24:27], v[84:87], %2\n\t" CPAD "v_mfma_f32_16x16x32_bf16 %3, a[24:27], v[88:91], %3\n\t" CPAD "v_mfma_f32_16x16x32_bf16 %4, a[24:27], v[92:95], %4\n\t" CPAD "v_mfma_f32_16x16x32_bf16 %5, a[24:27], v[96:99], %5\n\t" CPAD "v_mfma_f32_16x16x32_bf16 %6, a[24:27], v[100:103], %6\n\t" CPAD "v_mfma_f32_16x16x32_bf16 %7, a[24:27], v[104:107], %7\n\t" CPAD "ds_read_b64 a[36:37], %[aA] offset:10272\n\tds_read_b64 a[38:39], %[aA] offset:10240\n\ts_waitcnt lgkmcnt(4)\n\tv_mfma_f32_16x16x32_bf16 %3, a[28:31], v[84:87], %3\n\t" CPAD "v_mfma_f32_16x16x32_bf16 %4, a[28:31], v[88:91], %4\n\t" CPAD "v_mfma_f32_16x16x32_bf16 %5, a[28:31], v[92:95], %5\n\t" CPAD "v_mfma_f32_16x16x32_bf16 %6, a[28:31], v[96:99], %6\n\t" CPAD "v_mfma_f32_16x16x32_bf16 %7, a[28:31], v[100:103], %7\n\t" CPAD "" WOSTEP "ds_read_b64 a[40:41], %[aA] offset:12320\n\tds_read_b64 a[42:43], %[aA] offset:12288\n\ts_waitcnt lgkmcnt(4)\n\tv_mfma_f32_16x16x32_bf16 %4, a[32:35], v[84:87], %4\n\t" CPAD "v_mfma_f32_16x16x32_bf16 %5, a[32:35], v[88:91], %5\n\t" CPAD "v_mfma_f32_16x16x32_bf16 %6, a[32:35], v[92:95], %6\n\t" CPAD "v_mfma_f32_16x16x32_bf16 %7, a[32:35], v[96:99], %7\n\t" CPAD "ds_read_b64 a[44:45], %[aA] offset:14368\n\tds_read_b64 a[46:47], %[aA] offset:14336\n\ts_waitcnt lgkmcnt(4)\n\tv_mfma_f32_16x16x32_bf16 %5, a[36:39], v[84:87], %5\n\t" CPAD "v_mfma_f32_16x16x32_bf16 %6, a[36:39], v[88:91], %6\n\t" CPAD "v_mfma_f32_16x16x32_bf16 %7, a[36:39], v[92:95], %7\n\t" CPAD "" WOSTEP "s_waitcnt lgkmcnt(2)\n\tv_mfma_f32_16x16x32_bf16 %6, a[40:43], v[84:87], %6\n\t" CPAD "v_mfma_f32_16x16x32_bf16 %7, a[40:43], v[88:91], %7\n\t" CPAD "" WOSTEP "s_waitcnt lgkmcnt(0)\n\tv_mfma_f32_16x16x32_bf16 %7, a[44:47], v[84:87], %7\n\t" CPAD ""
        "v_add_u32 %[aA], 0x100, %[aA]\n\tv_add_u32 %[aF], 0xffffffc0, %[aF]\n\ts_sub_u32 %[n], %[n], 1\n\ts_cmp_lg_u32 %[n], 0\n\ts_cbranch_scc1 1b\n\t"
        "s_mov_b32 m0, %[k]\n\ts_setprio " STR(SSM_PRIO) "\n\ts_nop 7"
        : "=&v"(y[0]), "=&v"(y[1]), "=&v"(y[2]), "=&v"(y[3]), "=&v"(y[4]), "=&v"(y[5]), "=&v"(y[6]), "=&v"(y[7]),
          [n] "=&s"(cl), [aA] "+v"(aA), [aF] "+v"(aF), [k] "=&s"(wk_), [so] "=&s"(wso_), [w0] "+v"(wv0)
        : [z] "v"(__builtin_shufflevector(zero4, zero4, 0, 1)), [rw] "s"(make_srd(wo, 524288)), [wd] "s"(sb + L_WO + j * 16384)
        : "memory", "scc", CLOB_A4_47);
#define YSEND(o) _Pragma("unroll") for (int q = 0; q < 4; ++q) LDF4(pa + q * 1024) = y[o + q]
    if (par) { YSEND(0); asm volatile("" ::: "memory"); } else { YSEND(4); }
    STAMP(9);
    SYNC4(3);
    STAMP(12);
    {
#define YKEEP(o) _Pragma("unroll") for (int q = 0; q < 4; ++q) { \
        const f32x4 v = LDF4(pb + q * 1024) + y[o + q]; \
        LD64(yw + q * 512) = (u32x2){pkbf(v[0], v[1]), pkbf(v[2], v[3])}; }
      if (par) { YKEEP(4); asm volatile("" ::: "memory"); } else { YKEEP(0); }
      const u32x4 r0 = LD128(yr), r1 = LD128(yr + 1024);
      asm volatile("" ::: "memory");
      arrive_<M_SW + 28>(mb, one);
      const __amdgpu_buffer_rsrc_t rs = __builtin_amdgcn_make_buffer_rsrc(ws + W_YT, 0, 512 * TROW, 0x00020000);
      __builtin_amdgcn_raw_buffer_store_b128(r0, rs, chr, 0, 16);
      __builtin_amdgcn_raw_buffer_store_b128(r1, rs, chr + 1024, 0, 16);
      STAMP(14);
      asm volatile("s_waitcnt vmcnt(0)" ::: "memory");
      STAMP(15);
      SYNC4(4);
      if (j == 0 && lane == 0) __hip_atomic_store((gu32*)(ws + W_F4) + (xcd * 32 + bi) * 8, tag, RLX_AGENT);
      STAMP(10);
      if (j == 0) { __builtin_amdgcn_s_sleep(16); wait_flags32<FSLEEP>(ws + W_F4 + xcd * 1024, fl32, tag); arrive_<M_SW + 44>(mb, one); }
      else await_<M_SW + 44, 3>(mb, 1u);
      STAMP(16);
      asm volatile("buffer_load_dwordx4 a[8:11], %0, %2, 0 offen sc1\n\tbuffer_load_dwordx4 a[12:15], %1, %2, %3 offen sc1\n\ts_waitcnt vmcnt(0)\n\t"
                   "ds_write_b128 %4, a[8:11] offset:%c5\n\tds_write_b128 %4, a[12:15] offset:%c5+1024"
                   :: "v"(tri), "v"(tri ^ 64), "s"(make_srd(ws + W_YT, 512 * TROW)), "s"(8 * TROW), "v"(img), "i"(L_U - L_TA0) : "memory", CLOB_A4_47);
      STAMP(17);
      SYNC4(9);
      STAMP(18);
      u32x4 o0, o1;
      transpose_out<L_U - L_TA0>(tra, o0, o1);
      asm volatile("buffer_store_dwordx4 %0, %2, %3, 0 offen sc1\n\tbuffer_store_dwordx4 %1, %2, %3, %4 offen sc1\n\ts_nop 1"
                   :: "v"(o0), "v"(o1), "v"(oo), "s"(make_srd(out, 4194304)), "s"(16384) : "memory");
    }
    STAMP(11);
    STAMP_OUT();
    return;
  }

  LANE_VARS();
  const int qq = w & 3, kh = w >> 2, chain = hg >> 2;
  const bool crit = w == (hg & 3);
  const unsigned short* wkv = chain ? wv : wk;
  const bool helper = WHELP && w == (hg & 3) + 4;
  if (helper) {
#pragma unroll
    for (int i = 0; i < 4; ++i) gdma4((unsigned)(i * 4096 + lane * 16), wkv, sb + L_R2 + i * 4096);
  }
  if (!crit) asm volatile("s_barrier" ::: "memory");
  else __builtin_amdgcn_s_setprio(3);
  if (kh == 0) {
    const unsigned xo = (64 * grp + 16 * qq + r8) * 1024 + ((c8 ^ (r8 >> 1)) << 4);
    gdma_x(xo, (xo ^ 64) + 8192, x, sb + L_X + qq * 16384);
    gdma_x(xo + 512, (xo ^ 64) + 8704, x, sb + L_X + qq * 16384 + 8192);
  } else {
    const unsigned wq0 = (64 * qq + r8) * 1024 + hg * 128 + ((c8 ^ ((r8 & 2) << 1)) << 4);
    gdma_rows8(wq0, wq0 + 8192, wq, sb + L_WQ + qq * 8192);
    gdma_rows8(wq0 + 262144, wq0 + 270336, wq, sb + L_WQ + 32768 + qq * 8192);
  }
  if (helper) {
    asm volatile("s_waitcnt vmcnt(16)" ::: "memory");
    arrive(sb + L_M + M_HA + 8);
  }
  u32x2 fword;
  unsigned hook = crit ? 2 : kh ^ 1;
  if (!crit) asm volatile("" : "=v"(fword));
  if (crit) {
    if (!WHELP) {
#pragma unroll
      for (int i = 0; i < 4; ++i) gdma4((unsigned)(i * 4096 + lane * 16), wkv, sb + L_R2 + i * 4096);
    }
    asm volatile("s_barrier" ::: "memory");
    unsigned E;
    asm volatile("s_load_dword %0, %1, 0x0\n\ts_waitcnt lgkmcnt(0)" : "=s"(E) : "s"(ws) : "memory");
    const unsigned tg = E + (((E * 0x9E3779B1u ^ (E >> 15) ^ 0x5bd1e995u) & 0x7fffffffu) | 0x101u);
    LD32(sb + L_M + M_TAG) = tg;
    const unsigned wa = sb + L_R2 + (8 * g4 + (l15 >> 2)) * 32 + (lane & 3) * 8;
    const unsigned xe = sb + L_X + qq * 16384 + (l15 >> 3) * 4096 + (l15 & 7) * 128 + ((g4 ^ (l15 >> 1)) << 4);
    const unsigned tb = (2 * grp + (qq >> 1)) * KV_TILE;
    const unsigned off = chain ? ((g4 & 1) == 0 ? tb + 1024 + (qq & 1) * 640 + (g4 >> 1) * 256 + l15 * 16 : (lane >> 3) == 2 ? tb + 1536 + (qq & 1) * 640 + c8 * 16 : 0x80000000u)
                               : (g4 < 2 ? tb + (qq & 1) * 256 + g4 * 512 + l15 * 16 : 0x80000000u);
    f32x4 acc, acd;
#define KR(s, o) "v[36+8*(" #s "%%6)+" #o ":36+8*(" #s "%%6)+" #o "+3]"
#define K_LD(s, X) "ds_read_b128 " KR(s, 0) ", " X " offset:8192*(" #s "/8)+1024*((" #s "&7)/2)\n\t" \
      "ds_read_b64_tr_b16 v[36+8*(" #s "%%6)+4:36+8*(" #s "%%6)+5], %4 offset:1024*" #s "\n\tds_read_b64_tr_b16 v[36+8*(" #s "%%6)+6:36+8*(" #s "%%6)+7], %4 offset:1024*" #s "+128\n\t"
#define K_LD2(s, t) K_LD(s, "%2") K_LD(t, "%3")
#define K_MM(N, s, t, A, B, C0, C1) "s_waitcnt lgkmcnt(" #N ")\n\tv_mfma_f32_16x16x32_bf16 %0, " KR(s, A) ", " KR(s, B) ", " C0 "\n\t" \
      "v_mfma_f32_16x16x32_bf16 %1, " KR(t, A) ", " KR(t, B) ", " C1 "\n\t"
#define K_ALL(A, B) asm volatile("s_waitcnt lgkmcnt(0)\n\t" K_LD2(0, 1) K_LD2(2, 3) K_LD2(4, 5) K_MM(12, 0, 1, A, B, "0", "0") K_LD2(6, 7) K_MM(12, 2, 3, A, B, "%0", "%1") \
        K_LD2(8, 9) K_MM(12, 4, 5, A, B, "%0", "%1") K_LD2(10, 11) K_MM(12, 6, 7, A, B, "%0", "%1") K_LD2(12, 13) K_MM(12, 8, 9, A, B, "%0", "%1") \
        K_LD2(14, 15) K_MM(12, 10, 11, A, B, "%0", "%1") K_MM(6, 12, 13, A, B, "%0", "%1") K_MM(0, 14, 15, A, B, "%0", "%1") "s_nop 7" \
        : "=&v"(acc), "=&v"(acd) : "v"(xe), "v"(xe ^ 64), "v"(wa) : "memory", CLOB_V36_83)
    asm volatile("s_waitcnt vmcnt(0)" ::: "memory");
    arrive(sb + L_M + M_HA); arrive(sb + L_M + M_HA + 4);
    if (WHELP) await<0>(sb + L_M + M_HA + 8, 1u);
    if (chain) K_ALL(0, 4); else K_ALL(4, 0);
    STAMP(1);
    acc = (acc + acd) * (chain ? 1.0f : 0.25f * 1.44269504088896f);
    const u32x2 pk = {pkbf(acc[0], acc[1]), pkbf(acc[2], acc[3])};
    const __amdgpu_buffer_rsrc_t rs = __builtin_amdgcn_make_buffer_rsrc(ws + W_KV, 0, KV_BYTES, 0x00020000);
    u32x4 pv;
    unsigned kn2 = 0;
    if (chain) {
      auto x0 = __builtin_amdgcn_permlane16_swap(pk[0], pk[0], false, false);
      auto x1 = __builtin_amdgcn_permlane16_swap(pk[1], pk[1], false, false);
      pv = (g4 & 1) ? (u32x4){0x3f803f80u, 0x3f803f80u, 0x3f803f80u, 0x3f803f80u} : (u32x4){x0[0], x1[0], x0[1], x1[1]};
    } else {
      auto x0 = __builtin_amdgcn_permlane32_swap(pk[0], pk[0], false, false);
      auto x1 = __builtin_amdgcn_permlane32_swap(pk[1], pk[1], false, false);
      pv = (u32x4){x0[0], x1[0], x0[1], x1[1]};
    }
    __builtin_amdgcn_raw_buffer_store_b128(pv, rs, off, 0, 16);
    if (!chain) {
      const float n2 = sq2(pk[0], sq2(pk[1], 0.f));
      kn2 = row_umax(__float_as_uint(sum32(sum16(n2, n2))));
    }
    fword = (u32x2){kn2, tg};
    _Pragma("unroll 1") for (unsigned spins = 0; spins < kSpinCap; ++spins) {
      unsigned v, st;
      asm volatile("ds_read_b32 %0, %2\n\ts_waitcnt lgkmcnt(0)\n\ts_getreg_b32 %1, hwreg(HW_REG_IB_STS, 0, 4)" : "=v"(v), "=s"(st) : "v"(sb + L_M + M_HA) : "memory");
      if (EARLYPUB && st == 0) {
        asm volatile("s_waitcnt vmcnt(0)" ::: "memory");
        if (lane < NREP) __hip_atomic_store((gu64*)(ws + W_F1) + lane * 256 + bi * 8 + xcd, ((unsigned long long)tg << 32) | kn2, RLX_AGENT);
        arrive(sb + L_M + M_AB + 12);
        hook = 0;
        break;
      }
      if (__builtin_amdgcn_readfirstlane(v) >= 8u) break;
    }
    STAMP(2);
  } else {
    asm volatile("s_waitcnt vmcnt(8)" ::: "memory");
    arrive(sb + L_M + M_HA);
    if (kh) { asm volatile("s_waitcnt vmcnt(0)" ::: "memory"); arrive(sb + L_M + M_HA + 4); }
  }
  STAMP(3);
  await<0>(sb + L_M + M_HA + 4 * kh, 8u);
  STAMP(4);

  f32x16 qa16;
  {
    const unsigned A0 = sb + L_WQ + (256 * kh + 8 * h + (l15 >> 2)) * 128 + (((4 * (qq & 1) + 2 * ((g4 & 1) ^ kh) + ((lane & 3) >> 1)) ^ ((l15 >> 3) << 2)) << 4) + (lane & 1) * 8;
    const unsigned X0 = sb + L_X + ((qq & 2) + (g4 & 1)) * 16384 + kh * 8192 + (l15 >> 3) * 4096 + (l15 & 7) * 128 + ((h ^ (l15 >> 1)) << 4);
#define Q_LD(s, X) "ds_read_b128 " KR(s, 0) ", " X " offset:1024*(" #s "/4)\n\t" \
      "ds_read_b64_tr_b16 v[36+8*(" #s "%%6)+4:36+8*(" #s "%%6)+5], %1 offset:2048*" #s "\n\tds_read_b64_tr_b16 v[36+8*(" #s "%%6)+6:36+8*(" #s "%%6)+7], %1 offset:2048*" #s "+512\n\t"
#define Q_MM(N, s, C) "s_waitcnt lgkmcnt(" #N ")\n\tv_mfma_f32_32x32x16_bf16 %0, " KR(s, 4) ", " KR(s, 0) ", " C "\n\t"
    asm volatile(Q_LD(0, "%2") Q_LD(1, "%3") Q_LD(2, "%4") Q_LD(3, "%5") Q_LD(4, "%2") Q_MM(12, 0, "0") Q_LD(5, "%3") Q_MM(12, 1, "%0") Q_LD(6, "%4") Q_MM(12, 2, "%0")
                 Q_LD(7, "%5") Q_MM(12, 3, "%0") Q_LD(8, "%2") Q_MM(12, 4, "%0")
                 "s_cmp_eq_u32 %6, 0\n\ts_cbranch_scc1 1f\n\ts_waitcnt vmcnt(0)\n\ts_cmp_eq_u32 %6, 1\n\ts_cbranch_scc1 2f\n\t"
                 "s_mov_b64 exec, " STR((1 << NREP) - 1) "\n\tglobal_store_dwordx2 %9, %10, %11 sc1\n\t"
                 "2:\n\ts_mov_b64 exec, 1\n\tds_add_u32 %7, %8\n\ts_mov_b64 exec, -1\n\t1:\n\t"
                 Q_LD(9, "%3") Q_MM(12, 5, "%0") Q_LD(10, "%4") Q_MM(12, 6, "%0") Q_LD(11, "%5") Q_MM(12, 7, "%0")
                 Q_LD(12, "%2") Q_MM(12, 8, "%0") Q_LD(13, "%3") Q_MM(12, 9, "%0") Q_LD(14, "%4") Q_MM(12, 10, "%0") Q_LD(15, "%5") Q_MM(12, 11, "%0")
                 Q_MM(9, 12, "%0") Q_MM(6, 13, "%0") Q_MM(3, 14, "%0") Q_MM(0, 15, "%0") "s_setprio 0\n\ts_nop 10"
                 : "=&v"(qa16) : "v"(A0), "v"(X0), "v"(X0 ^ 32), "v"(X0 ^ 64), "v"(X0 ^ 96),
                   "s"(__builtin_amdgcn_readfirstlane(opaque(hook))), "v"(sb + L_M + (crit ? M_AB + 12 : M_HA + 4)), "v"(1u),
                   "v"(W_F1 + lane * 2048 + (bi * 8 + xcd) * 8), "v"(fword), "s"(ws) : "memory", "scc", CLOB_V36_83);
  }
  STAMP(5);
  const unsigned xa = opaque(sb + L_R2 + lane * 16 + w * 2048);
  LDF4(xa) = __builtin_shufflevector(qa16, qa16, 8, 9, 10, 11);
  LDF4(xa + 1024) = __builtin_shufflevector(qa16, qa16, 12, 13, 14, 15);
  BAR();
  STAMP(6);
  const i32x4 rsKV = make_srd(ws + W_KV, KV_BYTES);
  const unsigned ko = lane * 16, vo = l31 < 16 ? 1024 + h * 256 + l31 * 16 : l31 == 16 ? 1536u : 0x80000000u;
  const unsigned t0 = (16 * qq + 2 * bi) & 63;
  const unsigned tag = __builtin_amdgcn_readfirstlane(LD32(sb + L_M + M_TAG));
#define KV_FIRST() { unsigned tl_ = t0, sa_, sb_; asm volatile(A_FIRST : [tl] "+s"(tl_), [sa] "=&s"(sa_), [sb] "=&s"(sb_) \
    : [ko] "v"(ko), [vo] "v"(vo), [rk] "s"(rsKV) : "memory", "scc", CLOB_A4_47); }
  {
    const f32x4 q0 = __builtin_shufflevector(qa16, qa16, 0, 1, 2, 3) + LDF4(xa ^ 8192), q1 = __builtin_shufflevector(qa16, qa16, 4, 5, 6, 7) + LDF4((xa ^ 8192) + 1024);
    const u32x4 o = {pkbf(q0[0], q0[1]), pkbf(q0[2], q0[3]), pkbf(q1[0], q1[1]), pkbf(q1[2], q1[3])};
    LD128(sb + L_Q + (2 * qq + kh) * 1024 + lane * 16) = o;
    const unsigned n2 = row_umax(__float_as_uint(sum32(sq2(o[0], sq2(o[1], sq2(o[2], sq2(o[3], 0.f)))))));
    asm volatile("s_mov_b64 exec, 0x10001\n\tds_max_u32 %0, %1\n\ts_mov_b64 exec, -1" :: "v"(sb + L_M + M_QMAX), "v"(n2) : "memory");
  }
  STAMP(7);
  BAR();
  STAMP(8);
  RSTAMP(18);
  KV_FIRST();

  const u32x2 qk2 = LD64(sb + L_M + M_QMAX);
  const float bound = fmaf(__builtin_amdgcn_sqrtf(__uint_as_float(qk2[0]) * __uint_as_float(qk2[1])), 1.002f, 1e-3f);
  const unsigned exact = __builtin_amdgcn_readfirstlane(opaque(bound <= 32.0f ? 0u : 1u));
  {
    unsigned tl = t0, n = 6, sa, sbo, sw = kh * PSW;
    const unsigned qb = sb + L_Q + kh * 4096, ob = sb + L_OA + w * 4 * OA_SLOT;
    if (exact) {
      const unsigned maddr = sb + L_MX + kh * 512 + l31 * 4;
      if (w < 4) LDF(sb + L_MX + (w * 64 + lane) * 4) = -3.0e38f;
      arrive(sb + L_M + M_AB + 4);
      await<0>(sb + L_M + M_AB + 4, 8u);
      float m[4] = {-3.0e38f, -3.0e38f, -3.0e38f, -3.0e38f};
      const __amdgpu_buffer_rsrc_t rs = __builtin_amdgcn_make_buffer_rsrc(ws + W_KV, 0, KV_BYTES, 0x00020000);
#pragma unroll 1
      for (unsigned i = 0; i < 16; ++i) {
        u32x4 kf = __builtin_amdgcn_raw_buffer_load_b128(rs, ko, ((tl + i) & 63) * KV_TILE, 16);
#pragma unroll
        for (int t = 0; t < 4; ++t) {
          const u32x4 q = LD128(qb + ko + t * 1024);
          f32x16 s_;
          asm volatile("s_nop 1\n\tv_mfma_f32_32x32x16_bf16 %0, %1, %2, 0\n\ts_nop 15" : "=&v"(s_) : "v"(kf), "v"(q) : CLOB_A4_47);
#pragma unroll
          for (int r = 0; r < 16; ++r) m[t] = fmaxf(m[t], s_[r]);
        }
      }
#pragma unroll
      for (int t = 0; t < 4; ++t) __builtin_amdgcn_ds_fmaxf((LAS float*)(size_t)(maddr + t * 128), m[t], __ATOMIC_RELAXED, __HIP_MEMORY_SCOPE_WORKGROUP, false);
      arrive(sb + L_M + M_AB + 8);
      await<0>(sb + L_M + M_AB + 8, 8u);
      asm volatile("v_accvgpr_write_b32 a44, %0\n\tv_accvgpr_write_b32 a45, %1\n\tv_accvgpr_write_b32 a46, %2\n\tv_accvgpr_write_b32 a47, %3"
                   :: "v"(-LDF(maddr)), "v"(-LDF(maddr + 128)), "v"(-LDF(maddr + 256)), "v"(-LDF(maddr + 384)) : CLOB_A4_47);
    }
    asm volatile(A_ATT
                 : [tl] "+s"(tl), [n] "+s"(n), [sa] "=&s"(sa), [sb] "=&s"(sbo), [sw] "+s"(sw)
                 : [ko] "v"(ko), [vo] "v"(vo), [rk] "s"(rsKV), [qb] "s"(qb), [ob] "s"(ob), [pf] "s"(sb + L_M + M_SW + 28), [ex] "s"(exact)
                 : "memory", "scc", "vcc", CLOB_V4_83, CLOB_A4_47);
  }
  STAMP(9);
  RSTAMP(17);
  BAR();
  STAMP(10);

  {
    LANE_VARS();
    {
      const unsigned s0 = opaque(sb + L_OA + (16 * kh + qq) * OA_SLOT + lane * 16), s1 = opaque(sb + L_OA + (16 * kh + qq) * OA_SLOT + 2048 + lane * 4);
      constexpr unsigned W1 = 4 * OA_SLOT;
      f32x4 o0 = (LDF4(s0) + LDF4(s0 + W1)) + (LDF4(s0 + 2 * W1) + LDF4(s0 + 3 * W1));
      f32x4 o1 = (LDF4(s0 + 1024) + LDF4(s0 + W1 + 1024)) + (LDF4(s0 + 2 * W1 + 1024) + LDF4(s0 + 3 * W1 + 1024));
      const float inv = __builtin_amdgcn_rcpf(sum32((LDF(s1) + LDF(s1 + W1)) + (LDF(s1 + 2 * W1) + LDF(s1 + 3 * W1))));
      o0 *= inv; o1 *= inv;
      u32x4 pk = {pkbf(o0[0], o0[1]), pkbf(o0[2], o0[3]), pkbf(o1[0], o1[1]), pkbf(o1[2], o1[3])};
      const __amdgpu_buffer_rsrc_t rsO = __builtin_amdgcn_make_buffer_rsrc(ws + W_OX, 0, 2097152, 0x00020000);
      __builtin_amdgcn_raw_buffer_store_b128(pk, rsO, lane * 16 + ((2 * grp + kh) * 32 + 4 * hg + qq) * 1024, 0, 16);
    }
    STAMP(11);
    RSTAMP(13);
    const unsigned r0 = 256 * kh + 16 * (g4 >> 1) + 4 * (g4 & 1) + (l15 >> 2);
    const unsigned A0 = sb + L_WO + r0 * 128 + (((4 * kh + ((lane & 3) >> 1)) ^ wswz(r0)) << 4) + (lane & 1) * 8;
    const unsigned oo = (g4 >> 1) * 1024 + ((g4 & 1) * 32 + 16 * (qq & 1) + l15) * 16 + ((2 * grp + (qq >> 1)) * 32 + 16 * kh) * 1024;
    const unsigned xa = lane * 16;
    const unsigned xw = sb + L_OA + (2 * qq + kh) * 4352 + l15 * 272 + kh * 128 + g4 * 16;
    const unsigned char* f2 = ws + W_F2 + grp * 1024;
    unsigned t0, t1;
#define T_WF1(F, B, s, e, P, Q) "ds_read_b64_tr_b16 " F "[" #B "+4*" #e ":" #B "+4*" #e "+1], " P " offset:4096*" #s "\n\t" \
                                "ds_read_b64_tr_b16 " F "[" #B "+4*" #e "+2:" #B "+4*" #e "+3], " Q " offset:4096*" #s "+1024\n\t"
#define T_WF(F, B, s) T_WF1(F, B, s, 0, "%[a0]", "v6") T_WF1(F, B, s, 1, "v5", "v7") T_WF1(F, B, s, 2, "v6", "%[a0]") T_WF1(F, B, s, 3, "v7", "v5")
#define T_M1(F, B, s, e, C) "v_mfma_f32_16x16x32_bf16 v[104+4*" #e ":107+4*" #e "], " F "[" #B "+4*" #e ":" #B "+4*" #e "+3], v[72+4*" #s ":75+4*" #s "], " C "\n\t"
#define T_MA(e) "v[104+4*" #e ":107+4*" #e "]"
#define T_MM(F, B, s, N) "s_waitcnt vmcnt(" #N ")\n\t" T_M1(F, B, s, 0, T_MA(0)) T_M1(F, B, s, 1, T_MA(1)) T_M1(F, B, s, 2, T_MA(2)) T_M1(F, B, s, 3, T_MA(3))
#define OPF "v_and_b32 v72, 0x3f0, %[xa]\n\tbuffer_load_dwordx4 v[108:111], v72, %[ro], %[own] offen sc1\n\t"
#define T_OL(s, so, imm) "buffer_load_dwordx4 v[72+4*" #s ":75+4*" #s "], %[oo], %[ro], " so " offen offset:" #imm " sc1\n\t"
    asm volatile(
        "v_xor_b32 v5, 32, %[a0]\n\tv_xor_b32 v6, 64, %[a0]\n\tv_xor_b32 v7, 0x60, %[a0]\n\t"
        T_WF("v", 8, 0) T_WF("v", 24, 1) T_WF("v", 40, 2)
        "s_waitcnt vmcnt(0)\n\t" OPF "s_barrier\n\t"
        TS(80) "s_cmp_lg_u32 %[w], 0\n\ts_cbranch_scc1 1f\n\t"
        "v_mov_b32 v72, %[fo]\n\tv_mov_b32 v73, %[tg]\n\ts_mov_b64 exec, 1\n\tglobal_store_dword v72, v73, %[f2] sc1\n\ts_mov_b64 exec, -1\n\t"
        "1:\n\t"
        T_WF("v", 56, 3) T_WF("a", 8, 4) T_WF("a", 24, 5)
        "s_cbranch_scc1 3f\n\t"
        "v_and_b32 v4, 0x70, %[xa]\n\tv_lshlrev_b32 v4, 3, v4\n\ts_mov_b32 %[t0], 0x100000\n\ts_sleep " STR(TSLEEP) "\n\t"
        "2:\n\tglobal_load_dword v104, v4, %[f2] sc1\n\ts_waitcnt vmcnt(0)\n\tv_cmp_ne_u32 vcc, %[tg], v104\n\ts_cbranch_vccz 3f\n\t"
        "s_sub_u32 %[t0], %[t0], 1\n\ts_cbranch_scc0 2b\n\t"
        "3:\n\ts_barrier\n\t" TS(82) "s_movk_i32 %[t0], 0x1000\n\ts_movk_i32 %[t1], 0x2000\n\t"
        T_OL(0, "0", 0) T_OL(1, "0", 2048) T_OL(2, "%[t0]", 0) T_OL(3, "%[t0]", 2048) "s_movk_i32 %[t0], 0x3000\n\t"
        T_OL(4, "%[t1]", 0) T_OL(5, "%[t1]", 2048) T_OL(6, "%[t0]", 0) T_OL(7, "%[t0]", 2048)
        "s_waitcnt lgkmcnt(0) vmcnt(7)\n\t" T_M1("v", 8, 0, 0, "0") T_M1("v", 8, 0, 1, "0") T_M1("v", 8, 0, 2, "0") T_M1("v", 8, 0, 3, "0")
        T_WF("v", 8, 6) T_MM("v", 24, 1, 6) T_WF("v", 24, 7) T_MM("v", 40, 2, 5) T_MM("v", 56, 3, 4) T_MM("a", 8, 4, 3) T_MM("a", 24, 5, 2)
        "s_waitcnt lgkmcnt(0)\n\t" T_MM("v", 8, 6, 1) T_MM("v", 24, 7, 0)
        TS(84) "v_add_u32 v6, %[dk], %[xw]\n\ts_nop 6\n\t"
        "ds_write_b128 %[xw], v[104:107]\n\tds_write_b128 %[xw], v[108:111] offset:64\n\tds_write_b128 v6, v[112:115]\n\tds_write_b128 v6, v[116:119] offset:64\n\t"
        "v_lshrrev_b32 v4, 7, %[xa]\n\tv_mul_u32_u24 v4, 272, v4\n\tv_and_b32 v5, 0x70, %[xa]\n\tv_lshl_add_u32 v4, v5, 1, v4\n\tv_add_u32 v4, %[xq], v4\n\t"
        "s_waitcnt lgkmcnt(0)\n\ts_barrier\n\t"
        "ds_read_b128 v[72:75], v4\n\tds_read_b128 v[80:83], v4 offset:4352\n\tds_read_b128 v[76:79], v4 offset:16\n\tds_read_b128 v[84:87], v4 offset:4368\n\t"
        "s_waitcnt lgkmcnt(2)\n\t"
        "v_add_f32 v72, v72, v80\n\tv_add_f32 v73, v73, v81\n\tv_add_f32 v74, v74, v82\n\tv_add_f32 v75, v75, v83\n\ts_waitcnt lgkmcnt(0)\n\t"
        "v_add_f32 v76, v76, v84\n\tv_add_f32 v77, v77, v85\n\tv_add_f32 v78, v78, v86\n\tv_add_f32 v79, v79, v87\n\t"
        "v_cvt_pk_bf16_f32 v80, v72, v73\n\tv_cvt_pk_bf16_f32 v81, v74, v75\n\tv_cvt_pk_bf16_f32 v82, v76, v77\n\tv_cvt_pk_bf16_f32 v83, v78, v79\n\t"
        "v_lshl_add_u32 v4, %[xa], 4, %[go]\n\tv_and_b32 v5, 0x70, %[xa]\n\tv_and_b32 v4, 0xfffff800, v4\n\tv_add3_u32 v4, v4, v5, %[gc]\n\t"
        "global_store_dwordx4 v4, v[80:83], %[out] " OAUX "\n\ts_nop 1"
        : [t0] "=&s"(t0), [t1] "=&s"(t1)
        : [a0] "v"(A0), [oo] "v"(oo), [xa] "v"(xa), [xw] "v"(xw), [w] "s"(w), [go] "s"((64 * grp + 16 * qq + 8 * kh) * 2048), [gc] "s"(1024 + 128 * hg), [dk] "s"(128 - 256 * kh),
          [xq] "s"(sb + L_OA + 2 * qq * 4352 + 8 * kh * 272), [fo] "s"(hg * 128), [own] "s"(((2 * grp + kh) * 32 + 4 * hg + qq) * 1024), [tg] "s"(tag), [f2] "s"(f2),
          [ro] "s"(make_srd(ws + W_OX, 2097152)), [out] "s"(out)
        : "memory", "scc", "vcc", CLOB_V4_83, CLOB_A4_47 TSCLOB);
    TSTAMP_OUT();
    STAMP(12);
    STAMP_OUT();
  }
}

extern "C" void kernel_launch(void* const* d_in, const int* in_sizes, int n_in, void* d_out, int out_size, void* d_ws,
                              size_t ws_size, hipStream_t stream) {
  typedef const unsigned short* P;
  fused_kernel<<<256, 768, 0, stream>>>((P)d_in[0], (P)d_in[1], (P)d_in[2], (P)d_in[3], (P)d_in[4], (P)d_in[5], (P)d_in[6],
                                        (const unsigned*)d_in[7], (const unsigned*)d_in[8], (P)d_in[9], (P)d_in[10], (P)d_in[11],
                                        (P)d_in[12], (P)d_in[13], (unsigned short*)d_out, (unsigned char*)d_ws);
}
```
